```python
import torch

def _softmax(x, axis=-1):
    return torch.softmax(x, dim=axis)
def _gelu(x):
    return torch.nn.functional.gelu(x, approximate="tanh")
def _silu(x):
    return torch.nn.functional.silu(x)
def _sigmoid(x):
    return torch.sigmoid(x)
def _layernorm(x, axis=-1, eps=1e-5):
    m = x.mean(dim=axis, keepdim=True)
    v = x.var(dim=axis, keepdim=True, unbiased=False)
    return (x - m) / torch.sqrt(v + eps)
def _scatter_add(tgt, idx, src, axis=0):
    return tgt.clone().index_add_(axis, idx, src)
def _segment_sum(src, seg, n):
    out = torch.zeros((n,) + src.shape[1:], dtype=src.dtype)
    return out.index_add_(0, seg, src)
def _pad(x, pw):
    flat = tuple(v for pair in reversed(pw) for v in pair)
    return torch.nn.functional.pad(x, flat)

kernel_name = 'sequential_preLN__mha_block__dense_activation_relu__prompt'

def setup_inputs():

    _w = (lambda a: (lambda t: t.to(torch.bfloat16) if t.is_floating_point() else t)(torch.as_tensor(a)))
    def _A_setup():
        B, S, D, H = 1, 2048, 640, 32
        return {
            "x": _w((lambda *s: torch.randn(*s, dtype=torch.bfloat16) * 0.05)(B, S, D)),
            "wqkv": _w((lambda *s: torch.randn(*s, dtype=torch.bfloat16) * 0.05)(D, 3 * D)),
            "wo": _w((lambda *s: torch.randn(*s, dtype=torch.bfloat16) * 0.05)(D, D)),
        }
    _A_in = _A_setup()

    def _B_setup():
        B, S, D, Dff = 1, 2048, 640, 1728
        return {
            "x": _w((lambda *s: torch.randn(*s, dtype=torch.bfloat16) * 0.05)(B, S, D)),
            "w1": _w((lambda *s: torch.randn(*s, dtype=torch.bfloat16) * 0.05)(D, Dff)),
            "w2": _w((lambda *s: torch.randn(*s, dtype=torch.bfloat16) * 0.05)(Dff, D)),
        }
    _B_in = _B_setup()

    return {
        "A_x": _A_in["x"],
        "A_wqkv": _A_in["wqkv"],
        "A_wo": _A_in["wo"],
        "B_x": _B_in["x"],
        "B_w1": _B_in["w1"],
        "B_w2": _B_in["w2"],
    }

def reference(A_x, A_wqkv, A_wo, B_x, B_w1, B_w2):
    def _A_ref(x, wqkv, wo):
        B, S, D = x.shape
        H = 32
        Dh = D // H
        qkv = (lambda x, *d: x.permute(*d))((x @ wqkv).reshape(B, S, 3, H, Dh), 2, 0, 3, 1, 4)
        q, k, v = qkv[0], qkv[1], qkv[2]
        scores = torch.einsum("bhqd,bhkd->bhqk", q, k) * (Dh ** -0.5)
        p = _softmax(scores, axis=-1)
        o = torch.einsum("bhqk,bhkd->bhqd", p, v)
        return (lambda x, *d: x.permute(*d))(o, 0, 2, 1, 3).reshape(B, S, D) @ wo

    def _B_ref(x, w1, w2):
        h = torch.einsum("bsd,df->bsf", x, w1)
        zero = torch.zeros_like(h)
        h = torch.maximum(h, zero)
        return torch.einsum("bsf,fd->bsd", h, w2)

    x = A_x
    h = x + _A_ref(_layernorm(x, axis=-1), A_wqkv, A_wo)
    h2 = h + _B_ref(_layernorm(h, axis=-1), B_w1, B_w2)
    return h2
```

```cpp
#include <hip/hip_runtime.h>
#include <hip/hip_bf16.h>
#include <stdint.h>
typedef unsigned short u16;
__device__ __forceinline__ float bf2f(u16 v) { return __uint_as_float(((unsigned)v) << 16); }
__device__ __forceinline__ u16 f2bf(float f) { __hip_bfloat16 b = __float2bfloat16(f); return *(u16*)&b; }
constexpr int S = 2048, D = 640, H = 32, DH = 20, F = 1728;

__global__ void ln_kernel(const u16* x, u16* y) {
  int r = blockIdx.x; int t = threadIdx.x;
  float v[10]; float s = 0;
  for (int i = 0; i < 10; ++i) { v[i] = bf2f(x[r * D + t + 64 * i]); s += v[i]; }
  for (int o = 32; o; o >>= 1) s += __shfl_xor(s, o);
  float m = s / D; float q = 0;
  for (int i = 0; i < 10; ++i) { float d = v[i] - m; q += d * d; }
  for (int o = 32; o; o >>= 1) q += __shfl_xor(q, o);
  float rs = rsqrtf(q / D + 1e-5f);
  for (int i = 0; i < 10; ++i) y[r * D + t + 64 * i] = f2bf((v[i] - m) * rs);
}
template <bool RELU, bool RES>
__global__ void gemm_kernel(const u16* A, const u16* B, const u16* res, u16* C, int N, int K) {
  __shared__ float As[32][33], Bs[32][33];
  int tx = threadIdx.x & 31, ty = threadIdx.x >> 5;
  int r0 = blockIdx.y * 32, c0 = blockIdx.x * 32;
  float acc[4] = {0, 0, 0, 0};
  for (int k0 = 0; k0 < K; k0 += 32) {
    for (int i = 0; i < 4; ++i) { As[ty + 8 * i][tx] = bf2f(A[(r0 + ty + 8 * i) * K + k0 + tx]); Bs[ty + 8 * i][tx] = bf2f(B[(k0 + ty + 8 * i) * N + c0 + tx]); }
    __syncthreads();
    for (int k = 0; k < 32; ++k) { float b = Bs[k][tx]; for (int i = 0; i < 4; ++i) acc[i] += As[ty + 8 * i][k] * b; }
    __syncthreads();
  }
  for (int i = 0; i < 4; ++i) { float v = acc[i]; if (RELU) v = fmaxf(v, 0.f); int idx = (r0 + ty + 8 * i) * N + c0 + tx; if (RES) v += bf2f(res[idx]); C[idx] = f2bf(v); }
}
__global__ void attn_kernel(const u16* qkv, u16* o) {
  int h = blockIdx.y; int q = blockIdx.x * 256 + threadIdx.x;
  __shared__ float Ks[64][DH], Vs[64][DH];
  float qv[DH], acc[DH]; for (int d = 0; d < DH; ++d) { qv[d] = bf2f(qkv[q * 3 * D + h * DH + d]) * 0.22360679775f; acc[d] = 0; }
  float m = -1e30f, l = 0;
  for (int k0 = 0; k0 < S; k0 += 64) {
    for (int i = threadIdx.x; i < 64 * DH; i += 256) { int r = i / DH, d = i % DH; Ks[r][d] = bf2f(qkv[(k0 + r) * 3 * D + D + h * DH + d]); Vs[r][d] = bf2f(qkv[(k0 + r) * 3 * D + 2 * D + h * DH + d]); }
    __syncthreads();
    for (int r = 0; r < 64; ++r) { float s = 0; for (int d = 0; d < DH; ++d) s += qv[d] * Ks[r][d];
      float mn = fmaxf(m, s); float a = __expf(m - mn), p = __expf(s - mn); l = l * a + p; for (int d = 0; d < DH; ++d) acc[d] = acc[d] * a + p * Vs[r][d]; m = mn; }
    __syncthreads();
  }
  for (int d = 0; d < DH; ++d) o[q * D + h * DH + d] = f2bf(acc[d] / l);
}
extern "C" void kernel_launch(void* const* d_in, const int* in_sizes, int n_in, void* d_out, int out_size, void* d_ws, size_t ws_size, hipStream_t stream) {
  const u16* x = (const u16*)d_in[0]; const u16* wqkv = (const u16*)d_in[1]; const u16* wo = (const u16*)d_in[2];
  const u16* w1 = (const u16*)d_in[4]; const u16* w2 = (const u16*)d_in[5];
  u16* out = (u16*)d_out; u16* ws = (u16*)d_ws;
  u16* xn = ws; u16* qkv = xn + S * D; u16* o = qkv + S * 3 * D; u16* h = o + S * D; u16* hn = h + S * D; u16* h1 = hn + S * D;
  ln_kernel<<<S, 64, 0, stream>>>(x, xn);
  gemm_kernel<false, false><<<dim3(3 * D / 32, S / 32), 256, 0, stream>>>(xn, wqkv, nullptr, qkv, 3 * D, D);
  attn_kernel<<<dim3(S / 256, H), 256, 0, stream>>>(qkv, o);
  gemm_kernel<false, true><<<dim3(D / 32, S / 32), 256, 0, stream>>>(o, wo, x, h, D, D);
  ln_kernel<<<S, 64, 0, stream>>>(h, hn);
  gemm_kernel<true, false><<<dim3(F / 32, S / 32), 256, 0, stream>>>(hn, w1, nullptr, h1, F, D);
  gemm_kernel<false, true><<<dim3(D / 32, S / 32), 256, 0, stream>>>(h1, w2, h, out, D, F);
}
```

```cpp
#include <hip/hip_runtime.h>
#include <hip/hip_bf16.h>

__global__ void __launch_bounds__(512, 2) __attribute__((aligned(4096))) fused_block_kernel(const void* x, const void* wqkv, const void* wo, const void* w1,
                                                             const void* w2, void* out, void* ws) {
  __shared__ __attribute__((aligned(16))) char lds[163840];
  asm volatile(
".Lk_top:\n"
"s_load_dwordx2 s[8:9], s[0:1], 0x0\n"
"s_load_dwordx2 s[12:13], s[0:1], 0x8\n"
"s_load_dwordx2 s[4:5], s[0:1], 0x30\n"
"s_load_dwordx2 s[16:17], s[0:1], 0x10\n"
"s_load_dwordx2 s[20:21], s[0:1], 0x18\n"
"s_load_dwordx2 s[24:25], s[0:1], 0x20\n"
"s_load_dwordx2 s[28:29], s[0:1], 0x28\n"
"v_lshrrev_b32 v2, 6, v0\n"
"v_and_b32 v0, 63, v0\n"
"v_readfirstlane_b32 s34, v2\n"
"s_and_b32 s35, s2, 7\n"
"s_lshr_b32 s36, s2, 3\n"
"s_and_b32 s3, s34, 3\n"
"s_lshr_b32 s56, s35, 1\n"
"s_lshl_b32 s57, s56, 2\n"
"s_and_b32 s58, s36, 3\n"
"s_add_u32 s40, s57, s58\n"
"s_and_b32 s56, s35, 1\n"
"s_lshl_b32 s57, s56, 3\n"
"s_lshr_b32 s58, s36, 2\n"
"s_add_u32 s41, s57, s58\n"
"s_lshl_b32 s56, s35, 1\n"
"s_and_b32 s57, s36, 1\n"
"s_add_u32 s38, s56, s57\n"
"s_cmp_ge_u32 s36, 28\n"
"s_cselect_b32 s40, s38, s40\n"
"s_cselect_b32 s41, 7, s41\n"
"s_mov_b32 s10, 0x280000\n"
"s_mov_b32 s11, 0x20000\n"
"s_mov_b32 s14, 0x258000\n"
"s_mov_b32 s15, 0x20000\n"
"s_mov_b32 s6, 0x1c11000\n"
"s_mov_b32 s7, 0x20000\n"
"; ==== phase 1\n"
"s_cmp_ge_u32 s36, 30\n"
"s_cbranch_scc1 .Lk_idle_5\n"
"s_cmp_lt_u32 s34, 4\n"
"s_cbranch_scc1 .Lk_loader_1\n"
"v_lshrrev_b32 v8, 4, v0\n"
"s_lshl_b32 s56, s40, 7\n"
"s_lshl_b32 s57, s3, 5\n"
"s_add_u32 s58, s56, s57\n"
"v_lshrrev_b32 v2, 3, v0\n"
"v_add_u32 v3, s58, v2\n"
"v_mul_u32_u24 v4, 0x500, v3\n"
"v_and_b32 v5, 7, v0\n"
"v_xor_b32 v2, v5, v8\n"
"v_lshlrev_b32 v3, 4, v2\n"
"v_add_u32 v60, v4, v3\n"
"s_lshl_b32 s56, s3, 4\n"
"v_add_u32 v2, s56, v8\n"
"v_mul_u32_u24 v3, 0xf00, v2\n"
"s_lshl_b32 s57, s41, 8\n"
"v_add_u32 v4, s57, v3\n"
"v_and_b32 v5, 15, v0\n"
"v_lshlrev_b32 v2, 2, v8\n"
"v_xor_b32 v3, v5, v2\n"
"v_lshlrev_b32 v5, 4, v3\n"
"v_add_u32 v62, v4, v5\n"
"v_xor_b32 v61, 64, v60\n"
"v_add_u32 v61, 0x2800, v61\n"
"s_lshl_b32 s50, s3, 12\n"
"v_xor_b32 v63, 32, v62\n"
"s_add_u32 m0, s50, 0\n"
"s_mov_b32 s51, 0\n"
"s_waitcnt lgkmcnt(0)\n"
"buffer_load_dwordx4 v60, s[8:11], s51 offen lds\n"
"s_add_u32 m0, s50, 16384\n"
"s_mov_b32 s51, 0\n"
"buffer_load_dwordx4 v62, s[12:15], s51 offen lds\n"
"s_add_u32 m0, s50, 1024\n"
"s_mov_b32 s51, 0\n"
"buffer_load_dwordx4 v61, s[8:11], s51 offen lds\n"
"s_add_u32 m0, s50, 17408\n"
"s_mov_b32 s51, 15360\n"
"buffer_load_dwordx4 v62, s[12:15], s51 offen lds\n"
"s_add_u32 m0, s50, 2048\n"
"s_mov_b32 s51, 20480\n"
"buffer_load_dwordx4 v60, s[8:11], s51 offen lds\n"
"s_add_u32 m0, s50, 18432\n"
"s_mov_b32 s51, 30720\n"
"buffer_load_dwordx4 v63, s[12:15], s51 offen lds\n"
"s_add_u32 m0, s50, 3072\n"
"s_mov_b32 s51, 20480\n"
"buffer_load_dwordx4 v61, s[8:11], s51 offen lds\n"
"s_add_u32 m0, s50, 19456\n"
"s_mov_b32 s51, 46080\n"
"buffer_load_dwordx4 v63, s[12:15], s51 offen lds\n"
"buffer_load_dwordx2 v[6:7], off, s[4:7], 0 sc1\n"
"s_lshr_b32 s56, s35, 1\n"
"s_lshl_b32 s57, s56, 9\n"
"s_and_b32 s58, s35, 1\n"
"s_lshl_b32 s59, s58, 5\n"
"s_add_u32 s60, s59, s36\n"
"s_lshl_b32 s61, s60, 3\n"
"s_add_u32 s56, s57, s61\n"
"s_lshl_b32 s58, s3, 1\n"
"s_add_u32 s42, s56, s58\n"
"s_mul_i32 s43, s42, 0x500\n"
"v_lshlrev_b32 v8, 4, v0\n"
"v_lshlrev_b32 v9, 2, v0\n"
"buffer_load_dwordx4 v[10:13], v8, s[8:11], s43 offen offset:0\n"
"buffer_load_dword v14, v9, s[8:11], s43 offen offset:1024\n"
"buffer_load_dwordx4 v[16:19], v8, s[8:11], s43 offen offset:1280\n"
"buffer_load_dword v20, v9, s[8:11], s43 offen offset:2304\n"
"s_mov_b32 s18, 0xc8000\n"
"s_mov_b32 s19, 0x20000\n"
"s_mov_b32 s22, 0x21c000\n"
"s_mov_b32 s23, 0x20000\n"
"s_mov_b32 s26, 0x21c000\n"
"s_mov_b32 s27, 0x20000\n"
"s_mov_b32 s30, 0x280000\n"
"s_mov_b32 s31, 0x20000\n"
"s_mov_b32 s37, 1048576\n"
"s_add_u32 m0, s50, 32768\n"
"s_mov_b32 s51, 128\n"
"buffer_load_dwordx4 v60, s[8:11], s51 offen lds\n"
"s_add_u32 m0, s50, 49152\n"
"s_mov_b32 s51, 245760\n"
"buffer_load_dwordx4 v62, s[12:15], s51 offen lds\n"
"s_add_u32 m0, s50, 33792\n"
"s_mov_b32 s51, 128\n"
"buffer_load_dwordx4 v61, s[8:11], s51 offen lds\n"
"s_add_u32 m0, s50, 50176\n"
"s_mov_b32 s51, 261120\n"
"buffer_load_dwordx4 v62, s[12:15], s51 offen lds\n"
"s_add_u32 m0, s50, 34816\n"
"s_mov_b32 s51, 20608\n"
"buffer_load_dwordx4 v60, s[8:11], s51 offen lds\n"
"s_add_u32 m0, s50, 51200\n"
"s_mov_b32 s51, 276480\n"
"buffer_load_dwordx4 v63, s[12:15], s51 offen lds\n"
"s_add_u32 m0, s50, 35840\n"
"s_mov_b32 s51, 20608\n"
"buffer_load_dwordx4 v61, s[8:11], s51 offen lds\n"
"s_add_u32 m0, s50, 52224\n"
"s_mov_b32 s51, 291840\n"
"buffer_load_dwordx4 v63, s[12:15], s51 offen lds\n"
"s_waitcnt vmcnt(13)\n"
"s_barrier\n"
"s_sleep 2\n"
"s_add_u32 m0, s50, 65536\n"
"s_mov_b32 s51, 256\n"
"buffer_load_dwordx4 v60, s[8:11], s51 offen lds\n"
"s_add_u32 m0, s50, 81920\n"
"s_mov_b32 s51, 491520\n"
"buffer_load_dwordx4 v62, s[12:15], s51 offen lds\n"
"s_add_u32 m0, s50, 66560\n"
"s_mov_b32 s51, 256\n"
"buffer_load_dwordx4 v61, s[8:11], s51 offen lds\n"
"s_add_u32 m0, s50, 82944\n"
"s_mov_b32 s51, 506880\n"
"buffer_load_dwordx4 v62, s[12:15], s51 offen lds\n"
"s_add_u32 m0, s50, 67584\n"
"s_mov_b32 s51, 20736\n"
"buffer_load_dwordx4 v60, s[8:11], s51 offen lds\n"
"s_add_u32 m0, s50, 83968\n"
"s_mov_b32 s51, 522240\n"
"buffer_load_dwordx4 v63, s[12:15], s51 offen lds\n"
"s_add_u32 m0, s50, 68608\n"
"s_mov_b32 s51, 20736\n"
"buffer_load_dwordx4 v61, s[8:11], s51 offen lds\n"
"s_add_u32 m0, s50, 84992\n"
"s_mov_b32 s51, 537600\n"
"buffer_load_dwordx4 v63, s[12:15], s51 offen lds\n"
"s_add_u32 m0, s50, 98304\n"
"s_mov_b32 s51, 384\n"
"buffer_load_dwordx4 v60, s[8:11], s51 offen lds\n"
"s_add_u32 m0, s50, 114688\n"
"s_mov_b32 s51, 737280\n"
"buffer_load_dwordx4 v62, s[12:15], s51 offen lds\n"
"s_add_u32 m0, s50, 99328\n"
"s_mov_b32 s51, 384\n"
"buffer_load_dwordx4 v61, s[8:11], s51 offen lds\n"
"s_add_u32 m0, s50, 115712\n"
"s_mov_b32 s51, 752640\n"
"buffer_load_dwordx4 v62, s[12:15], s51 offen lds\n"
"s_waitcnt vmcnt(12)\n"
"s_barrier\n"
"s_sleep 2\n"
"s_add_u32 m0, s50, 100352\n"
"s_mov_b32 s51, 20864\n"
"buffer_load_dwordx4 v60, s[8:11], s51 offen lds\n"
"v_readfirstlane_b32 s0, v6\n"
"v_readfirstlane_b32 s1, v7\n"
"s_mul_hi_u32 s33, s0, 0x4c957f2d\n"
"s_mul_i32 s32, s0, 0x4c957f2d\n"
"s_mul_i32 s0, s0, 0x5851f42d\n"
"s_add_u32 m0, s50, 116736\n"
"s_mov_b32 s51, 768000\n"
"buffer_load_dwordx4 v63, s[12:15], s51 offen lds\n"
"s_mul_i32 s1, s1, 0x4c957f2d\n"
"s_add_u32 s33, s33, s0\n"
"s_add_u32 s33, s33, s1\n"
"s_add_u32 s32, s32, 0xf767814f\n"
"s_add_u32 s33, s33, 0x14057b7e\n"
"s_add_u32 m0, s50, 101376\n"
"s_mov_b32 s51, 20864\n"
"buffer_load_dwordx4 v61, s[8:11], s51 offen lds\n"
"v_lshlrev_b32 v32, 16, v10\n"
"v_and_b32 v33, 0xffff0000, v10\n"
"v_lshlrev_b32 v34, 16, v11\n"
"v_and_b32 v35, 0xffff0000, v11\n"
"v_lshlrev_b32 v36, 16, v12\n"
"s_add_u32 m0, s50, 117760\n"
"s_mov_b32 s51, 783360\n"
"buffer_load_dwordx4 v63, s[12:15], s51 offen lds\n"
"v_and_b32 v37, 0xffff0000, v12\n"
"v_lshlrev_b32 v38, 16, v13\n"
"v_and_b32 v39, 0xffff0000, v13\n"
"v_lshlrev_b32 v40, 16, v14\n"
"v_and_b32 v41, 0xffff0000, v14\n"
"s_add_u32 m0, s50, 0\n"
"s_mov_b32 s51, 512\n"
"buffer_load_dwordx4 v60, s[8:11], s51 offen lds\n"
"v_lshlrev_b32 v42, 16, v16\n"
"v_and_b32 v43, 0xffff0000, v16\n"
"v_lshlrev_b32 v44, 16, v17\n"
"v_and_b32 v45, 0xffff0000, v17\n"
"v_lshlrev_b32 v46, 16, v18\n"
"s_add_u32 m0, s50, 16384\n"
"s_mov_b32 s51, 983040\n"
"buffer_load_dwordx4 v62, s[12:15], s51 offen lds\n"
"v_and_b32 v47, 0xffff0000, v18\n"
"v_lshlrev_b32 v48, 16, v19\n"
"v_and_b32 v49, 0xffff0000, v19\n"
"v_lshlrev_b32 v50, 16, v20\n"
"v_and_b32 v51, 0xffff0000, v20\n"
"s_add_u32 m0, s50, 1024\n"
"s_mov_b32 s51, 512\n"
"buffer_load_dwordx4 v61, s[8:11], s51 offen lds\n"
"v_add_f32 v24, v32, v33\n"
"v_add_f32 v28, v42, v43\n"
"v_add_f32 v24, v24, v34\n"
"v_add_f32 v28, v28, v44\n"
"v_add_f32 v24, v24, v35\n"
"s_add_u32 m0, s50, 17408\n"
"s_mov_b32 s51, 998400\n"
"buffer_load_dwordx4 v62, s[12:15], s51 offen lds\n"
"v_add_f32 v28, v28, v45\n"
"v_add_f32 v24, v24, v36\n"
"v_add_f32 v28, v28, v46\n"
"s_waitcnt vmcnt(12)\n"
"s_barrier\n"
"s_sleep 2\n"
"s_add_u32 m0, s50, 2048\n"
"s_mov_b32 s51, 20992\n"
"buffer_load_dwordx4 v60, s[8:11], s51 offen lds\n"
"v_add_f32 v24, v24, v37\n"
"v_add_f32 v28, v28, v47\n"
"v_add_f32 v24, v24, v38\n"
"v_add_f32 v28, v28, v48\n"
"v_add_f32 v24, v24, v39\n"
"s_add_u32 m0, s50, 18432\n"
"s_mov_b32 s51, 1013760\n"
"buffer_load_dwordx4 v63, s[12:15], s51 offen lds\n"
"v_add_f32 v28, v28, v49\n"
"v_add_f32 v24, v24, v40\n"
"v_add_f32 v28, v28, v50\n"
"v_add_f32 v24, v24, v41\n"
"v_add_f32 v28, v28, v51\n"
"s_add_u32 m0, s50, 3072\n"
"s_mov_b32 s51, 20992\n"
"buffer_load_dwordx4 v61, s[8:11], s51 offen lds\n"
"v_add_f32_dpp v24, v24, v24 quad_perm:[1,0,3,2] row_mask:0xf bank_mask:0xf\n"
"v_add_f32_dpp v28, v28, v28 quad_perm:[1,0,3,2] row_mask:0xf bank_mask:0xf\n"
"s_nop 0\n"
"v_add_f32_dpp v24, v24, v24 quad_perm:[2,3,0,1] row_mask:0xf bank_mask:0xf\n"
"v_add_f32_dpp v28, v28, v28 quad_perm:[2,3,0,1] row_mask:0xf bank_mask:0xf\n"
"s_nop 0\n"
"v_add_f32_dpp v24, v24, v24 row_half_mirror row_mask:0xf bank_mask:0xf\n"
"s_add_u32 m0, s50, 19456\n"
"s_mov_b32 s51, 1029120\n"
"buffer_load_dwordx4 v63, s[12:15], s51 offen lds\n"
"v_add_f32_dpp v28, v28, v28 row_half_mirror row_mask:0xf bank_mask:0xf\n"
"v_add_f32_dpp v24, v24, v24 row_mirror row_mask:0xf bank_mask:0xf\n"
"s_nop 0\n"
"v_add_f32_dpp v28, v28, v28 row_mirror row_mask:0xf bank_mask:0xf\n"
"v_mov_b32 v22, v24\n"
"v_mov_b32 v23, v28\n"
"s_add_u32 m0, s50, 32768\n"
"s_mov_b32 s51, 640\n"
"buffer_load_dwordx4 v60, s[8:11], s51 offen lds\n"
"v_permlane16_swap_b32 v24, v22\n"
"v_permlane16_swap_b32 v28, v23\n"
"v_add_f32 v24, v24, v22\n"
"v_add_f32 v28, v28, v23\n"
"v_mov_b32 v22, v24\n"
"s_add_u32 m0, s50, 49152\n"
"s_mov_b32 s51, 1228800\n"
"buffer_load_dwordx4 v62, s[12:15], s51 offen lds\n"
"v_mov_b32 v23, v28\n"
"v_permlane32_swap_b32 v24, v22\n"
"s_nop 0\n"
"v_permlane32_swap_b32 v28, v23\n"
"v_add_f32 v24, v24, v22\n"
"v_add_f32 v28, v28, v23\n"
"s_add_u32 m0, s50, 33792\n"
"s_mov_b32 s51, 640\n"
"buffer_load_dwordx4 v61, s[8:11], s51 offen lds\n"
"v_mul_f32 v24, 0x3acccccd, v24\n"
"v_mul_f32 v28, 0x3acccccd, v28\n"
"v_sub_f32 v32, v32, v24\n"
"v_sub_f32 v42, v42, v28\n"
"v_sub_f32 v33, v33, v24\n"
"s_add_u32 m0, s50, 50176\n"
"s_mov_b32 s51, 1244160\n"
"buffer_load_dwordx4 v62, s[12:15], s51 offen lds\n"
"v_sub_f32 v43, v43, v28\n"
"v_sub_f32 v34, v34, v24\n"
"v_sub_f32 v44, v44, v28\n"
"v_sub_f32 v35, v35, v24\n"
"s_waitcnt vmcnt(12)\n"
"s_barrier\n"
"s_sleep 2\n"
"s_add_u32 m0, s50, 34816\n"
"s_mov_b32 s51, 21120\n"
"buffer_load_dwordx4 v60, s[8:11], s51 offen lds\n"
"v_sub_f32 v45, v45, v28\n"
"v_sub_f32 v36, v36, v24\n"
"v_sub_f32 v46, v46, v28\n"
"v_sub_f32 v37, v37, v24\n"
"v_sub_f32 v47, v47, v28\n"
"s_add_u32 m0, s50, 51200\n"
"s_mov_b32 s51, 1259520\n"
"buffer_load_dwordx4 v63, s[12:15], s51 offen lds\n"
"v_sub_f32 v38, v38, v24\n"
"v_sub_f32 v48, v48, v28\n"
"v_sub_f32 v39, v39, v24\n"
"v_sub_f32 v49, v49, v28\n"
"v_sub_f32 v40, v40, v24\n"
"s_add_u32 m0, s50, 35840\n"
"s_mov_b32 s51, 21120\n"
"buffer_load_dwordx4 v61, s[8:11], s51 offen lds\n"
"v_sub_f32 v50, v50, v28\n"
"v_sub_f32 v41, v41, v24\n"
"v_sub_f32 v51, v51, v28\n"
"v_mul_f32 v26, v32, v32\n"
"v_mul_f32 v30, v42, v42\n"
"s_add_u32 m0, s50, 52224\n"
"s_mov_b32 s51, 1274880\n"
"buffer_load_dwordx4 v63, s[12:15], s51 offen lds\n"
"v_fmac_f32 v26, v33, v33\n"
"v_fmac_f32 v30, v43, v43\n"
"v_fmac_f32 v26, v34, v34\n"
"v_fmac_f32 v30, v44, v44\n"
"v_fmac_f32 v26, v35, v35\n"
"s_add_u32 m0, s50, 65536\n"
"s_mov_b32 s51, 768\n"
"buffer_load_dwordx4 v60, s[8:11], s51 offen lds\n"
"v_fmac_f32 v30, v45, v45\n"
"v_fmac_f32 v26, v36, v36\n"
"v_fmac_f32 v30, v46, v46\n"
"v_fmac_f32 v26, v37, v37\n"
"v_fmac_f32 v30, v47, v47\n"
"s_add_u32 m0, s50, 81920\n"
"s_mov_b32 s51, 1474560\n"
"buffer_load_dwordx4 v62, s[12:15], s51 offen lds\n"
"v_fmac_f32 v26, v38, v38\n"
"v_fmac_f32 v30, v48, v48\n"
"v_fmac_f32 v26, v39, v39\n"
"v_fmac_f32 v30, v49, v49\n"
"v_fmac_f32 v26, v40, v40\n"
"s_add_u32 m0, s50, 66560\n"
"s_mov_b32 s51, 768\n"
"buffer_load_dwordx4 v61, s[8:11], s51 offen lds\n"
"v_fmac_f32 v30, v50, v50\n"
"v_fmac_f32 v26, v41, v41\n"
"v_fmac_f32 v30, v51, v51\n"
"s_nop 0\n"
"v_add_f32_dpp v26, v26, v26 quad_perm:[1,0,3,2] row_mask:0xf bank_mask:0xf\n"
"v_add_f32_dpp v30, v30, v30 quad_perm:[1,0,3,2] row_mask:0xf bank_mask:0xf\n"
"s_add_u32 m0, s50, 82944\n"
"s_mov_b32 s51, 1489920\n"
"buffer_load_dwordx4 v62, s[12:15], s51 offen lds\n"
"v_add_f32_dpp v26, v26, v26 quad_perm:[2,3,0,1] row_mask:0xf bank_mask:0xf\n"
"v_add_f32_dpp v30, v30, v30 quad_perm:[2,3,0,1] row_mask:0xf bank_mask:0xf\n"
"s_nop 0\n"
"v_add_f32_dpp v26, v26, v26 row_half_mirror row_mask:0xf bank_mask:0xf\n"
"s_waitcnt vmcnt(12)\n"
"s_barrier\n"
"s_sleep 2\n"
"s_add_u32 m0, s50, 67584\n"
"s_mov_b32 s51, 21248\n"
"buffer_load_dwordx4 v60, s[8:11], s51 offen lds\n"
"v_add_f32_dpp v30, v30, v30 row_half_mirror row_mask:0xf bank_mask:0xf\n"
"v_add_f32_dpp v26, v26, v26 row_mirror row_mask:0xf bank_mask:0xf\n"
"s_nop 0\n"
"v_add_f32_dpp v30, v30, v30 row_mirror row_mask:0xf bank_mask:0xf\n"
"v_mov_b32 v22, v26\n"
"v_mov_b32 v23, v30\n"
"s_add_u32 m0, s50, 83968\n"
"s_mov_b32 s51, 1505280\n"
"buffer_load_dwordx4 v63, s[12:15], s51 offen lds\n"
"v_permlane16_swap_b32 v26, v22\n"
"v_permlane16_swap_b32 v30, v23\n"
"v_add_f32 v26, v26, v22\n"
"v_add_f32 v30, v30, v23\n"
"v_mov_b32 v22, v26\n"
"s_add_u32 m0, s50, 68608\n"
"s_mov_b32 s51, 21248\n"
"buffer_load_dwordx4 v61, s[8:11], s51 offen lds\n"
"v_mov_b32 v23, v30\n"
"v_permlane32_swap_b32 v26, v22\n"
"s_nop 0\n"
"v_permlane32_swap_b32 v30, v23\n"
"v_add_f32 v26, v26, v22\n"
"v_add_f32 v30, v30, v23\n"
"s_add_u32 m0, s50, 84992\n"
"s_mov_b32 s51, 1520640\n"
"buffer_load_dwordx4 v63, s[12:15], s51 offen lds\n"
"v_cmp_eq_u32 vcc, 0, v0\n"
"s_nop 1\n"
"v_cndmask_b32 v10, v28, v24, vcc\n"
"v_cndmask_b32 v12, v30, v26, vcc\n"
"v_mul_f32 v12, 0x3acccccd, v12\n"
"v_cvt_pk_bf16_f32 v12, v12, v12\n"
"s_add_u32 m0, s50, 98304\n"
"s_mov_b32 s51, 896\n"
"buffer_load_dwordx4 v60, s[8:11], s51 offen lds\n"
"v_and_b32 v12, 0xffff0000, v12\n"
"v_add_f32 v12, 0x3727c5ac, v12\n"
"v_cvt_pk_bf16_f32 v12, v12, v12\n"
"v_and_b32 v12, 0xffff0000, v12\n"
"v_sqrt_f32 v12, v12\n"
"s_add_u32 m0, s50, 114688\n"
"s_mov_b32 s51, 1720320\n"
"buffer_load_dwordx4 v62, s[12:15], s51 offen lds\n"
"v_cvt_pk_bf16_f32 v10, v10, v10\n"
"v_cvt_pk_bf16_f32 v12, v12, v12\n"
"s_mov_b32 s38, 0xffff\n"
"v_mov_b32 v11, s32\n"
"v_bfi_b32 v10, s38, v10, v11\n"
"s_add_u32 m0, s50, 99328\n"
"s_mov_b32 s51, 896\n"
"buffer_load_dwordx4 v61, s[8:11], s51 offen lds\n"
"v_bfi_b32 v12, s38, v12, v11\n"
"v_mov_b32 v11, s33\n"
"v_mov_b32 v13, s33\n"
"s_lshl_b32 s56, s42, 4\n"
"s_add_u32 s43, 0x301000, s56\n"
"s_add_u32 m0, s50, 115712\n"
"s_mov_b32 s51, 1735680\n"
"buffer_load_dwordx4 v62, s[12:15], s51 offen lds\n"
"v_cmp_gt_u32 vcc, 2, v0\n"
"v_mov_b32 v9, 0x7ff00000\n"
"s_nop 0\n"
"v_cndmask_b32 v9, v9, v8, vcc\n"
"buffer_store_dwordx4 v[10:13], v9, s[4:7], s43 offen sc1\n"
"s_waitcnt vmcnt(13)\n"
"s_barrier\n"
"s_sleep 2\n"
"s_add_u32 m0, s50, 100352\n"
"s_mov_b32 s51, 21376\n"
"buffer_load_dwordx4 v60, s[8:11], s51 offen lds\n"
"s_add_u32 m0, s50, 116736\n"
"s_mov_b32 s51, 1751040\n"
"buffer_load_dwordx4 v63, s[12:15], s51 offen lds\n"
"s_add_u32 m0, s50, 101376\n"
"s_mov_b32 s51, 21376\n"
"buffer_load_dwordx4 v61, s[8:11], s51 offen lds\n"
"s_add_u32 m0, s50, 117760\n"
"s_mov_b32 s51, 1766400\n"
"buffer_load_dwordx4 v63, s[12:15], s51 offen lds\n"
"s_add_u32 m0, s50, 0\n"
"s_mov_b32 s51, 1024\n"
"buffer_load_dwordx4 v60, s[8:11], s51 offen lds\n"
"s_add_u32 m0, s50, 16384\n"
"s_mov_b32 s51, 1966080\n"
"buffer_load_dwordx4 v62, s[12:15], s51 offen lds\n"
"s_add_u32 m0, s50, 1024\n"
"s_mov_b32 s51, 1024\n"
"buffer_load_dwordx4 v61, s[8:11], s51 offen lds\n"
"s_add_u32 m0, s50, 17408\n"
"s_mov_b32 s51, 1981440\n"
"buffer_load_dwordx4 v62, s[12:15], s51 offen lds\n"
"s_waitcnt vmcnt(13)\n"
"s_barrier\n"
"s_sleep 2\n"
"s_add_u32 m0, s50, 2048\n"
"s_mov_b32 s51, 21504\n"
"buffer_load_dwordx4 v60, s[8:11], s51 offen lds\n"
"s_and_b32 s56, s3, 1\n"
"s_lshl_b32 s57, s56, 6\n"
"s_add_u32 m0, s50, 18432\n"
"s_mov_b32 s51, 1996800\n"
"buffer_load_dwordx4 v63, s[12:15], s51 offen lds\n"
"v_add_u32 v2, s57, v0\n"
"v_lshlrev_b32 v9, 4, v2\n"
"s_add_u32 m0, s50, 3072\n"
"s_mov_b32 s51, 21504\n"
"buffer_load_dwordx4 v61, s[8:11], s51 offen lds\n"
"s_lshl_b32 s56, s40, 11\n"
"s_add_u32 s57, 0x301000, s56\n"
"s_add_u32 m0, s50, 19456\n"
"s_mov_b32 s51, 2012160\n"
"buffer_load_dwordx4 v63, s[12:15], s51 offen lds\n"
"s_lshr_b32 s58, s3, 1\n"
"s_lshl_b32 s59, s41, 11\n"
"s_add_u32 m0, s50, 32768\n"
"s_mov_b32 s51, 1152\n"
"buffer_load_dwordx4 v60, s[8:11], s51 offen lds\n"
"s_add_u32 s60, 0x8000, s59\n"
"s_lshl_b32 s61, s40, 11\n"
"s_add_u32 m0, s50, 49152\n"
"s_mov_b32 s51, 2211840\n"
"buffer_load_dwordx4 v62, s[12:15], s51 offen lds\n"
"s_sub_u32 s56, s60, s61\n"
"s_mul_i32 s59, s58, s56\n"
"s_add_u32 m0, s50, 33792\n"
"s_mov_b32 s51, 1152\n"
"buffer_load_dwordx4 v61, s[8:11], s51 offen lds\n"
"s_add_u32 s42, s57, s59\n"
"s_add_u32 m0, s50, 50176\n"
"s_mov_b32 s51, 2227200\n"
"buffer_load_dwordx4 v62, s[12:15], s51 offen lds\n"
"s_waitcnt vmcnt(12)\n"
"s_barrier\n"
"buffer_load_dwordx4 v[56:59], v9, s[4:7], s42 offen sc1\n"
"s_sleep 2\n"
"s_add_u32 m0, s50, 34816\n"
"s_mov_b32 s51, 21632\n"
"buffer_load_dwordx4 v60, s[8:11], s51 offen lds\n"
"s_add_u32 m0, s50, 51200\n"
"s_mov_b32 s51, 2242560\n"
"buffer_load_dwordx4 v63, s[12:15], s51 offen lds\n"
"s_add_u32 m0, s50, 35840\n"
"s_mov_b32 s51, 21632\n"
"buffer_load_dwordx4 v61, s[8:11], s51 offen lds\n"
"s_add_u32 m0, s50, 52224\n"
"s_mov_b32 s51, 2257920\n"
"buffer_load_dwordx4 v63, s[12:15], s51 offen lds\n"
"s_waitcnt vmcnt(9)\n"
"s_barrier\n"
"s_sleep 2\n"
"s_waitcnt vmcnt(4)\n"
"v_xor_b32 v2, s32, v56\n"
"v_xor_b32 v3, s32, v58\n"
"v_or_b32 v2, v2, v3\n"
"v_lshrrev_b32 v2, 16, v2\n"
"v_xor_b32 v3, s33, v57\n"
"v_xor_b32 v4, s33, v59\n"
"v_or3_b32 v2, v2, v3, v4\n"
"v_cmp_ne_u32 vcc, 0, v2\n"
"s_nop 0\n"
"s_cmp_eq_u64 vcc, 0\n"
"s_cbranch_scc1 .Lk_std_8\n"
".Lk_stp_7:\n"
"buffer_load_dwordx4 v[56:59], v9, s[4:7], s42 offen sc1\n"
"s_waitcnt vmcnt(0)\n"
"v_xor_b32 v2, s32, v56\n"
"v_xor_b32 v3, s32, v58\n"
"v_or_b32 v2, v2, v3\n"
"v_lshrrev_b32 v2, 16, v2\n"
"v_xor_b32 v3, s33, v57\n"
"v_xor_b32 v4, s33, v59\n"
"v_or3_b32 v2, v2, v3, v4\n"
"v_cmp_ne_u32 vcc, 0, v2\n"
"s_nop 0\n"
"s_cmp_eq_u64 vcc, 0\n"
"s_cbranch_scc1 .Lk_std_8\n"
"s_sub_u32 s37, s37, 1\n"
"s_cmp_gt_i32 s37, 0\n"
"s_cbranch_scc1 .Lk_stp_7\n"
".Lk_std_8:\n"
"v_lshrrev_b32 v8, 1, v9\n"
"v_add_u32 v8, 0x20000, v8\n"
"v_lshlrev_b32 v3, 16, v58\n"
"s_mov_b32 s38, 0xffff\n"
"s_cmp_ge_u32 s3, 2\n"
"s_cbranch_scc1 .Lk_tcs_9\n"
"v_rcp_f32 v3, v3\n"
"v_xor_b32 v5, 0x8000, v56\n"
"v_and_b32 v5, s38, v5\n"
"v_lshl_or_b32 v4, v5, 16, v5\n"
"s_cmp_lt_u32 s41, 5\n"
"s_cselect_b32 s38, 0x3ea52b5c, 1.0\n"
"v_mul_f32 v3, s38, v3\n"
"v_lshrrev_b32 v9, 2, v9\n"
"v_add_u32 v9, 0x20000, v9\n"
"ds_write_b64 v8, v[4:5] offset:1024\n"
"ds_write_b32 v9, v3 offset:2048\n"
"s_branch .Lk_tdn_10\n"
".Lk_tcs_9:\n"
"v_bfi_b32 v2, s38, v56, v3\n"
"v_sub_f32 v2, v2, v3\n"
"v_and_b32 v4, 0xffff0000, v2\n"
"v_sub_f32 v5, v2, v4\n"
"v_lshrrev_b32 v5, 16, v5\n"
"v_bfi_b32 v4, s38, v58, v4\n"
"ds_write_b64 v8, v[4:5] offset:0\n"
".Lk_tdn_10:\n"
"s_waitcnt lgkmcnt(0)\n"
"s_waitcnt vmcnt(0)\n"
"s_barrier\n"
"s_sleep 2\n"
"s_branch .Lk_rpst_6\n"
".Lk_tramp:\n"
"s_setpc_b64 s[38:39]\n"
".Lk_loader_1:\n"
"s_setprio 1\n"
"s_mov_b32 s18, 0xc8000\n"
"s_mov_b32 s19, 0x20000\n"
"s_mov_b32 s22, 0x21c000\n"
"s_mov_b32 s23, 0x20000\n"
"s_mov_b32 s26, 0x21c000\n"
"s_mov_b32 s27, 0x20000\n"
"s_mov_b32 s30, 0x280000\n"
"s_mov_b32 s31, 0x20000\n"
"s_mov_b32 s37, 1048576\n"
"s_waitcnt lgkmcnt(0)\n"
"buffer_load_dwordx2 v[6:7], off, s[4:7], 0 sc1\n"
"s_getpc_b64 s[0:1]\n"
".Lk_cpf_here:\n"
"s_sub_u32 s0, s0, .Lk_cpf_here-.Lk_top\n"
"s_subb_u32 s1, s1, 0\n"
"s_lshl_b32 s56, s36, 2\n"
"s_add_u32 s57, s56, s3\n"
"s_mul_i32 s58, s57, 5\n"
"v_min_u32 v2, 4, v0\n"
"v_add_u32 v3, s58, v2\n"
"v_lshlrev_b32 v54, 7, v3\n"
"v_min_u32 v54, .Lk_end-.Lk_top-4, v54\n"
"global_load_dword v56, v54, s[0:1]\n"
"s_cmp_ge_u32 s41, 10\n"
"s_cbranch_scc1 .Lk_vmode_2\n"
"v_and_b32 v8, 15, v0\n"
"v_lshrrev_b32 v9, 4, v0\n"
"s_lshr_b32 s56, s3, 1\n"
"s_lshl_b32 s57, s56, 6\n"
"v_add_u32 v2, s57, v8\n"
"v_lshlrev_b32 v3, 7, v2\n"
"v_lshrrev_b32 v4, 1, v8\n"
"v_xor_b32 v5, v9, v4\n"
"v_lshlrev_b32 v2, 4, v5\n"
"v_add_u32 v10, v3, v2\n"
"v_xor_b32 v11, 64, v10\n"
"v_lshlrev_b32 v2, 3, v9\n"
"v_lshrrev_b32 v3, 2, v8\n"
"v_add_u32 v4, v2, v3\n"
"v_lshlrev_b32 v5, 8, v4\n"
"v_add_u32 v2, 0x4000, v5\n"
"s_and_b32 s56, s3, 1\n"
"s_lshl_b32 s57, s56, 2\n"
"v_lshrrev_b32 v3, 2, v8\n"
"v_lshlrev_b32 v4, 1, v3\n"
"v_xor_b32 v5, s57, v4\n"
"v_and_b32 v3, 1, v9\n"
"v_xor_b32 v4, v5, v3\n"
"v_lshlrev_b32 v5, 5, v4\n"
"v_add_u32 v3, v2, v5\n"
"v_and_b32 v4, 3, v0\n"
"v_lshlrev_b32 v2, 3, v4\n"
"v_add_u32 v14, v3, v2\n"
"v_xor_b32 v15, 0x20, v14\n"
"v_xor_b32 v16, 0x40, v14\n"
"v_xor_b32 v17, 0x60, v14\n"
"v_add_u32 v12, 0x10000, v10\n"
"v_add_u32 v13, 0x10000, v11\n"
"v_add_u32 v18, 0x10000, v14\n"
"v_add_u32 v19, 0x10000, v15\n"
"v_add_u32 v20, 0x10000, v16\n"
"v_add_u32 v21, 0x10000, v17\n"
"v_and_b32 v2, 1, v9\n"
"v_lshlrev_b32 v3, 1, v2\n"
"v_lshrrev_b32 v4, 1, v9\n"
"v_add_u32 v54, v3, v4\n"
"s_cmp_lt_u32 s41, 5\n"
"s_cselect_b32 s42, 0, 5\n"
"s_cselect_b32 s43, 0, 1\n"
"s_cselect_b32 s49, 0x311000, 0\n"
"s_cselect_b32 s50, 0, 0x591000\n"
"s_add_u32 s49, s49, s50\n"
"s_mul_i32 s56, s40, 0x1400\n"
"s_lshl_b32 s57, s56, s43\n"
"s_add_u32 s49, s49, s57\n"
"s_nop 0\n"
"s_cbranch_execz .Lk_tramp\n"
"s_nop 0\n"
"s_lshl_b32 s50, 0x14000, s43\n"
"s_add_u32 s51, s49, s50\n"
"s_lshr_b32 s56, s3, 1\n"
"s_lshl_b32 s57, s56, 6\n"
"v_add_u32 v2, s57, v8\n"
"v_lshlrev_b32 v55, 3, v2\n"
"s_sub_u32 s56, s41, s42\n"
"s_lshl_b32 s57, s56, 4\n"
"s_and_b32 s58, s3, 1\n"
"s_lshl_b32 s59, s58, 3\n"
"s_add_u32 s60, s57, s59\n"
"v_add_u32 v52, s60, v54\n"
"v_mul_u32_u24 v2, 0xcccd, v52\n"
"v_lshrrev_b32 v53, 18, v2\n"
"v_mul_u32_u24 v2, 5, v53\n"
"v_sub_u32 v52, v52, v2\n"
"v_cmp_eq_u32 s[44:45], 2, v52\n"
"v_add_u32 v2, 1, v52\n"
"v_lshrrev_b32 v1, 2, v2\n"
"v_lshlrev_b32 v2, 1, v53\n"
"v_add_u32 v3, v2, v1\n"
"v_mul_u32_u24 v4, s50, v3\n"
"v_mul_u32_u24 v5, 3, v1\n"
"v_sub_u32 v2, v52, v5\n"
"v_lshlrev_b32 v3, 11, v2\n"
"v_add_u32 v5, v4, v3\n"
"v_lshlrev_b32 v2, 1, v55\n"
"v_add_u32 v22, v5, v2\n"
"v_lshlrev_b32 v2, 1, v53\n"
"v_mul_u32_u24 v3, s50, v2\n"
"v_add_u32 v4, 0x1000, v3\n"
"v_add_u32 v23, v4, v55\n"
"s_sub_u32 s56, s41, s42\n"
"s_lshl_b32 s57, s56, 4\n"
"s_and_b32 s58, s3, 1\n"
"s_lshl_b32 s59, s58, 3\n"
"s_add_u32 s60, s57, s59\n"
"s_add_u32 s61, s60, 4\n"
"v_add_u32 v52, s61, v54\n"
"v_mul_u32_u24 v2, 0xcccd, v52\n"
"v_lshrrev_b32 v53, 18, v2\n"
"v_mul_u32_u24 v2, 5, v53\n"
"v_sub_u32 v52, v52, v2\n"
"v_cmp_eq_u32 s[46:47], 2, v52\n"
"v_add_u32 v2, 1, v52\n"
"v_lshrrev_b32 v1, 2, v2\n"
"v_lshlrev_b32 v2, 1, v53\n"
"v_add_u32 v3, v2, v1\n"
"v_mul_u32_u24 v4, s50, v3\n"
"v_mul_u32_u24 v5, 3, v1\n"
"v_sub_u32 v2, v52, v5\n"
"v_lshlrev_b32 v3, 11, v2\n"
"v_add_u32 v5, v4, v3\n"
"v_lshlrev_b32 v2, 1, v55\n"
"v_add_u32 v24, v5, v2\n"
"v_lshlrev_b32 v2, 1, v53\n"
"v_mul_u32_u24 v3, s50, v2\n"
"v_add_u32 v4, 0x1000, v3\n"
"v_add_u32 v25, v4, v55\n"
"s_lshr_b32 s56, s3, 1\n"
"s_lshl_b32 s57, s56, 6\n"
"v_add_u32 v2, s57, v8\n"
"v_lshlrev_b32 v3, 2, v2\n"
"v_add_u32 v26, 0x20800, v3\n"
"s_lshr_b32 s56, s3, 1\n"
"s_lshl_b32 s57, s56, 6\n"
"v_add_u32 v2, s57, v8\n"
"v_lshlrev_b32 v3, 3, v2\n"
"v_add_u32 v1, 0x20400, v3\n"
"s_and_b32 s56, s3, 1\n"
"s_lshl_b32 s57, s56, 6\n"
"v_add_u32 v2, s57, v8\n"
"v_lshlrev_b32 v3, 3, v2\n"
"v_add_u32 v9, 0x20000, v3\n"
"v_mov_b32 v28, 0\n"
"v_mov_b32 v29, 0\n"
"v_mov_b32 v30, 0\n"
"v_mov_b32 v31, 0\n"
"v_mov_b32 v32, 0\n"
"v_mov_b32 v33, 0\n"
"v_mov_b32 v34, 0\n"
"v_mov_b32 v35, 0\n"
"s_waitcnt vmcnt(1)\n"
"v_readfirstlane_b32 s0, v6\n"
"v_readfirstlane_b32 s1, v7\n"
"s_mul_hi_u32 s33, s0, 0x4c957f2d\n"
"s_mul_i32 s32, s0, 0x4c957f2d\n"
"s_mul_i32 s0, s0, 0x5851f42d\n"
"s_mul_i32 s1, s1, 0x4c957f2d\n"
"s_add_u32 s33, s33, s0\n"
"s_add_u32 s33, s33, s1\n"
"s_add_u32 s32, s32, 0xf767814f\n"
"s_add_u32 s33, s33, 0x14057b7e\n"
"s_lshl_b32 s56, s41, 4\n"
"s_add_u32 s57, s56, s40\n"
"s_lshl_b32 s58, s57, 10\n"
"s_add_u32 s12, 0x1000, s58\n"
"v_mov_b32 v60, s32\n"
"v_mov_b32 v61, s33\n"
"s_barrier\n"
"ds_read_b128 v[128:131], v10 offset:0\n"
"ds_read_b64_tr_b16 v[160:161], v14 offset:0\n"
"ds_read_b64_tr_b16 v[162:163], v14 offset:1024\n"
"ds_read_b128 v[132:135], v10 offset:2048\n"
"ds_read_b64_tr_b16 v[164:165], v15 offset:0\n"
"ds_read_b64_tr_b16 v[166:167], v15 offset:1024\n"
"ds_read_b128 v[136:139], v10 offset:4096\n"
"ds_read_b64_tr_b16 v[168:169], v16 offset:0\n"
"ds_read_b64_tr_b16 v[170:171], v16 offset:1024\n"
"ds_read_b128 v[140:143], v10 offset:6144\n"
"ds_read_b64_tr_b16 v[172:173], v17 offset:0\n"
"ds_read_b64_tr_b16 v[174:175], v17 offset:1024\n"
"ds_read_b128 v[144:147], v11 offset:0\n"
"ds_read_b64_tr_b16 v[176:177], v14 offset:8192\n"
"ds_read_b64_tr_b16 v[178:179], v14 offset:9216\n"
"ds_read_b128 v[148:151], v11 offset:2048\n"
"ds_read_b64_tr_b16 v[180:181], v15 offset:8192\n"
"ds_read_b64_tr_b16 v[182:183], v15 offset:9216\n"
"ds_read_b128 v[152:155], v11 offset:4096\n"
"ds_read_b64_tr_b16 v[184:185], v16 offset:8192\n"
"ds_read_b64_tr_b16 v[186:187], v16 offset:9216\n"
"ds_read_b128 v[156:159], v11 offset:6144\n"
"ds_read_b64_tr_b16 v[188:189], v17 offset:8192\n"
"ds_read_b64_tr_b16 v[190:191], v17 offset:9216\n"
"s_waitcnt lgkmcnt(12)\n"
"v_mfma_f32_16x16x32_bf16 v[64:67], v[160:163], v[128:131], 0\n"
"v_mfma_f32_16x16x32_bf16 v[68:71], v[164:167], v[128:131], 0\n"
"v_mfma_f32_16x16x32_bf16 v[72:75], v[168:171], v[128:131], 0\n"
"v_mfma_f32_16x16x32_bf16 v[76:79], v[172:175], v[128:131], 0\n"
"v_mfma_f32_16x16x32_bf16 v[92:95], v[172:175], v[132:135], 0\n"
"v_mfma_f32_16x16x32_bf16 v[88:91], v[168:171], v[132:135], 0\n"
"v_mfma_f32_16x16x32_bf16 v[84:87], v[164:167], v[132:135], 0\n"
"v_mfma_f32_16x16x32_bf16 v[80:83], v[160:163], v[132:135], 0\n"
"v_mfma_f32_16x16x32_bf16 v[96:99], v[160:163], v[136:139], 0\n"
"v_mfma_f32_16x16x32_bf16 v[100:103], v[164:167], v[136:139], 0\n"
"v_mfma_f32_16x16x32_bf16 v[104:107], v[168:171], v[136:139], 0\n"
"v_mfma_f32_16x16x32_bf16 v[108:111], v[172:175], v[136:139], 0\n"
"v_mfma_f32_16x16x32_bf16 v[124:127], v[172:175], v[140:143], 0\n"
"v_mfma_f32_16x16x32_bf16 v[120:123], v[168:171], v[140:143], 0\n"
"v_mfma_f32_16x16x32_bf16 v[116:119], v[164:167], v[140:143], 0\n"
"v_mfma_f32_16x16x32_bf16 v[112:115], v[160:163], v[140:143], 0\n"
"s_waitcnt lgkmcnt(0)\n"
"s_barrier\n"
"s_sleep 1\n"
"v_mfma_f32_16x16x32_bf16 v[64:67], v[176:179], v[144:147], v[64:67]\n"
"ds_read_b128 v[192:195], v10 offset:32768\n"
"ds_read_b64_tr_b16 v[224:225], v14 offset:32768\n"
"v_mfma_f32_16x16x32_bf16 v[68:71], v[180:183], v[144:147], v[68:71]\n"
"ds_read_b64_tr_b16 v[226:227], v14 offset:33792\n"
"v_mfma_f32_16x16x32_bf16 v[72:75], v[184:187], v[144:147], v[72:75]\n"
"ds_read_b128 v[196:199], v10 offset:34816\n"
"ds_read_b64_tr_b16 v[228:229], v15 offset:32768\n"
"v_mfma_f32_16x16x32_bf16 v[76:79], v[188:191], v[144:147], v[76:79]\n"
"ds_read_b64_tr_b16 v[230:231], v15 offset:33792\n"
"v_mfma_f32_16x16x32_bf16 v[92:95], v[188:191], v[148:151], v[92:95]\n"
"ds_read_b128 v[200:203], v10 offset:36864\n"
"ds_read_b64_tr_b16 v[232:233], v16 offset:32768\n"
"v_mfma_f32_16x16x32_bf16 v[88:91], v[184:187], v[148:151], v[88:91]\n"
"ds_read_b64_tr_b16 v[234:235], v16 offset:33792\n"
"v_mfma_f32_16x16x32_bf16 v[84:87], v[180:183], v[148:151], v[84:87]\n"
"ds_read_b128 v[204:207], v10 offset:38912\n"
"ds_read_b64_tr_b16 v[236:237], v17 offset:32768\n"
"v_mfma_f32_16x16x32_bf16 v[80:83], v[176:179], v[148:151], v[80:83]\n"
"ds_read_b64_tr_b16 v[238:239], v17 offset:33792\n"
"v_mfma_f32_16x16x32_bf16 v[96:99], v[176:179], v[152:155], v[96:99]\n"
"ds_read_b128 v[208:211], v11 offset:32768\n"
"ds_read_b64_tr_b16 v[240:241], v14 offset:40960\n"
"v_mfma_f32_16x16x32_bf16 v[100:103], v[180:183], v[152:155], v[100:103]\n"
"ds_read_b64_tr_b16 v[242:243], v14 offset:41984\n"
"v_mfma_f32_16x16x32_bf16 v[104:107], v[184:187], v[152:155], v[104:107]\n"
"ds_read_b128 v[212:215], v11 offset:34816\n"
"ds_read_b64_tr_b16 v[244:245], v15 offset:40960\n"
"v_mfma_f32_16x16x32_bf16 v[108:111], v[188:191], v[152:155], v[108:111]\n"
"ds_read_b64_tr_b16 v[246:247], v15 offset:41984\n"
"v_mfma_f32_16x16x32_bf16 v[124:127], v[188:191], v[156:159], v[124:127]\n"
"ds_read_b128 v[216:219], v11 offset:36864\n"
"ds_read_b64_tr_b16 v[248:249], v16 offset:40960\n"
"v_mfma_f32_16x16x32_bf16 v[120:123], v[184:187], v[156:159], v[120:123]\n"
"ds_read_b64_tr_b16 v[250:251], v16 offset:41984\n"
"v_mfma_f32_16x16x32_bf16 v[116:119], v[180:183], v[156:159], v[116:119]\n"
"ds_read_b128 v[220:223], v11 offset:38912\n"
"ds_read_b64_tr_b16 v[252:253], v17 offset:40960\n"
"v_mfma_f32_16x16x32_bf16 v[112:115], v[176:179], v[156:159], v[112:115]\n"
"ds_read_b64_tr_b16 v[254:255], v17 offset:41984\n"
"s_waitcnt lgkmcnt(12)\n"
"v_mfma_f32_16x16x32_bf16 v[64:67], v[224:227], v[192:195], v[64:67]\n"
"v_mfma_f32_16x16x32_bf16 v[68:71], v[228:231], v[192:195], v[68:71]\n"
"v_mfma_f32_16x16x32_bf16 v[72:75], v[232:235], v[192:195], v[72:75]\n"
"v_mfma_f32_16x16x32_bf16 v[76:79], v[236:239], v[192:195], v[76:79]\n"
"v_mfma_f32_16x16x32_bf16 v[92:95], v[236:239], v[196:199], v[92:95]\n"
"v_mfma_f32_16x16x32_bf16 v[88:91], v[232:235], v[196:199], v[88:91]\n"
"v_mfma_f32_16x16x32_bf16 v[84:87], v[228:231], v[196:199], v[84:87]\n"
"v_mfma_f32_16x16x32_bf16 v[80:83], v[224:227], v[196:199], v[80:83]\n"
"v_mfma_f32_16x16x32_bf16 v[96:99], v[224:227], v[200:203], v[96:99]\n"
"v_mfma_f32_16x16x32_bf16 v[100:103], v[228:231], v[200:203], v[100:103]\n"
"v_mfma_f32_16x16x32_bf16 v[104:107], v[232:235], v[200:203], v[104:107]\n"
"v_mfma_f32_16x16x32_bf16 v[108:111], v[236:239], v[200:203], v[108:111]\n"
"v_mfma_f32_16x16x32_bf16 v[124:127], v[236:239], v[204:207], v[124:127]\n"
"v_mfma_f32_16x16x32_bf16 v[120:123], v[232:235], v[204:207], v[120:123]\n"
"v_mfma_f32_16x16x32_bf16 v[116:119], v[228:231], v[204:207], v[116:119]\n"
"v_mfma_f32_16x16x32_bf16 v[112:115], v[224:227], v[204:207], v[112:115]\n"
"s_waitcnt lgkmcnt(0)\n"
"s_barrier\n"
"s_sleep 1\n"
"v_mfma_f32_16x16x32_bf16 v[64:67], v[240:243], v[208:211], v[64:67]\n"
"ds_read_b128 v[128:131], v12 offset:0\n"
"ds_read_b64_tr_b16 v[160:161], v18 offset:0\n"
"v_mfma_f32_16x16x32_bf16 v[68:71], v[244:247], v[208:211], v[68:71]\n"
"ds_read_b64_tr_b16 v[162:163], v18 offset:1024\n"
"v_mfma_f32_16x16x32_bf16 v[72:75], v[248:251], v[208:211], v[72:75]\n"
"ds_read_b128 v[132:135], v12 offset:2048\n"
"ds_read_b64_tr_b16 v[164:165], v19 offset:0\n"
"v_mfma_f32_16x16x32_bf16 v[76:79], v[252:255], v[208:211], v[76:79]\n"
"ds_read_b64_tr_b16 v[166:167], v19 offset:1024\n"
"v_mfma_f32_16x16x32_bf16 v[92:95], v[252:255], v[212:215], v[92:95]\n"
"ds_read_b128 v[136:139], v12 offset:4096\n"
"ds_read_b64_tr_b16 v[168:169], v20 offset:0\n"
"v_mfma_f32_16x16x32_bf16 v[88:91], v[248:251], v[212:215], v[88:91]\n"
"ds_read_b64_tr_b16 v[170:171], v20 offset:1024\n"
"v_mfma_f32_16x16x32_bf16 v[84:87], v[244:247], v[212:215], v[84:87]\n"
"ds_read_b128 v[140:143], v12 offset:6144\n"
"ds_read_b64_tr_b16 v[172:173], v21 offset:0\n"
"v_mfma_f32_16x16x32_bf16 v[80:83], v[240:243], v[212:215], v[80:83]\n"
"ds_read_b64_tr_b16 v[174:175], v21 offset:1024\n"
"v_mfma_f32_16x16x32_bf16 v[96:99], v[240:243], v[216:219], v[96:99]\n"
"ds_read_b128 v[144:147], v13 offset:0\n"
"ds_read_b64_tr_b16 v[176:177], v18 offset:8192\n"
"v_mfma_f32_16x16x32_bf16 v[100:103], v[244:247], v[216:219], v[100:103]\n"
"ds_read_b64_tr_b16 v[178:179], v18 offset:9216\n"
"v_mfma_f32_16x16x32_bf16 v[104:107], v[248:251], v[216:219], v[104:107]\n"
"ds_read_b128 v[148:151], v13 offset:2048\n"
"ds_read_b64_tr_b16 v[180:181], v19 offset:8192\n"
"v_mfma_f32_16x16x32_bf16 v[108:111], v[252:255], v[216:219], v[108:111]\n"
"ds_read_b64_tr_b16 v[182:183], v19 offset:9216\n"
"v_mfma_f32_16x16x32_bf16 v[124:127], v[252:255], v[220:223], v[124:127]\n"
"ds_read_b128 v[152:155], v13 offset:4096\n"
"ds_read_b64_tr_b16 v[184:185], v20 offset:8192\n"
"v_mfma_f32_16x16x32_bf16 v[120:123], v[248:251], v[220:223], v[120:123]\n"
"ds_read_b64_tr_b16 v[186:187], v20 offset:9216\n"
"v_mfma_f32_16x16x32_bf16 v[116:119], v[244:247], v[220:223], v[116:119]\n"
"ds_read_b128 v[156:159], v13 offset:6144\n"
"ds_read_b64_tr_b16 v[188:189], v21 offset:8192\n"
"v_mfma_f32_16x16x32_bf16 v[112:115], v[240:243], v[220:223], v[112:115]\n"
"ds_read_b64_tr_b16 v[190:191], v21 offset:9216\n"
"s_waitcnt lgkmcnt(12)\n"
"v_mfma_f32_16x16x32_bf16 v[64:67], v[160:163], v[128:131], v[64:67]\n"
"v_mfma_f32_16x16x32_bf16 v[68:71], v[164:167], v[128:131], v[68:71]\n"
"v_mfma_f32_16x16x32_bf16 v[72:75], v[168:171], v[128:131], v[72:75]\n"
"v_mfma_f32_16x16x32_bf16 v[76:79], v[172:175], v[128:131], v[76:79]\n"
"v_mfma_f32_16x16x32_bf16 v[92:95], v[172:175], v[132:135], v[92:95]\n"
"v_mfma_f32_16x16x32_bf16 v[88:91], v[168:171], v[132:135], v[88:91]\n"
"v_mfma_f32_16x16x32_bf16 v[84:87], v[164:167], v[132:135], v[84:87]\n"
"v_mfma_f32_16x16x32_bf16 v[80:83], v[160:163], v[132:135], v[80:83]\n"
"v_mfma_f32_16x16x32_bf16 v[96:99], v[160:163], v[136:139], v[96:99]\n"
"v_mfma_f32_16x16x32_bf16 v[100:103], v[164:167], v[136:139], v[100:103]\n"
"v_mfma_f32_16x16x32_bf16 v[104:107], v[168:171], v[136:139], v[104:107]\n"
"v_mfma_f32_16x16x32_bf16 v[108:111], v[172:175], v[136:139], v[108:111]\n"
"v_mfma_f32_16x16x32_bf16 v[124:127], v[172:175], v[140:143], v[124:127]\n"
"v_mfma_f32_16x16x32_bf16 v[120:123], v[168:171], v[140:143], v[120:123]\n"
"v_mfma_f32_16x16x32_bf16 v[116:119], v[164:167], v[140:143], v[116:119]\n"
"v_mfma_f32_16x16x32_bf16 v[112:115], v[160:163], v[140:143], v[112:115]\n"
"s_waitcnt lgkmcnt(0)\n"
"s_barrier\n"
"s_sleep 1\n"
"v_mfma_f32_16x16x32_bf16 v[64:67], v[176:179], v[144:147], v[64:67]\n"
"ds_read_b128 v[192:195], v12 offset:32768\n"
"ds_read_b64_tr_b16 v[224:225], v18 offset:32768\n"
"v_mfma_f32_16x16x32_bf16 v[68:71], v[180:183], v[144:147], v[68:71]\n"
"ds_read_b64_tr_b16 v[226:227], v18 offset:33792\n"
"v_mfma_f32_16x16x32_bf16 v[72:75], v[184:187], v[144:147], v[72:75]\n"
"ds_read_b128 v[196:199], v12 offset:34816\n"
"ds_read_b64_tr_b16 v[228:229], v19 offset:32768\n"
"v_mfma_f32_16x16x32_bf16 v[76:79], v[188:191], v[144:147], v[76:79]\n"
"ds_read_b64_tr_b16 v[230:231], v19 offset:33792\n"
"v_mfma_f32_16x16x32_bf16 v[92:95], v[188:191], v[148:151], v[92:95]\n"
"ds_read_b128 v[200:203], v12 offset:36864\n"
"ds_read_b64_tr_b16 v[232:233], v20 offset:32768\n"
"v_mfma_f32_16x16x32_bf16 v[88:91], v[184:187], v[148:151], v[88:91]\n"
"ds_read_b64_tr_b16 v[234:235], v20 offset:33792\n"
"v_mfma_f32_16x16x32_bf16 v[84:87], v[180:183], v[148:151], v[84:87]\n"
"ds_read_b128 v[204:207], v12 offset:38912\n"
"ds_read_b64_tr_b16 v[236:237], v21 offset:32768\n"
"v_mfma_f32_16x16x32_bf16 v[80:83], v[176:179], v[148:151], v[80:83]\n"
"ds_read_b64_tr_b16 v[238:239], v21 offset:33792\n"
"v_mfma_f32_16x16x32_bf16 v[96:99], v[176:179], v[152:155], v[96:99]\n"
"ds_read_b128 v[208:211], v13 offset:32768\n"
"ds_read_b64_tr_b16 v[240:241], v18 offset:40960\n"
"v_mfma_f32_16x16x32_bf16 v[100:103], v[180:183], v[152:155], v[100:103]\n"
"ds_read_b64_tr_b16 v[242:243], v18 offset:41984\n"
"v_mfma_f32_16x16x32_bf16 v[104:107], v[184:187], v[152:155], v[104:107]\n"
"ds_read_b128 v[212:215], v13 offset:34816\n"
"ds_read_b64_tr_b16 v[244:245], v19 offset:40960\n"
"v_mfma_f32_16x16x32_bf16 v[108:111], v[188:191], v[152:155], v[108:111]\n"
"ds_read_b64_tr_b16 v[246:247], v19 offset:41984\n"
"v_mfma_f32_16x16x32_bf16 v[124:127], v[188:191], v[156:159], v[124:127]\n"
"ds_read_b128 v[216:219], v13 offset:36864\n"
"ds_read_b64_tr_b16 v[248:249], v20 offset:40960\n"
"v_mfma_f32_16x16x32_bf16 v[120:123], v[184:187], v[156:159], v[120:123]\n"
"ds_read_b64_tr_b16 v[250:251], v20 offset:41984\n"
"v_mfma_f32_16x16x32_bf16 v[116:119], v[180:183], v[156:159], v[116:119]\n"
"ds_read_b128 v[220:223], v13 offset:38912\n"
"ds_read_b64_tr_b16 v[252:253], v21 offset:40960\n"
"v_mfma_f32_16x16x32_bf16 v[112:115], v[176:179], v[156:159], v[112:115]\n"
"ds_read_b64_tr_b16 v[254:255], v21 offset:41984\n"
"s_waitcnt lgkmcnt(12)\n"
"v_mfma_f32_16x16x32_bf16 v[64:67], v[224:227], v[192:195], v[64:67]\n"
"v_mfma_f32_16x16x32_bf16 v[68:71], v[228:231], v[192:195], v[68:71]\n"
"v_mfma_f32_16x16x32_bf16 v[72:75], v[232:235], v[192:195], v[72:75]\n"
"v_mfma_f32_16x16x32_bf16 v[76:79], v[236:239], v[192:195], v[76:79]\n"
"v_mfma_f32_16x16x32_bf16 v[92:95], v[236:239], v[196:199], v[92:95]\n"
"v_mfma_f32_16x16x32_bf16 v[88:91], v[232:235], v[196:199], v[88:91]\n"
"v_mfma_f32_16x16x32_bf16 v[84:87], v[228:231], v[196:199], v[84:87]\n"
"v_mfma_f32_16x16x32_bf16 v[80:83], v[224:227], v[196:199], v[80:83]\n"
"v_mfma_f32_16x16x32_bf16 v[96:99], v[224:227], v[200:203], v[96:99]\n"
"v_mfma_f32_16x16x32_bf16 v[100:103], v[228:231], v[200:203], v[100:103]\n"
"v_mfma_f32_16x16x32_bf16 v[104:107], v[232:235], v[200:203], v[104:107]\n"
"v_mfma_f32_16x16x32_bf16 v[108:111], v[236:239], v[200:203], v[108:111]\n"
"v_mfma_f32_16x16x32_bf16 v[124:127], v[236:239], v[204:207], v[124:127]\n"
"v_mfma_f32_16x16x32_bf16 v[120:123], v[232:235], v[204:207], v[120:123]\n"
"v_mfma_f32_16x16x32_bf16 v[116:119], v[228:231], v[204:207], v[116:119]\n"
"v_mfma_f32_16x16x32_bf16 v[112:115], v[224:227], v[204:207], v[112:115]\n"
"s_waitcnt lgkmcnt(0)\n"
"s_barrier\n"
"s_sleep 1\n"
"v_mfma_f32_16x16x32_bf16 v[64:67], v[240:243], v[208:211], v[64:67]\n"
"ds_read_b128 v[128:131], v10 offset:0\n"
"ds_read_b64_tr_b16 v[160:161], v14 offset:0\n"
"v_mfma_f32_16x16x32_bf16 v[68:71], v[244:247], v[208:211], v[68:71]\n"
"ds_read_b64_tr_b16 v[162:163], v14 offset:1024\n"
"v_mfma_f32_16x16x32_bf16 v[72:75], v[248:251], v[208:211], v[72:75]\n"
"ds_read_b128 v[132:135], v10 offset:2048\n"
"ds_read_b64_tr_b16 v[164:165], v15 offset:0\n"
"v_mfma_f32_16x16x32_bf16 v[76:79], v[252:255], v[208:211], v[76:79]\n"
"ds_read_b64_tr_b16 v[166:167], v15 offset:1024\n"
"v_mfma_f32_16x16x32_bf16 v[92:95], v[252:255], v[212:215], v[92:95]\n"
"ds_read_b128 v[136:139], v10 offset:4096\n"
"ds_read_b64_tr_b16 v[168:169], v16 offset:0\n"
"v_mfma_f32_16x16x32_bf16 v[88:91], v[248:251], v[212:215], v[88:91]\n"
"ds_read_b64_tr_b16 v[170:171], v16 offset:1024\n"
"v_mfma_f32_16x16x32_bf16 v[84:87], v[244:247], v[212:215], v[84:87]\n"
"ds_read_b128 v[140:143], v10 offset:6144\n"
"ds_read_b64_tr_b16 v[172:173], v17 offset:0\n"
"v_mfma_f32_16x16x32_bf16 v[80:83], v[240:243], v[212:215], v[80:83]\n"
"ds_read_b64_tr_b16 v[174:175], v17 offset:1024\n"
"v_mfma_f32_16x16x32_bf16 v[96:99], v[240:243], v[216:219], v[96:99]\n"
"ds_read_b128 v[144:147], v11 offset:0\n"
"ds_read_b64_tr_b16 v[176:177], v14 offset:8192\n"
"v_mfma_f32_16x16x32_bf16 v[100:103], v[244:247], v[216:219], v[100:103]\n"
"ds_read_b64_tr_b16 v[178:179], v14 offset:9216\n"
"v_mfma_f32_16x16x32_bf16 v[104:107], v[248:251], v[216:219], v[104:107]\n"
"ds_read_b128 v[148:151], v11 offset:2048\n"
"ds_read_b64_tr_b16 v[180:181], v15 offset:8192\n"
"v_mfma_f32_16x16x32_bf16 v[108:111], v[252:255], v[216:219], v[108:111]\n"
"ds_read_b64_tr_b16 v[182:183], v15 offset:9216\n"
"v_mfma_f32_16x16x32_bf16 v[124:127], v[252:255], v[220:223], v[124:127]\n"
"ds_read_b128 v[152:155], v11 offset:4096\n"
"ds_read_b64_tr_b16 v[184:185], v16 offset:8192\n"
"v_mfma_f32_16x16x32_bf16 v[120:123], v[248:251], v[220:223], v[120:123]\n"
"ds_read_b64_tr_b16 v[186:187], v16 offset:9216\n"
"v_mfma_f32_16x16x32_bf16 v[116:119], v[244:247], v[220:223], v[116:119]\n"
"ds_read_b128 v[156:159], v11 offset:6144\n"
"ds_read_b64_tr_b16 v[188:189], v17 offset:8192\n"
"v_mfma_f32_16x16x32_bf16 v[112:115], v[240:243], v[220:223], v[112:115]\n"
"ds_read_b64_tr_b16 v[190:191], v17 offset:9216\n"
"s_waitcnt lgkmcnt(12)\n"
"v_mfma_f32_16x16x32_bf16 v[64:67], v[160:163], v[128:131], v[64:67]\n"
"v_mfma_f32_16x16x32_bf16 v[68:71], v[164:167], v[128:131], v[68:71]\n"
"v_mfma_f32_16x16x32_bf16 v[72:75], v[168:171], v[128:131], v[72:75]\n"
"v_mfma_f32_16x16x32_bf16 v[76:79], v[172:175], v[128:131], v[76:79]\n"
"v_mfma_f32_16x16x32_bf16 v[92:95], v[172:175], v[132:135], v[92:95]\n"
"v_mfma_f32_16x16x32_bf16 v[88:91], v[168:171], v[132:135], v[88:91]\n"
"v_mfma_f32_16x16x32_bf16 v[84:87], v[164:167], v[132:135], v[84:87]\n"
"v_mfma_f32_16x16x32_bf16 v[80:83], v[160:163], v[132:135], v[80:83]\n"
"v_mfma_f32_16x16x32_bf16 v[96:99], v[160:163], v[136:139], v[96:99]\n"
"v_mfma_f32_16x16x32_bf16 v[100:103], v[164:167], v[136:139], v[100:103]\n"
"v_mfma_f32_16x16x32_bf16 v[104:107], v[168:171], v[136:139], v[104:107]\n"
"v_mfma_f32_16x16x32_bf16 v[108:111], v[172:175], v[136:139], v[108:111]\n"
"v_mfma_f32_16x16x32_bf16 v[124:127], v[172:175], v[140:143], v[124:127]\n"
"v_mfma_f32_16x16x32_bf16 v[120:123], v[168:171], v[140:143], v[120:123]\n"
"v_mfma_f32_16x16x32_bf16 v[116:119], v[164:167], v[140:143], v[116:119]\n"
"v_mfma_f32_16x16x32_bf16 v[112:115], v[160:163], v[140:143], v[112:115]\n"
"s_waitcnt lgkmcnt(0)\n"
"s_barrier\n"
"s_sleep 1\n"
"v_mfma_f32_16x16x32_bf16 v[64:67], v[176:179], v[144:147], v[64:67]\n"
"ds_read_b128 v[192:195], v10 offset:32768\n"
"ds_read_b64_tr_b16 v[224:225], v14 offset:32768\n"
"v_mfma_f32_16x16x32_bf16 v[68:71], v[180:183], v[144:147], v[68:71]\n"
"ds_read_b64_tr_b16 v[226:227], v14 offset:33792\n"
"v_mfma_f32_16x16x32_bf16 v[72:75], v[184:187], v[144:147], v[72:75]\n"
"ds_read_b128 v[196:199], v10 offset:34816\n"
"ds_read_b64_tr_b16 v[228:229], v15 offset:32768\n"
"v_mfma_f32_16x16x32_bf16 v[76:79], v[188:191], v[144:147], v[76:79]\n"
"ds_read_b64_tr_b16 v[230:231], v15 offset:33792\n"
"v_mfma_f32_16x16x32_bf16 v[92:95], v[188:191], v[148:151], v[92:95]\n"
"ds_read_b128 v[200:203], v10 offset:36864\n"
"ds_read_b64_tr_b16 v[232:233], v16 offset:32768\n"
"v_mfma_f32_16x16x32_bf16 v[88:91], v[184:187], v[148:151], v[88:91]\n"
"ds_read_b64_tr_b16 v[234:235], v16 offset:33792\n"
"v_mfma_f32_16x16x32_bf16 v[84:87], v[180:183], v[148:151], v[84:87]\n"
"ds_read_b128 v[204:207], v10 offset:38912\n"
"ds_read_b64_tr_b16 v[236:237], v17 offset:32768\n"
"v_mfma_f32_16x16x32_bf16 v[80:83], v[176:179], v[148:151], v[80:83]\n"
"ds_read_b64_tr_b16 v[238:239], v17 offset:33792\n"
"v_mfma_f32_16x16x32_bf16 v[96:99], v[176:179], v[152:155], v[96:99]\n"
"ds_read_b128 v[208:211], v11 offset:32768\n"
"ds_read_b64_tr_b16 v[240:241], v14 offset:40960\n"
"v_mfma_f32_16x16x32_bf16 v[100:103], v[180:183], v[152:155], v[100:103]\n"
"ds_read_b64_tr_b16 v[242:243], v14 offset:41984\n"
"v_mfma_f32_16x16x32_bf16 v[104:107], v[184:187], v[152:155], v[104:107]\n"
"ds_read_b128 v[212:215], v11 offset:34816\n"
"ds_read_b64_tr_b16 v[244:245], v15 offset:40960\n"
"v_mfma_f32_16x16x32_bf16 v[108:111], v[188:191], v[152:155], v[108:111]\n"
"ds_read_b64_tr_b16 v[246:247], v15 offset:41984\n"
"v_mfma_f32_16x16x32_bf16 v[124:127], v[188:191], v[156:159], v[124:127]\n"
"ds_read_b128 v[216:219], v11 offset:36864\n"
"ds_read_b64_tr_b16 v[248:249], v16 offset:40960\n"
"v_mfma_f32_16x16x32_bf16 v[120:123], v[184:187], v[156:159], v[120:123]\n"
"ds_read_b64_tr_b16 v[250:251], v16 offset:41984\n"
"v_mfma_f32_16x16x32_bf16 v[116:119], v[180:183], v[156:159], v[116:119]\n"
"ds_read_b128 v[220:223], v11 offset:38912\n"
"ds_read_b64_tr_b16 v[252:253], v17 offset:40960\n"
"v_mfma_f32_16x16x32_bf16 v[112:115], v[176:179], v[156:159], v[112:115]\n"
"ds_read_b64_tr_b16 v[254:255], v17 offset:41984\n"
"s_waitcnt lgkmcnt(12)\n"
"v_mfma_f32_16x16x32_bf16 v[64:67], v[224:227], v[192:195], v[64:67]\n"
"v_mfma_f32_16x16x32_bf16 v[68:71], v[228:231], v[192:195], v[68:71]\n"
"v_mfma_f32_16x16x32_bf16 v[72:75], v[232:235], v[192:195], v[72:75]\n"
"v_mfma_f32_16x16x32_bf16 v[76:79], v[236:239], v[192:195], v[76:79]\n"
"v_mfma_f32_16x16x32_bf16 v[92:95], v[236:239], v[196:199], v[92:95]\n"
"v_mfma_f32_16x16x32_bf16 v[88:91], v[232:235], v[196:199], v[88:91]\n"
"v_mfma_f32_16x16x32_bf16 v[84:87], v[228:231], v[196:199], v[84:87]\n"
"v_mfma_f32_16x16x32_bf16 v[80:83], v[224:227], v[196:199], v[80:83]\n"
"v_mfma_f32_16x16x32_bf16 v[96:99], v[224:227], v[200:203], v[96:99]\n"
"v_mfma_f32_16x16x32_bf16 v[100:103], v[228:231], v[200:203], v[100:103]\n"
"v_mfma_f32_16x16x32_bf16 v[104:107], v[232:235], v[200:203], v[104:107]\n"
"v_mfma_f32_16x16x32_bf16 v[108:111], v[236:239], v[200:203], v[108:111]\n"
"v_mfma_f32_16x16x32_bf16 v[124:127], v[236:239], v[204:207], v[124:127]\n"
"v_mfma_f32_16x16x32_bf16 v[120:123], v[232:235], v[204:207], v[120:123]\n"
"v_mfma_f32_16x16x32_bf16 v[116:119], v[228:231], v[204:207], v[116:119]\n"
"v_mfma_f32_16x16x32_bf16 v[112:115], v[224:227], v[204:207], v[112:115]\n"
"s_waitcnt lgkmcnt(0)\n"
"s_barrier\n"
"s_sleep 1\n"
"v_mfma_f32_16x16x32_bf16 v[64:67], v[240:243], v[208:211], v[64:67]\n"
"ds_read_b128 v[128:131], v12 offset:0\n"
"ds_read_b64_tr_b16 v[160:161], v18 offset:0\n"
"v_mfma_f32_16x16x32_bf16 v[68:71], v[244:247], v[208:211], v[68:71]\n"
"ds_read_b64_tr_b16 v[162:163], v18 offset:1024\n"
"v_mfma_f32_16x16x32_bf16 v[72:75], v[248:251], v[208:211], v[72:75]\n"
"ds_read_b128 v[132:135], v12 offset:2048\n"
"ds_read_b64_tr_b16 v[164:165], v19 offset:0\n"
"v_mfma_f32_16x16x32_bf16 v[76:79], v[252:255], v[208:211], v[76:79]\n"
"ds_read_b64_tr_b16 v[166:167], v19 offset:1024\n"
"v_mfma_f32_16x16x32_bf16 v[92:95], v[252:255], v[212:215], v[92:95]\n"
"ds_read_b128 v[136:139], v12 offset:4096\n"
"ds_read_b64_tr_b16 v[168:169], v20 offset:0\n"
"v_mfma_f32_16x16x32_bf16 v[88:91], v[248:251], v[212:215], v[88:91]\n"
"ds_read_b64_tr_b16 v[170:171], v20 offset:1024\n"
"v_mfma_f32_16x16x32_bf16 v[84:87], v[244:247], v[212:215], v[84:87]\n"
"ds_read_b128 v[140:143], v12 offset:6144\n"
"ds_read_b64_tr_b16 v[172:173], v21 offset:0\n"
"v_mfma_f32_16x16x32_bf16 v[80:83], v[240:243], v[212:215], v[80:83]\n"
"ds_read_b64_tr_b16 v[174:175], v21 offset:1024\n"
"v_mfma_f32_16x16x32_bf16 v[96:99], v[240:243], v[216:219], v[96:99]\n"
"ds_read_b128 v[144:147], v13 offset:0\n"
"ds_read_b64_tr_b16 v[176:177], v18 offset:8192\n"
"v_mfma_f32_16x16x32_bf16 v[100:103], v[244:247], v[216:219], v[100:103]\n"
"ds_read_b64_tr_b16 v[178:179], v18 offset:9216\n"
"v_mfma_f32_16x16x32_bf16 v[104:107], v[248:251], v[216:219], v[104:107]\n"
"ds_read_b128 v[148:151], v13 offset:2048\n"
"ds_read_b64_tr_b16 v[180:181], v19 offset:8192\n"
"v_mfma_f32_16x16x32_bf16 v[108:111], v[252:255], v[216:219], v[108:111]\n"
"ds_read_b64_tr_b16 v[182:183], v19 offset:9216\n"
"v_mfma_f32_16x16x32_bf16 v[124:127], v[252:255], v[220:223], v[124:127]\n"
"ds_read_b128 v[152:155], v13 offset:4096\n"
"ds_read_b64_tr_b16 v[184:185], v20 offset:8192\n"
"v_mfma_f32_16x16x32_bf16 v[120:123], v[248:251], v[220:223], v[120:123]\n"
"ds_read_b64_tr_b16 v[186:187], v20 offset:9216\n"
"v_mfma_f32_16x16x32_bf16 v[116:119], v[244:247], v[220:223], v[116:119]\n"
"ds_read_b128 v[156:159], v13 offset:6144\n"
"ds_read_b64_tr_b16 v[188:189], v21 offset:8192\n"
"v_mfma_f32_16x16x32_bf16 v[112:115], v[240:243], v[220:223], v[112:115]\n"
"ds_read_b64_tr_b16 v[190:191], v21 offset:9216\n"
"s_waitcnt lgkmcnt(12)\n"
"v_mfma_f32_16x16x32_bf16 v[64:67], v[160:163], v[128:131], v[64:67]\n"
"v_mfma_f32_16x16x32_bf16 v[68:71], v[164:167], v[128:131], v[68:71]\n"
"v_mfma_f32_16x16x32_bf16 v[72:75], v[168:171], v[128:131], v[72:75]\n"
"v_mfma_f32_16x16x32_bf16 v[76:79], v[172:175], v[128:131], v[76:79]\n"
"v_mfma_f32_16x16x32_bf16 v[92:95], v[172:175], v[132:135], v[92:95]\n"
"v_mfma_f32_16x16x32_bf16 v[88:91], v[168:171], v[132:135], v[88:91]\n"
"v_mfma_f32_16x16x32_bf16 v[84:87], v[164:167], v[132:135], v[84:87]\n"
"v_mfma_f32_16x16x32_bf16 v[80:83], v[160:163], v[132:135], v[80:83]\n"
"v_mfma_f32_16x16x32_bf16 v[96:99], v[160:163], v[136:139], v[96:99]\n"
"v_mfma_f32_16x16x32_bf16 v[100:103], v[164:167], v[136:139], v[100:103]\n"
"v_mfma_f32_16x16x32_bf16 v[104:107], v[168:171], v[136:139], v[104:107]\n"
"v_mfma_f32_16x16x32_bf16 v[108:111], v[172:175], v[136:139], v[108:111]\n"
"v_mfma_f32_16x16x32_bf16 v[124:127], v[172:175], v[140:143], v[124:127]\n"
"v_mfma_f32_16x16x32_bf16 v[120:123], v[168:171], v[140:143], v[120:123]\n"
"v_mfma_f32_16x16x32_bf16 v[116:119], v[164:167], v[140:143], v[116:119]\n"
"v_mfma_f32_16x16x32_bf16 v[112:115], v[160:163], v[140:143], v[112:115]\n"
"s_waitcnt lgkmcnt(0)\n"
"s_barrier\n"
"s_sleep 1\n"
"v_mfma_f32_16x16x32_bf16 v[64:67], v[176:179], v[144:147], v[64:67]\n"
"ds_read_b128 v[192:195], v12 offset:32768\n"
"ds_read_b64_tr_b16 v[224:225], v18 offset:32768\n"
"v_mfma_f32_16x16x32_bf16 v[68:71], v[180:183], v[144:147], v[68:71]\n"
"ds_read_b64_tr_b16 v[226:227], v18 offset:33792\n"
"v_mfma_f32_16x16x32_bf16 v[72:75], v[184:187], v[144:147], v[72:75]\n"
"ds_read_b128 v[196:199], v12 offset:34816\n"
"ds_read_b64_tr_b16 v[228:229], v19 offset:32768\n"
"v_mfma_f32_16x16x32_bf16 v[76:79], v[188:191], v[144:147], v[76:79]\n"
"ds_read_b64_tr_b16 v[230:231], v19 offset:33792\n"
"v_mfma_f32_16x16x32_bf16 v[92:95], v[188:191], v[148:151], v[92:95]\n"
"ds_read_b128 v[200:203], v12 offset:36864\n"
"ds_read_b64_tr_b16 v[232:233], v20 offset:32768\n"
"v_mfma_f32_16x16x32_bf16 v[88:91], v[184:187], v[148:151], v[88:91]\n"
"ds_read_b64_tr_b16 v[234:235], v20 offset:33792\n"
"v_mfma_f32_16x16x32_bf16 v[84:87], v[180:183], v[148:151], v[84:87]\n"
"ds_read_b128 v[204:207], v12 offset:38912\n"
"ds_read_b64_tr_b16 v[236:237], v21 offset:32768\n"
"v_mfma_f32_16x16x32_bf16 v[80:83], v[176:179], v[148:151], v[80:83]\n"
"ds_read_b64_tr_b16 v[238:239], v21 offset:33792\n"
"v_mfma_f32_16x16x32_bf16 v[96:99], v[176:179], v[152:155], v[96:99]\n"
"ds_read_b128 v[208:211], v13 offset:32768\n"
"ds_read_b64_tr_b16 v[240:241], v18 offset:40960\n"
"v_mfma_f32_16x16x32_bf16 v[100:103], v[180:183], v[152:155], v[100:103]\n"
"ds_read_b64_tr_b16 v[242:243], v18 offset:41984\n"
"v_mfma_f32_16x16x32_bf16 v[104:107], v[184:187], v[152:155], v[104:107]\n"
"ds_read_b128 v[212:215], v13 offset:34816\n"
"ds_read_b64_tr_b16 v[244:245], v19 offset:40960\n"
"v_mfma_f32_16x16x32_bf16 v[108:111], v[188:191], v[152:155], v[108:111]\n"
"ds_read_b64_tr_b16 v[246:247], v19 offset:41984\n"
"v_mfma_f32_16x16x32_bf16 v[124:127], v[188:191], v[156:159], v[124:127]\n"
"ds_read_b128 v[216:219], v13 offset:36864\n"
"ds_read_b64_tr_b16 v[248:249], v20 offset:40960\n"
"v_mfma_f32_16x16x32_bf16 v[120:123], v[184:187], v[156:159], v[120:123]\n"
"ds_read_b64_tr_b16 v[250:251], v20 offset:41984\n"
"v_mfma_f32_16x16x32_bf16 v[116:119], v[180:183], v[156:159], v[116:119]\n"
"ds_read_b128 v[220:223], v13 offset:38912\n"
"ds_read_b64_tr_b16 v[252:253], v21 offset:40960\n"
"v_mfma_f32_16x16x32_bf16 v[112:115], v[176:179], v[156:159], v[112:115]\n"
"ds_read_b64_tr_b16 v[254:255], v21 offset:41984\n"
"s_waitcnt lgkmcnt(12)\n"
"v_mfma_f32_16x16x32_bf16 v[64:67], v[224:227], v[192:195], v[64:67]\n"
"v_mfma_f32_16x16x32_bf16 v[68:71], v[228:231], v[192:195], v[68:71]\n"
"v_mfma_f32_16x16x32_bf16 v[72:75], v[232:235], v[192:195], v[72:75]\n"
"v_mfma_f32_16x16x32_bf16 v[76:79], v[236:239], v[192:195], v[76:79]\n"
"v_mfma_f32_16x16x32_bf16 v[92:95], v[236:239], v[196:199], v[92:95]\n"
"v_mfma_f32_16x16x32_bf16 v[88:91], v[232:235], v[196:199], v[88:91]\n"
"v_mfma_f32_16x16x32_bf16 v[84:87], v[228:231], v[196:199], v[84:87]\n"
"v_mfma_f32_16x16x32_bf16 v[80:83], v[224:227], v[196:199], v[80:83]\n"
"v_mfma_f32_16x16x32_bf16 v[96:99], v[224:227], v[200:203], v[96:99]\n"
"v_mfma_f32_16x16x32_bf16 v[100:103], v[228:231], v[200:203], v[100:103]\n"
"v_mfma_f32_16x16x32_bf16 v[104:107], v[232:235], v[200:203], v[104:107]\n"
"v_mfma_f32_16x16x32_bf16 v[108:111], v[236:239], v[200:203], v[108:111]\n"
"v_mfma_f32_16x16x32_bf16 v[124:127], v[236:239], v[204:207], v[124:127]\n"
"v_mfma_f32_16x16x32_bf16 v[120:123], v[232:235], v[204:207], v[120:123]\n"
"v_mfma_f32_16x16x32_bf16 v[116:119], v[228:231], v[204:207], v[116:119]\n"
"v_mfma_f32_16x16x32_bf16 v[112:115], v[224:227], v[204:207], v[112:115]\n"
"s_waitcnt lgkmcnt(0)\n"
"s_barrier\n"
"s_sleep 1\n"
"v_mfma_f32_16x16x32_bf16 v[64:67], v[240:243], v[208:211], v[64:67]\n"
"ds_read_b128 v[128:131], v10 offset:0\n"
"ds_read_b64_tr_b16 v[160:161], v14 offset:0\n"
"v_mfma_f32_16x16x32_bf16 v[68:71], v[244:247], v[208:211], v[68:71]\n"
"s_nop 0\n"
"s_cbranch_execz .Lk_tramp\n"
"s_nop 0\n"
"ds_read_b64_tr_b16 v[162:163], v14 offset:1024\n"
"v_mfma_f32_16x16x32_bf16 v[72:75], v[248:251], v[208:211], v[72:75]\n"
"ds_read_b128 v[132:135], v10 offset:2048\n"
"ds_read_b64_tr_b16 v[164:165], v15 offset:0\n"
"v_mfma_f32_16x16x32_bf16 v[76:79], v[252:255], v[208:211], v[76:79]\n"
"ds_read_b64_tr_b16 v[166:167], v15 offset:1024\n"
"v_mfma_f32_16x16x32_bf16 v[92:95], v[252:255], v[212:215], v[92:95]\n"
"ds_read_b128 v[136:139], v10 offset:4096\n"
"ds_read_b64_tr_b16 v[168:169], v16 offset:0\n"
"v_mfma_f32_16x16x32_bf16 v[88:91], v[248:251], v[212:215], v[88:91]\n"
"ds_read_b64_tr_b16 v[170:171], v16 offset:1024\n"
"v_mfma_f32_16x16x32_bf16 v[84:87], v[244:247], v[212:215], v[84:87]\n"
"ds_read_b128 v[140:143], v10 offset:6144\n"
"ds_read_b64_tr_b16 v[172:173], v17 offset:0\n"
"v_mfma_f32_16x16x32_bf16 v[80:83], v[240:243], v[212:215], v[80:83]\n"
"ds_read_b64_tr_b16 v[174:175], v17 offset:1024\n"
"v_mfma_f32_16x16x32_bf16 v[96:99], v[240:243], v[216:219], v[96:99]\n"
"ds_read_b128 v[144:147], v11 offset:0\n"
"ds_read_b64_tr_b16 v[176:177], v14 offset:8192\n"
"v_mfma_f32_16x16x32_bf16 v[100:103], v[244:247], v[216:219], v[100:103]\n"
"ds_read_b64_tr_b16 v[178:179], v14 offset:9216\n"
"v_mfma_f32_16x16x32_bf16 v[104:107], v[248:251], v[216:219], v[104:107]\n"
"ds_read_b128 v[148:151], v11 offset:2048\n"
"ds_read_b64_tr_b16 v[180:181], v15 offset:8192\n"
"v_mfma_f32_16x16x32_bf16 v[108:111], v[252:255], v[216:219], v[108:111]\n"
"ds_read_b64_tr_b16 v[182:183], v15 offset:9216\n"
"v_mfma_f32_16x16x32_bf16 v[124:127], v[252:255], v[220:223], v[124:127]\n"
"ds_read_b128 v[152:155], v11 offset:4096\n"
"ds_read_b64_tr_b16 v[184:185], v16 offset:8192\n"
"v_mfma_f32_16x16x32_bf16 v[120:123], v[248:251], v[220:223], v[120:123]\n"
"ds_read_b64_tr_b16 v[186:187], v16 offset:9216\n"
"v_mfma_f32_16x16x32_bf16 v[116:119], v[244:247], v[220:223], v[116:119]\n"
"ds_read_b128 v[156:159], v11 offset:6144\n"
"ds_read_b64_tr_b16 v[188:189], v17 offset:8192\n"
"v_mfma_f32_16x16x32_bf16 v[112:115], v[240:243], v[220:223], v[112:115]\n"
"ds_read_b64_tr_b16 v[190:191], v17 offset:9216\n"
"s_waitcnt lgkmcnt(12)\n"
"v_mfma_f32_16x16x32_bf16 v[64:67], v[160:163], v[128:131], v[64:67]\n"
"v_mfma_f32_16x16x32_bf16 v[68:71], v[164:167], v[128:131], v[68:71]\n"
"v_mfma_f32_16x16x32_bf16 v[72:75], v[168:171], v[128:131], v[72:75]\n"
"v_mfma_f32_16x16x32_bf16 v[76:79], v[172:175], v[128:131], v[76:79]\n"
"v_mfma_f32_16x16x32_bf16 v[92:95], v[172:175], v[132:135], v[92:95]\n"
"v_mfma_f32_16x16x32_bf16 v[88:91], v[168:171], v[132:135], v[88:91]\n"
"v_mfma_f32_16x16x32_bf16 v[84:87], v[164:167], v[132:135], v[84:87]\n"
"v_mfma_f32_16x16x32_bf16 v[80:83], v[160:163], v[132:135], v[80:83]\n"
"v_mfma_f32_16x16x32_bf16 v[96:99], v[160:163], v[136:139], v[96:99]\n"
"v_mfma_f32_16x16x32_bf16 v[100:103], v[164:167], v[136:139], v[100:103]\n"
"v_mfma_f32_16x16x32_bf16 v[104:107], v[168:171], v[136:139], v[104:107]\n"
"v_mfma_f32_16x16x32_bf16 v[108:111], v[172:175], v[136:139], v[108:111]\n"
"v_mfma_f32_16x16x32_bf16 v[124:127], v[172:175], v[140:143], v[124:127]\n"
"v_mfma_f32_16x16x32_bf16 v[120:123], v[168:171], v[140:143], v[120:123]\n"
"v_mfma_f32_16x16x32_bf16 v[116:119], v[164:167], v[140:143], v[116:119]\n"
"v_mfma_f32_16x16x32_bf16 v[112:115], v[160:163], v[140:143], v[112:115]\n"
"s_waitcnt lgkmcnt(0)\n"
"s_barrier\n"
"s_mov_b64 exec, 0xffff\n"
"ds_read_b64 v[28:29], v1 offset:0\n"
"ds_read_b64 v[30:31], v1 offset:128\n"
"ds_read_b64 v[32:33], v1 offset:256\n"
"ds_read_b64 v[34:35], v1 offset:384\n"
"s_mov_b64 exec, -1\n"
"v_mfma_f32_16x16x32_bf16 v[64:67], v[176:179], v[144:147], v[64:67]\n"
"ds_read_b64 v[52:53], v9 offset:0\n"
"ds_read_b64 v[54:55], v9 offset:128\n"
"v_mfma_f32_16x16x32_bf16 v[68:71], v[180:183], v[144:147], v[68:71]\n"
"s_waitcnt vmcnt(0)\n"
"ds_read_b64 v[56:57], v9 offset:256\n"
"v_mfma_f32_16x16x32_bf16 v[72:75], v[184:187], v[144:147], v[72:75]\n"
"ds_read_b64 v[58:59], v9 offset:384\n"
"ds_read_b128 v[192:195], v10 offset:32768\n"
"v_mfma_f32_16x16x32_bf16 v[76:79], v[188:191], v[144:147], v[76:79]\n"
"ds_read_b64_tr_b16 v[224:225], v14 offset:32768\n"
"v_mfma_f32_16x16x32_bf16 v[92:95], v[188:191], v[148:151], v[92:95]\n"
"ds_read_b64_tr_b16 v[226:227], v14 offset:33792\n"
"ds_read_b64_tr_b16 v[228:229], v15 offset:32768\n"
"v_mfma_f32_16x16x32_bf16 v[88:91], v[184:187], v[148:151], v[88:91]\n"
"ds_read_b64_tr_b16 v[230:231], v15 offset:33792\n"
"v_mfma_f32_16x16x32_bf16 v[84:87], v[180:183], v[148:151], v[84:87]\n"
"ds_read_b64_tr_b16 v[232:233], v16 offset:32768\n"
"ds_read_b64_tr_b16 v[234:235], v16 offset:33792\n"
"v_mfma_f32_16x16x32_bf16 v[80:83], v[176:179], v[148:151], v[80:83]\n"
"ds_read_b64_tr_b16 v[236:237], v17 offset:32768\n"
"v_mfma_f32_16x16x32_bf16 v[96:99], v[176:179], v[152:155], v[96:99]\n"
"ds_read_b64_tr_b16 v[238:239], v17 offset:33792\n"
"ds_read_b128 v[208:211], v11 offset:32768\n"
"v_mfma_f32_16x16x32_bf16 v[100:103], v[180:183], v[152:155], v[100:103]\n"
"ds_read_b64_tr_b16 v[240:241], v14 offset:40960\n"
"v_mfma_f32_16x16x32_bf16 v[104:107], v[184:187], v[152:155], v[104:107]\n"
"ds_read_b64_tr_b16 v[242:243], v14 offset:41984\n"
"ds_read_b64_tr_b16 v[244:245], v15 offset:40960\n"
"v_mfma_f32_16x16x32_bf16 v[108:111], v[188:191], v[152:155], v[108:111]\n"
"ds_read_b64_tr_b16 v[246:247], v15 offset:41984\n"
"v_mfma_f32_16x16x32_bf16 v[124:127], v[188:191], v[156:159], v[124:127]\n"
"ds_read_b64_tr_b16 v[248:249], v16 offset:40960\n"
"ds_read_b64_tr_b16 v[250:251], v16 offset:41984\n"
"v_mfma_f32_16x16x32_bf16 v[120:123], v[184:187], v[156:159], v[120:123]\n"
"ds_read_b64_tr_b16 v[252:253], v17 offset:40960\n"
"v_mfma_f32_16x16x32_bf16 v[116:119], v[180:183], v[156:159], v[116:119]\n"
"ds_read_b64_tr_b16 v[254:255], v17 offset:41984\n"
"ds_read_b128 v[196:199], v10 offset:34816\n"
"v_mfma_f32_16x16x32_bf16 v[112:115], v[176:179], v[156:159], v[112:115]\n"
"ds_read_b128 v[212:215], v11 offset:34816\n"
"s_waitcnt lgkmcnt(15)\n"
"v_mfma_f32_16x16x16_bf16 v[64:67], v[52:53], v[28:29], v[64:67]\n"
"ds_read_b128 v[200:203], v10 offset:36864\n"
"ds_read_b128 v[216:219], v11 offset:36864\n"
"v_mfma_f32_16x16x16_bf16 v[68:71], v[54:55], v[28:29], v[68:71]\n"
"ds_read_b128 v[204:207], v10 offset:38912\n"
"v_mfma_f32_16x16x16_bf16 v[72:75], v[56:57], v[28:29], v[72:75]\n"
"ds_read_b128 v[220:223], v11 offset:38912\n"
"ds_read_b32 v128, v26 offset:0\n"
"v_mfma_f32_16x16x16_bf16 v[76:79], v[58:59], v[28:29], v[76:79]\n"
"ds_read_b32 v129, v26 offset:64\n"
"v_mfma_f32_16x16x32_bf16 v[64:67], v[224:227], v[192:195], v[64:67]\n"
"ds_read_b32 v130, v26 offset:128\n"
"ds_read_b32 v131, v26 offset:192\n"
"v_mfma_f32_16x16x32_bf16 v[68:71], v[228:231], v[192:195], v[68:71]\n"
"s_waitcnt lgkmcnt(15)\n"
"v_mfma_f32_16x16x32_bf16 v[72:75], v[232:235], v[192:195], v[72:75]\n"
"v_mfma_f32_16x16x32_bf16 v[76:79], v[236:239], v[192:195], v[76:79]\n"
"v_mfma_f32_16x16x32_bf16 v[64:67], v[240:243], v[208:211], v[64:67]\n"
"s_waitcnt lgkmcnt(14)\n"
"v_mfma_f32_16x16x32_bf16 v[68:71], v[244:247], v[208:211], v[68:71]\n"
"s_waitcnt lgkmcnt(12)\n"
"v_mfma_f32_16x16x32_bf16 v[72:75], v[248:251], v[208:211], v[72:75]\n"
"s_waitcnt lgkmcnt(10)\n"
"v_mfma_f32_16x16x32_bf16 v[76:79], v[252:255], v[208:211], v[76:79]\n"
"v_mfma_f32_16x16x16_bf16 v[80:83], v[52:53], v[30:31], v[80:83]\n"
"v_mfma_f32_16x16x16_bf16 v[84:87], v[54:55], v[30:31], v[84:87]\n"
"v_mfma_f32_16x16x16_bf16 v[88:91], v[56:57], v[30:31], v[88:91]\n"
"v_mfma_f32_16x16x16_bf16 v[92:95], v[58:59], v[30:31], v[92:95]\n"
"s_waitcnt lgkmcnt(3)\n"
"v_mul_f32 v64, v64, v128\n"
"v_mul_f32 v65, v65, v128\n"
"v_mfma_f32_16x16x32_bf16 v[80:83], v[224:227], v[196:199], v[80:83]\n"
"v_mul_f32 v66, v66, v128\n"
"v_mul_f32 v67, v67, v128\n"
"v_mfma_f32_16x16x32_bf16 v[84:87], v[228:231], v[196:199], v[84:87]\n"
"v_mul_f32 v68, v68, v128\n"
"v_mul_f32 v69, v69, v128\n"
"v_mfma_f32_16x16x32_bf16 v[88:91], v[232:235], v[196:199], v[88:91]\n"
"v_mul_f32 v70, v70, v128\n"
"v_mul_f32 v71, v71, v128\n"
"v_mfma_f32_16x16x32_bf16 v[92:95], v[236:239], v[196:199], v[92:95]\n"
"v_cvt_pk_bf16_f32 v36, v64, v65\n"
"v_cvt_pk_bf16_f32 v37, v66, v67\n"
"v_mfma_f32_16x16x32_bf16 v[80:83], v[240:243], v[212:215], v[80:83]\n"
"v_cvt_pk_bf16_f32 v38, v68, v69\n"
"v_cvt_pk_bf16_f32 v39, v70, v71\n"
"v_mfma_f32_16x16x32_bf16 v[84:87], v[244:247], v[212:215], v[84:87]\n"
"v_mul_f32 v72, v72, v128\n"
"v_mul_f32 v73, v73, v128\n"
"v_mfma_f32_16x16x32_bf16 v[88:91], v[248:251], v[212:215], v[88:91]\n"
"v_permlane16_swap_b32 v36, v38\n"
"v_permlane16_swap_b32 v37, v39\n"
"v_mfma_f32_16x16x32_bf16 v[92:95], v[252:255], v[212:215], v[92:95]\n"
"s_andn2_b64 exec, -1, s[44:45]\n"
"buffer_store_dwordx4 v[36:39], v22, s[4:7], s49 offen offset:0 sc1\n"
"s_mov_b64 exec, s[44:45]\n"
"s_cbranch_execz .Lk_nor_11\n"
"buffer_store_dwordx2 v[36:37], v23, s[4:7], s49 offen offset:0 sc1\n"
"buffer_store_dwordx2 v[38:39], v23, s[4:7], s51 offen offset:0 sc1\n"
".Lk_nor_11:\n"
"s_mov_b64 exec, -1\n"
"v_mul_f32 v74, v74, v128\n"
"v_mul_f32 v75, v75, v128\n"
"v_mfma_f32_16x16x16_bf16 v[96:99], v[52:53], v[32:33], v[96:99]\n"
"v_mul_f32 v76, v76, v128\n"
"v_mul_f32 v77, v77, v128\n"
"v_mfma_f32_16x16x16_bf16 v[100:103], v[54:55], v[32:33], v[100:103]\n"
"v_mul_f32 v78, v78, v128\n"
"v_mul_f32 v79, v79, v128\n"
"v_mfma_f32_16x16x16_bf16 v[104:107], v[56:57], v[32:33], v[104:107]\n"
"v_cvt_pk_bf16_f32 v40, v72, v73\n"
"v_cvt_pk_bf16_f32 v41, v74, v75\n"
"v_mfma_f32_16x16x16_bf16 v[108:111], v[58:59], v[32:33], v[108:111]\n"
"v_cvt_pk_bf16_f32 v42, v76, v77\n"
"v_cvt_pk_bf16_f32 v43, v78, v79\n"
"v_mfma_f32_16x16x32_bf16 v[96:99], v[224:227], v[200:203], v[96:99]\n"
"v_permlane16_swap_b32 v40, v42\n"
"v_permlane16_swap_b32 v41, v43\n"
"v_mfma_f32_16x16x32_bf16 v[100:103], v[228:231], v[200:203], v[100:103]\n"
"s_andn2_b64 exec, -1, s[46:47]\n"
"buffer_store_dwordx4 v[40:43], v24, s[4:7], s49 offen offset:0 sc1\n"
"s_mov_b64 exec, s[46:47]\n"
"s_cbranch_execz .Lk_nor_12\n"
"buffer_store_dwordx2 v[40:41], v25, s[4:7], s49 offen offset:0 sc1\n"
"buffer_store_dwordx2 v[42:43], v25, s[4:7], s51 offen offset:0 sc1\n"
".Lk_nor_12:\n"
"s_mov_b64 exec, -1\n"
"s_waitcnt lgkmcnt(2)\n"
"v_mul_f32 v80, v80, v129\n"
"v_mul_f32 v81, v81, v129\n"
"v_mfma_f32_16x16x32_bf16 v[104:107], v[232:235], v[200:203], v[104:107]\n"
"v_mul_f32 v82, v82, v129\n"
"v_mul_f32 v83, v83, v129\n"
"v_mfma_f32_16x16x32_bf16 v[108:111], v[236:239], v[200:203], v[108:111]\n"
"v_mul_f32 v84, v84, v129\n"
"v_mul_f32 v85, v85, v129\n"
"v_mfma_f32_16x16x32_bf16 v[96:99], v[240:243], v[216:219], v[96:99]\n"
"v_mul_f32 v86, v86, v129\n"
"v_mul_f32 v87, v87, v129\n"
"v_mfma_f32_16x16x32_bf16 v[100:103], v[244:247], v[216:219], v[100:103]\n"
"v_cvt_pk_bf16_f32 v44, v80, v81\n"
"v_cvt_pk_bf16_f32 v45, v82, v83\n"
"v_mfma_f32_16x16x32_bf16 v[104:107], v[248:251], v[216:219], v[104:107]\n"
"v_cvt_pk_bf16_f32 v46, v84, v85\n"
"v_cvt_pk_bf16_f32 v47, v86, v87\n"
"v_mfma_f32_16x16x32_bf16 v[108:111], v[252:255], v[216:219], v[108:111]\n"
"v_mul_f32 v88, v88, v129\n"
"v_mul_f32 v89, v89, v129\n"
"v_mfma_f32_16x16x16_bf16 v[112:115], v[52:53], v[34:35], v[112:115]\n"
"v_permlane16_swap_b32 v44, v46\n"
"v_permlane16_swap_b32 v45, v47\n"
"v_mfma_f32_16x16x16_bf16 v[116:119], v[54:55], v[34:35], v[116:119]\n"
"s_andn2_b64 exec, -1, s[44:45]\n"
"buffer_store_dwordx4 v[44:47], v22, s[4:7], s49 offen offset:256 sc1\n"
"s_mov_b64 exec, s[44:45]\n"
"s_cbranch_execz .Lk_nor_13\n"
"buffer_store_dwordx2 v[44:45], v23, s[4:7], s49 offen offset:128 sc1\n"
"buffer_store_dwordx2 v[46:47], v23, s[4:7], s51 offen offset:128 sc1\n"
".Lk_nor_13:\n"
"s_mov_b64 exec, -1\n"
"v_mul_f32 v90, v90, v129\n"
"v_mul_f32 v91, v91, v129\n"
"v_mfma_f32_16x16x16_bf16 v[120:123], v[56:57], v[34:35], v[120:123]\n"
"v_mul_f32 v92, v92, v129\n"
"v_mul_f32 v93, v93, v129\n"
"v_mfma_f32_16x16x16_bf16 v[124:127], v[58:59], v[34:35], v[124:127]\n"
"v_mul_f32 v94, v94, v129\n"
"v_mul_f32 v95, v95, v129\n"
"v_mfma_f32_16x16x32_bf16 v[112:115], v[224:227], v[204:207], v[112:115]\n"
"v_cvt_pk_bf16_f32 v48, v88, v89\n"
"v_cvt_pk_bf16_f32 v49, v90, v91\n"
"v_mfma_f32_16x16x32_bf16 v[116:119], v[228:231], v[204:207], v[116:119]\n"
"v_cvt_pk_bf16_f32 v50, v92, v93\n"
"v_cvt_pk_bf16_f32 v51, v94, v95\n"
"v_mfma_f32_16x16x32_bf16 v[120:123], v[232:235], v[204:207], v[120:123]\n"
"v_permlane16_swap_b32 v48, v50\n"
"v_permlane16_swap_b32 v49, v51\n"
"v_mfma_f32_16x16x32_bf16 v[124:127], v[236:239], v[204:207], v[124:127]\n"
"s_andn2_b64 exec, -1, s[46:47]\n"
"buffer_store_dwordx4 v[48:51], v24, s[4:7], s49 offen offset:256 sc1\n"
"s_mov_b64 exec, s[46:47]\n"
"s_cbranch_execz .Lk_nor_14\n"
"buffer_store_dwordx2 v[48:49], v25, s[4:7], s49 offen offset:128 sc1\n"
"buffer_store_dwordx2 v[50:51], v25, s[4:7], s51 offen offset:128 sc1\n"
".Lk_nor_14:\n"
"s_mov_b64 exec, -1\n"
"s_waitcnt lgkmcnt(1)\n"
"v_mul_f32 v96, v96, v130\n"
"v_mul_f32 v97, v97, v130\n"
"v_mfma_f32_16x16x32_bf16 v[112:115], v[240:243], v[220:223], v[112:115]\n"
"v_mul_f32 v98, v98, v130\n"
"v_mul_f32 v99, v99, v130\n"
"v_mfma_f32_16x16x32_bf16 v[116:119], v[244:247], v[220:223], v[116:119]\n"
"v_mul_f32 v100, v100, v130\n"
"v_mul_f32 v101, v101, v130\n"
"v_mfma_f32_16x16x32_bf16 v[120:123], v[248:251], v[220:223], v[120:123]\n"
"v_mul_f32 v102, v102, v130\n"
"v_mul_f32 v103, v103, v130\n"
"v_mfma_f32_16x16x32_bf16 v[124:127], v[252:255], v[220:223], v[124:127]\n"
"v_cvt_pk_bf16_f32 v36, v96, v97\n"
"v_cvt_pk_bf16_f32 v37, v98, v99\n"
"v_cvt_pk_bf16_f32 v38, v100, v101\n"
"v_cvt_pk_bf16_f32 v39, v102, v103\n"
"v_mul_f32 v104, v104, v130\n"
"v_mul_f32 v105, v105, v130\n"
"v_permlane16_swap_b32 v36, v38\n"
"v_permlane16_swap_b32 v37, v39\n"
"s_andn2_b64 exec, -1, s[44:45]\n"
"buffer_store_dwordx4 v[36:39], v22, s[4:7], s49 offen offset:512 sc1\n"
"s_mov_b64 exec, s[44:45]\n"
"s_cbranch_execz .Lk_nor_15\n"
"buffer_store_dwordx2 v[36:37], v23, s[4:7], s49 offen offset:256 sc1\n"
"buffer_store_dwordx2 v[38:39], v23, s[4:7], s51 offen offset:256 sc1\n"
".Lk_nor_15:\n"
"s_mov_b64 exec, -1\n"
"v_mul_f32 v106, v106, v130\n"
"v_mul_f32 v107, v107, v130\n"
"v_mul_f32 v108, v108, v130\n"
"v_mul_f32 v109, v109, v130\n"
"v_mul_f32 v110, v110, v130\n"
"v_mul_f32 v111, v111, v130\n"
"v_cvt_pk_bf16_f32 v40, v104, v105\n"
"v_cvt_pk_bf16_f32 v41, v106, v107\n"
"v_cvt_pk_bf16_f32 v42, v108, v109\n"
"v_cvt_pk_bf16_f32 v43, v110, v111\n"
"s_nop 0\n"
"v_permlane16_swap_b32 v40, v42\n"
"v_permlane16_swap_b32 v41, v43\n"
"s_andn2_b64 exec, -1, s[46:47]\n"
"buffer_store_dwordx4 v[40:43], v24, s[4:7], s49 offen offset:512 sc1\n"
"s_mov_b64 exec, s[46:47]\n"
"s_cbranch_execz .Lk_nor_16\n"
"buffer_store_dwordx2 v[40:41], v25, s[4:7], s49 offen offset:256 sc1\n"
"buffer_store_dwordx2 v[42:43], v25, s[4:7], s51 offen offset:256 sc1\n"
".Lk_nor_16:\n"
"s_mov_b64 exec, -1\n"
"s_waitcnt lgkmcnt(0)\n"
"v_mul_f32 v112, v112, v131\n"
"v_mul_f32 v113, v113, v131\n"
"v_mul_f32 v114, v114, v131\n"
"v_mul_f32 v115, v115, v131\n"
"v_mul_f32 v116, v116, v131\n"
"v_mul_f32 v117, v117, v131\n"
"v_mul_f32 v118, v118, v131\n"
"v_mul_f32 v119, v119, v131\n"
"v_cvt_pk_bf16_f32 v44, v112, v113\n"
"v_cvt_pk_bf16_f32 v45, v114, v115\n"
"v_cvt_pk_bf16_f32 v46, v116, v117\n"
"v_cvt_pk_bf16_f32 v47, v118, v119\n"
"v_mul_f32 v120, v120, v131\n"
"v_mul_f32 v121, v121, v131\n"
"v_permlane16_swap_b32 v44, v46\n"
"v_permlane16_swap_b32 v45, v47\n"
"s_andn2_b64 exec, -1, s[44:45]\n"
"buffer_store_dwordx4 v[44:47], v22, s[4:7], s49 offen offset:768 sc1\n"
"s_mov_b64 exec, s[44:45]\n"
"s_cbranch_execz .Lk_nor_17\n"
"buffer_store_dwordx2 v[44:45], v23, s[4:7], s49 offen offset:384 sc1\n"
"buffer_store_dwordx2 v[46:47], v23, s[4:7], s51 offen offset:384 sc1\n"
".Lk_nor_17:\n"
"s_mov_b64 exec, -1\n"
"v_mul_f32 v122, v122, v131\n"
"v_mul_f32 v123, v123, v131\n"
"v_mul_f32 v124, v124, v131\n"
"v_mul_f32 v125, v125, v131\n"
"v_mul_f32 v126, v126, v131\n"
"v_mul_f32 v127, v127, v131\n"
"v_cvt_pk_bf16_f32 v48, v120, v121\n"
"v_cvt_pk_bf16_f32 v49, v122, v123\n"
"v_cvt_pk_bf16_f32 v50, v124, v125\n"
"v_cvt_pk_bf16_f32 v51, v126, v127\n"
"s_nop 0\n"
"v_permlane16_swap_b32 v48, v50\n"
"v_permlane16_swap_b32 v49, v51\n"
"s_andn2_b64 exec, -1, s[46:47]\n"
"buffer_store_dwordx4 v[48:51], v24, s[4:7], s49 offen offset:768 sc1\n"
"s_mov_b64 exec, s[46:47]\n"
"s_cbranch_execz .Lk_nor_18\n"
"buffer_store_dwordx2 v[48:49], v25, s[4:7], s49 offen offset:384 sc1\n"
"buffer_store_dwordx2 v[50:51], v25, s[4:7], s51 offen offset:384 sc1\n"
".Lk_nor_18:\n"
"s_mov_b64 exec, -1\n"
"v_lshlrev_b32 v14, 4, v0\n"
"s_waitcnt vmcnt(0)\n"
"s_barrier\n"
"s_setprio 0\n"
"s_cmp_lg_u32 s3, 0\n"
"s_cbranch_scc1 .Lk_p1np_19\n"
"s_mov_b64 exec, 0xff\n"
"buffer_store_dwordx4 v[60:63], v14, s[4:7], s12 offen sc1\n"
"s_mov_b64 exec, -1\n"
".Lk_p1np_19:\n"
"s_branch .Lk_cdone_3\n"
".Lk_vmode_2:\n"
"v_and_b32 v8, 15, v0\n"
"v_lshrrev_b32 v9, 4, v0\n"
"s_lshr_b32 s56, s3, 1\n"
"s_lshl_b32 s57, s56, 6\n"
"v_add_u32 v2, s57, v8\n"
"v_lshlrev_b32 v3, 7, v2\n"
"v_lshrrev_b32 v4, 1, v8\n"
"v_xor_b32 v5, v9, v4\n"
"v_lshlrev_b32 v2, 4, v5\n"
"v_add_u32 v10, v3, v2\n"
"v_xor_b32 v11, 64, v10\n"
"v_lshlrev_b32 v2, 3, v9\n"
"v_lshrrev_b32 v3, 2, v8\n"
"v_add_u32 v4, v2, v3\n"
"v_lshlrev_b32 v5, 8, v4\n"
"v_add_u32 v2, 0x4000, v5\n"
"s_and_b32 s56, s3, 1\n"
"s_lshl_b32 s57, s56, 2\n"
"v_lshrrev_b32 v3, 2, v8\n"
"v_lshlrev_b32 v4, 1, v3\n"
"v_xor_b32 v5, s57, v4\n"
"v_and_b32 v3, 1, v9\n"
"v_xor_b32 v4, v5, v3\n"
"v_lshlrev_b32 v5, 5, v4\n"
"v_add_u32 v3, v2, v5\n"
"v_and_b32 v4, 3, v0\n"
"v_lshlrev_b32 v2, 3, v4\n"
"v_add_u32 v14, v3, v2\n"
"v_xor_b32 v15, 0x20, v14\n"
"v_xor_b32 v16, 0x40, v14\n"
"v_xor_b32 v17, 0x60, v14\n"
"v_add_u32 v12, 0x10000, v10\n"
"v_add_u32 v13, 0x10000, v11\n"
"v_add_u32 v18, 0x10000, v14\n"
"v_add_u32 v19, 0x10000, v15\n"
"v_add_u32 v20, 0x10000, v16\n"
"v_add_u32 v21, 0x10000, v17\n"
"v_and_b32 v2, 1, v9\n"
"v_lshlrev_b32 v3, 1, v2\n"
"v_lshrrev_b32 v4, 1, v9\n"
"v_add_u32 v54, v3, v4\n"
"s_mul_i32 s56, s40, 0x2800\n"
"s_add_u32 s57, 0x591000, s56\n"
"s_add_u32 s49, s57, 0x1400\n"
"s_lshr_b32 s56, s3, 1\n"
"s_lshl_b32 s57, s56, 3\n"
"v_add_u32 v2, s57, v54\n"
"v_mul_u32_u24 v55, 0x140, v2\n"
"s_sub_u32 s56, s41, 10\n"
"s_lshl_b32 s57, s56, 7\n"
"s_and_b32 s58, s3, 1\n"
"s_lshl_b32 s59, s58, 6\n"
"s_add_u32 s60, s57, s59\n"
"v_add_u32 v52, s60, v8\n"
"v_mul_u32_u24 v2, 0x3334, v52\n"
"v_lshrrev_b32 v53, 18, v2\n"
"v_mul_u32_u24 v2, 0x28000, v53\n"
"v_mul_u32_u24 v3, 20, v53\n"
"v_sub_u32 v4, v52, v3\n"
"v_lshlrev_b32 v5, 4, v4\n"
"v_add_u32 v3, v2, v5\n"
"v_add_u32 v22, v3, v55\n"
"s_sub_u32 s56, s41, 10\n"
"s_lshl_b32 s57, s56, 7\n"
"s_and_b32 s58, s3, 1\n"
"s_lshl_b32 s59, s58, 6\n"
"s_add_u32 s60, s57, s59\n"
"s_add_u32 s61, s60, 16\n"
"v_add_u32 v52, s61, v8\n"
"v_mul_u32_u24 v2, 0x3334, v52\n"
"v_lshrrev_b32 v53, 18, v2\n"
"v_mul_u32_u24 v2, 0x28000, v53\n"
"v_mul_u32_u24 v3, 20, v53\n"
"v_sub_u32 v4, v52, v3\n"
"v_lshlrev_b32 v5, 4, v4\n"
"v_add_u32 v3, v2, v5\n"
"v_add_u32 v23, v3, v55\n"
"s_sub_u32 s56, s41, 10\n"
"s_lshl_b32 s57, s56, 7\n"
"s_and_b32 s58, s3, 1\n"
"s_lshl_b32 s59, s58, 6\n"
"s_add_u32 s60, s57, s59\n"
"s_add_u32 s61, s60, 32\n"
"v_add_u32 v52, s61, v8\n"
"v_mul_u32_u24 v2, 0x3334, v52\n"
"v_lshrrev_b32 v53, 18, v2\n"
"v_mul_u32_u24 v2, 0x28000, v53\n"
"v_mul_u32_u24 v3, 20, v53\n"
"v_sub_u32 v4, v52, v3\n"
"v_lshlrev_b32 v5, 4, v4\n"
"v_add_u32 v3, v2, v5\n"
"v_add_u32 v24, v3, v55\n"
"s_sub_u32 s56, s41, 10\n"
"s_lshl_b32 s57, s56, 7\n"
"s_and_b32 s58, s3, 1\n"
"s_lshl_b32 s59, s58, 6\n"
"s_add_u32 s60, s57, s59\n"
"s_add_u32 s61, s60, 48\n"
"v_add_u32 v52, s61, v8\n"
"v_mul_u32_u24 v2, 0x3334, v52\n"
"v_lshrrev_b32 v53, 18, v2\n"
"v_mul_u32_u24 v2, 0x28000, v53\n"
"v_mul_u32_u24 v3, 20, v53\n"
"v_sub_u32 v4, v52, v3\n"
"v_lshlrev_b32 v5, 4, v4\n"
"v_add_u32 v3, v2, v5\n"
"v_add_u32 v25, v3, v55\n"
"s_lshr_b32 s56, s3, 1\n"
"s_lshl_b32 s57, s56, 6\n"
"v_lshlrev_b32 v2, 2, v9\n"
"v_add_u32 v3, s57, v2\n"
"v_lshlrev_b32 v4, 2, v3\n"
"v_add_u32 v26, 0x20800, v4\n"
"s_lshr_b32 s56, s3, 1\n"
"s_lshl_b32 s57, s56, 6\n"
"v_add_u32 v2, s57, v8\n"
"v_lshlrev_b32 v3, 3, v2\n"
"v_add_u32 v1, 0x20400, v3\n"
"s_and_b32 s56, s3, 1\n"
"s_lshl_b32 s57, s56, 6\n"
"v_add_u32 v2, s57, v8\n"
"v_lshlrev_b32 v3, 3, v2\n"
"v_add_u32 v9, 0x20000, v3\n"
"v_mov_b32 v28, 0\n"
"v_mov_b32 v29, 0\n"
"v_mov_b32 v30, 0\n"
"v_mov_b32 v31, 0\n"
"v_mov_b32 v32, 0\n"
"v_mov_b32 v33, 0\n"
"v_mov_b32 v34, 0\n"
"v_mov_b32 v35, 0\n"
"s_waitcnt vmcnt(1)\n"
"v_readfirstlane_b32 s0, v6\n"
"v_readfirstlane_b32 s1, v7\n"
"s_mul_hi_u32 s33, s0, 0x4c957f2d\n"
"s_mul_i32 s32, s0, 0x4c957f2d\n"
"s_mul_i32 s0, s0, 0x5851f42d\n"
"s_mul_i32 s1, s1, 0x4c957f2d\n"
"s_add_u32 s33, s33, s0\n"
"s_add_u32 s33, s33, s1\n"
"s_add_u32 s32, s32, 0xf767814f\n"
"s_add_u32 s33, s33, 0x14057b7e\n"
"s_lshl_b32 s56, s41, 4\n"
"s_add_u32 s57, s56, s40\n"
"s_lshl_b32 s58, s57, 10\n"
"s_add_u32 s12, 0x1000, s58\n"
"v_mov_b32 v60, s32\n"
"v_mov_b32 v61, s33\n"
"s_barrier\n"
"ds_read_b128 v[128:131], v10 offset:0\n"
"ds_read_b64_tr_b16 v[160:161], v14 offset:0\n"
"ds_read_b64_tr_b16 v[162:163], v14 offset:1024\n"
"ds_read_b128 v[132:135], v10 offset:2048\n"
"ds_read_b64_tr_b16 v[164:165], v15 offset:0\n"
"ds_read_b64_tr_b16 v[166:167], v15 offset:1024\n"
"ds_read_b128 v[136:139], v10 offset:4096\n"
"ds_read_b64_tr_b16 v[168:169], v16 offset:0\n"
"ds_read_b64_tr_b16 v[170:171], v16 offset:1024\n"
"ds_read_b128 v[140:143], v10 offset:6144\n"
"ds_read_b64_tr_b16 v[172:173], v17 offset:0\n"
"ds_read_b64_tr_b16 v[174:175], v17 offset:1024\n"
"ds_read_b128 v[144:147], v11 offset:0\n"
"ds_read_b64_tr_b16 v[176:177], v14 offset:8192\n"
"ds_read_b64_tr_b16 v[178:179], v14 offset:9216\n"
"ds_read_b128 v[148:151], v11 offset:2048\n"
"ds_read_b64_tr_b16 v[180:181], v15 offset:8192\n"
"ds_read_b64_tr_b16 v[182:183], v15 offset:9216\n"
"ds_read_b128 v[152:155], v11 offset:4096\n"
"ds_read_b64_tr_b16 v[184:185], v16 offset:8192\n"
"ds_read_b64_tr_b16 v[186:187], v16 offset:9216\n"
"ds_read_b128 v[156:159], v11 offset:6144\n"
"ds_read_b64_tr_b16 v[188:189], v17 offset:8192\n"
"ds_read_b64_tr_b16 v[190:191], v17 offset:9216\n"
"s_waitcnt lgkmcnt(12)\n"
"v_mfma_f32_16x16x32_bf16 v[64:67], v[128:131], v[160:163], 0\n"
"v_mfma_f32_16x16x32_bf16 v[68:71], v[128:131], v[164:167], 0\n"
"v_mfma_f32_16x16x32_bf16 v[72:75], v[128:131], v[168:171], 0\n"
"v_mfma_f32_16x16x32_bf16 v[76:79], v[128:131], v[172:175], 0\n"
"v_mfma_f32_16x16x32_bf16 v[92:95], v[132:135], v[172:175], 0\n"
"v_mfma_f32_16x16x32_bf16 v[88:91], v[132:135], v[168:171], 0\n"
"v_mfma_f32_16x16x32_bf16 v[84:87], v[132:135], v[164:167], 0\n"
"v_mfma_f32_16x16x32_bf16 v[80:83], v[132:135], v[160:163], 0\n"
"v_mfma_f32_16x16x32_bf16 v[96:99], v[136:139], v[160:163], 0\n"
"v_mfma_f32_16x16x32_bf16 v[100:103], v[136:139], v[164:167], 0\n"
"v_mfma_f32_16x16x32_bf16 v[104:107], v[136:139], v[168:171], 0\n"
"v_mfma_f32_16x16x32_bf16 v[108:111], v[136:139], v[172:175], 0\n"
"v_mfma_f32_16x16x32_bf16 v[124:127], v[140:143], v[172:175], 0\n"
"v_mfma_f32_16x16x32_bf16 v[120:123], v[140:143], v[168:171], 0\n"
"v_mfma_f32_16x16x32_bf16 v[116:119], v[140:143], v[164:167], 0\n"
"v_mfma_f32_16x16x32_bf16 v[112:115], v[140:143], v[160:163], 0\n"
"s_waitcnt lgkmcnt(0)\n"
"s_barrier\n"
"s_sleep 1\n"
"v_mfma_f32_16x16x32_bf16 v[64:67], v[144:147], v[176:179], v[64:67]\n"
"ds_read_b128 v[192:195], v10 offset:32768\n"
"ds_read_b64_tr_b16 v[224:225], v14 offset:32768\n"
"v_mfma_f32_16x16x32_bf16 v[68:71], v[144:147], v[180:183], v[68:71]\n"
"ds_read_b64_tr_b16 v[226:227], v14 offset:33792\n"
"v_mfma_f32_16x16x32_bf16 v[72:75], v[144:147], v[184:187], v[72:75]\n"
"ds_read_b128 v[196:199], v10 offset:34816\n"
"ds_read_b64_tr_b16 v[228:229], v15 offset:32768\n"
"v_mfma_f32_16x16x32_bf16 v[76:79], v[144:147], v[188:191], v[76:79]\n"
"ds_read_b64_tr_b16 v[230:231], v15 offset:33792\n"
"v_mfma_f32_16x16x32_bf16 v[92:95], v[148:151], v[188:191], v[92:95]\n"
"ds_read_b128 v[200:203], v10 offset:36864\n"
"ds_read_b64_tr_b16 v[232:233], v16 offset:32768\n"
"v_mfma_f32_16x16x32_bf16 v[88:91], v[148:151], v[184:187], v[88:91]\n"
"ds_read_b64_tr_b16 v[234:235], v16 offset:33792\n"
"v_mfma_f32_16x16x32_bf16 v[84:87], v[148:151], v[180:183], v[84:87]\n"
"ds_read_b128 v[204:207], v10 offset:38912\n"
"ds_read_b64_tr_b16 v[236:237], v17 offset:32768\n"
"v_mfma_f32_16x16x32_bf16 v[80:83], v[148:151], v[176:179], v[80:83]\n"
"ds_read_b64_tr_b16 v[238:239], v17 offset:33792\n"
"v_mfma_f32_16x16x32_bf16 v[96:99], v[152:155], v[176:179], v[96:99]\n"
"ds_read_b128 v[208:211], v11 offset:32768\n"
"ds_read_b64_tr_b16 v[240:241], v14 offset:40960\n"
"v_mfma_f32_16x16x32_bf16 v[100:103], v[152:155], v[180:183], v[100:103]\n"
"ds_read_b64_tr_b16 v[242:243], v14 offset:41984\n"
"v_mfma_f32_16x16x32_bf16 v[104:107], v[152:155], v[184:187], v[104:107]\n"
"ds_read_b128 v[212:215], v11 offset:34816\n"
"ds_read_b64_tr_b16 v[244:245], v15 offset:40960\n"
"v_mfma_f32_16x16x32_bf16 v[108:111], v[152:155], v[188:191], v[108:111]\n"
"ds_read_b64_tr_b16 v[246:247], v15 offset:41984\n"
"v_mfma_f32_16x16x32_bf16 v[124:127], v[156:159], v[188:191], v[124:127]\n"
"ds_read_b128 v[216:219], v11 offset:36864\n"
"ds_read_b64_tr_b16 v[248:249], v16 offset:40960\n"
"v_mfma_f32_16x16x32_bf16 v[120:123], v[156:159], v[184:187], v[120:123]\n"
"ds_read_b64_tr_b16 v[250:251], v16 offset:41984\n"
"v_mfma_f32_16x16x32_bf16 v[116:119], v[156:159], v[180:183], v[116:119]\n"
"ds_read_b128 v[220:223], v11 offset:38912\n"
"ds_read_b64_tr_b16 v[252:253], v17 offset:40960\n"
"v_mfma_f32_16x16x32_bf16 v[112:115], v[156:159], v[176:179], v[112:115]\n"
"ds_read_b64_tr_b16 v[254:255], v17 offset:41984\n"
"s_waitcnt lgkmcnt(12)\n"
"v_mfma_f32_16x16x32_bf16 v[64:67], v[192:195], v[224:227], v[64:67]\n"
"v_mfma_f32_16x16x32_bf16 v[68:71], v[192:195], v[228:231], v[68:71]\n"
"v_mfma_f32_16x16x32_bf16 v[72:75], v[192:195], v[232:235], v[72:75]\n"
"v_mfma_f32_16x16x32_bf16 v[76:79], v[192:195], v[236:239], v[76:79]\n"
"v_mfma_f32_16x16x32_bf16 v[92:95], v[196:199], v[236:239], v[92:95]\n"
"v_mfma_f32_16x16x32_bf16 v[88:91], v[196:199], v[232:235], v[88:91]\n"
"v_mfma_f32_16x16x32_bf16 v[84:87], v[196:199], v[228:231], v[84:87]\n"
"v_mfma_f32_16x16x32_bf16 v[80:83], v[196:199], v[224:227], v[80:83]\n"
"v_mfma_f32_16x16x32_bf16 v[96:99], v[200:203], v[224:227], v[96:99]\n"
"v_mfma_f32_16x16x32_bf16 v[100:103], v[200:203], v[228:231], v[100:103]\n"
"v_mfma_f32_16x16x32_bf16 v[104:107], v[200:203], v[232:235], v[104:107]\n"
"v_mfma_f32_16x16x32_bf16 v[108:111], v[200:203], v[236:239], v[108:111]\n"
"v_mfma_f32_16x16x32_bf16 v[124:127], v[204:207], v[236:239], v[124:127]\n"
"v_mfma_f32_16x16x32_bf16 v[120:123], v[204:207], v[232:235], v[120:123]\n"
"v_mfma_f32_16x16x32_bf16 v[116:119], v[204:207], v[228:231], v[116:119]\n"
"v_mfma_f32_16x16x32_bf16 v[112:115], v[204:207], v[224:227], v[112:115]\n"
"s_waitcnt lgkmcnt(0)\n"
"s_barrier\n"
"s_sleep 1\n"
"v_mfma_f32_16x16x32_bf16 v[64:67], v[208:211], v[240:243], v[64:67]\n"
"ds_read_b128 v[128:131], v12 offset:0\n"
"ds_read_b64_tr_b16 v[160:161], v18 offset:0\n"
"v_mfma_f32_16x16x32_bf16 v[68:71], v[208:211], v[244:247], v[68:71]\n"
"ds_read_b64_tr_b16 v[162:163], v18 offset:1024\n"
"v_mfma_f32_16x16x32_bf16 v[72:75], v[208:211], v[248:251], v[72:75]\n"
"ds_read_b128 v[132:135], v12 offset:2048\n"
"ds_read_b64_tr_b16 v[164:165], v19 offset:0\n"
"v_mfma_f32_16x16x32_bf16 v[76:79], v[208:211], v[252:255], v[76:79]\n"
"ds_read_b64_tr_b16 v[166:167], v19 offset:1024\n"
"v_mfma_f32_16x16x32_bf16 v[92:95], v[212:215], v[252:255], v[92:95]\n"
"ds_read_b128 v[136:139], v12 offset:4096\n"
"ds_read_b64_tr_b16 v[168:169], v20 offset:0\n"
"v_mfma_f32_16x16x32_bf16 v[88:91], v[212:215], v[248:251], v[88:91]\n"
"ds_read_b64_tr_b16 v[170:171], v20 offset:1024\n"
"v_mfma_f32_16x16x32_bf16 v[84:87], v[212:215], v[244:247], v[84:87]\n"
"ds_read_b128 v[140:143], v12 offset:6144\n"
"ds_read_b64_tr_b16 v[172:173], v21 offset:0\n"
"v_mfma_f32_16x16x32_bf16 v[80:83], v[212:215], v[240:243], v[80:83]\n"
"ds_read_b64_tr_b16 v[174:175], v21 offset:1024\n"
"v_mfma_f32_16x16x32_bf16 v[96:99], v[216:219], v[240:243], v[96:99]\n"
"ds_read_b128 v[144:147], v13 offset:0\n"
"ds_read_b64_tr_b16 v[176:177], v18 offset:8192\n"
"v_mfma_f32_16x16x32_bf16 v[100:103], v[216:219], v[244:247], v[100:103]\n"
"ds_read_b64_tr_b16 v[178:179], v18 offset:9216\n"
"v_mfma_f32_16x16x32_bf16 v[104:107], v[216:219], v[248:251], v[104:107]\n"
"ds_read_b128 v[148:151], v13 offset:2048\n"
"ds_read_b64_tr_b16 v[180:181], v19 offset:8192\n"
"v_mfma_f32_16x16x32_bf16 v[108:111], v[216:219], v[252:255], v[108:111]\n"
"ds_read_b64_tr_b16 v[182:183], v19 offset:9216\n"
"v_mfma_f32_16x16x32_bf16 v[124:127], v[220:223], v[252:255], v[124:127]\n"
"ds_read_b128 v[152:155], v13 offset:4096\n"
"ds_read_b64_tr_b16 v[184:185], v20 offset:8192\n"
"v_mfma_f32_16x16x32_bf16 v[120:123], v[220:223], v[248:251], v[120:123]\n"
"ds_read_b64_tr_b16 v[186:187], v20 offset:9216\n"
"v_mfma_f32_16x16x32_bf16 v[116:119], v[220:223], v[244:247], v[116:119]\n"
"ds_read_b128 v[156:159], v13 offset:6144\n"
"ds_read_b64_tr_b16 v[188:189], v21 offset:8192\n"
"v_mfma_f32_16x16x32_bf16 v[112:115], v[220:223], v[240:243], v[112:115]\n"
"ds_read_b64_tr_b16 v[190:191], v21 offset:9216\n"
"s_waitcnt lgkmcnt(12)\n"
"v_mfma_f32_16x16x32_bf16 v[64:67], v[128:131], v[160:163], v[64:67]\n"
"v_mfma_f32_16x16x32_bf16 v[68:71], v[128:131], v[164:167], v[68:71]\n"
"s_cbranch_execz .Lk_tramp\n"
"s_nop 0\n"
"v_mfma_f32_16x16x32_bf16 v[72:75], v[128:131], v[168:171], v[72:75]\n"
"v_mfma_f32_16x16x32_bf16 v[76:79], v[128:131], v[172:175], v[76:79]\n"
"v_mfma_f32_16x16x32_bf16 v[92:95], v[132:135], v[172:175], v[92:95]\n"
"v_mfma_f32_16x16x32_bf16 v[88:91], v[132:135], v[168:171], v[88:91]\n"
"v_mfma_f32_16x16x32_bf16 v[84:87], v[132:135], v[164:167], v[84:87]\n"
"v_mfma_f32_16x16x32_bf16 v[80:83], v[132:135], v[160:163], v[80:83]\n"
"v_mfma_f32_16x16x32_bf16 v[96:99], v[136:139], v[160:163], v[96:99]\n"
"v_mfma_f32_16x16x32_bf16 v[100:103], v[136:139], v[164:167], v[100:103]\n"
"v_mfma_f32_16x16x32_bf16 v[104:107], v[136:139], v[168:171], v[104:107]\n"
"v_mfma_f32_16x16x32_bf16 v[108:111], v[136:139], v[172:175], v[108:111]\n"
"v_mfma_f32_16x16x32_bf16 v[124:127], v[140:143], v[172:175], v[124:127]\n"
"v_mfma_f32_16x16x32_bf16 v[120:123], v[140:143], v[168:171], v[120:123]\n"
"v_mfma_f32_16x16x32_bf16 v[116:119], v[140:143], v[164:167], v[116:119]\n"
"v_mfma_f32_16x16x32_bf16 v[112:115], v[140:143], v[160:163], v[112:115]\n"
"s_waitcnt lgkmcnt(0)\n"
"s_barrier\n"
"s_sleep 1\n"
"v_mfma_f32_16x16x32_bf16 v[64:67], v[144:147], v[176:179], v[64:67]\n"
"ds_read_b128 v[192:195], v12 offset:32768\n"
"ds_read_b64_tr_b16 v[224:225], v18 offset:32768\n"
"v_mfma_f32_16x16x32_bf16 v[68:71], v[144:147], v[180:183], v[68:71]\n"
"ds_read_b64_tr_b16 v[226:227], v18 offset:33792\n"
"v_mfma_f32_16x16x32_bf16 v[72:75], v[144:147], v[184:187], v[72:75]\n"
"ds_read_b128 v[196:199], v12 offset:34816\n"
"ds_read_b64_tr_b16 v[228:229], v19 offset:32768\n"
"v_mfma_f32_16x16x32_bf16 v[76:79], v[144:147], v[188:191], v[76:79]\n"
"ds_read_b64_tr_b16 v[230:231], v19 offset:33792\n"
"v_mfma_f32_16x16x32_bf16 v[92:95], v[148:151], v[188:191], v[92:95]\n"
"ds_read_b128 v[200:203], v12 offset:36864\n"
"ds_read_b64_tr_b16 v[232:233], v20 offset:32768\n"
"v_mfma_f32_16x16x32_bf16 v[88:91], v[148:151], v[184:187], v[88:91]\n"
"ds_read_b64_tr_b16 v[234:235], v20 offset:33792\n"
"v_mfma_f32_16x16x32_bf16 v[84:87], v[148:151], v[180:183], v[84:87]\n"
"ds_read_b128 v[204:207], v12 offset:38912\n"
"ds_read_b64_tr_b16 v[236:237], v21 offset:32768\n"
"v_mfma_f32_16x16x32_bf16 v[80:83], v[148:151], v[176:179], v[80:83]\n"
"ds_read_b64_tr_b16 v[238:239], v21 offset:33792\n"
"v_mfma_f32_16x16x32_bf16 v[96:99], v[152:155], v[176:179], v[96:99]\n"
"ds_read_b128 v[208:211], v13 offset:32768\n"
"ds_read_b64_tr_b16 v[240:241], v18 offset:40960\n"
"v_mfma_f32_16x16x32_bf16 v[100:103], v[152:155], v[180:183], v[100:103]\n"
"ds_read_b64_tr_b16 v[242:243], v18 offset:41984\n"
"v_mfma_f32_16x16x32_bf16 v[104:107], v[152:155], v[184:187], v[104:107]\n"
"ds_read_b128 v[212:215], v13 offset:34816\n"
"ds_read_b64_tr_b16 v[244:245], v19 offset:40960\n"
"v_mfma_f32_16x16x32_bf16 v[108:111], v[152:155], v[188:191], v[108:111]\n"
"ds_read_b64_tr_b16 v[246:247], v19 offset:41984\n"
"v_mfma_f32_16x16x32_bf16 v[124:127], v[156:159], v[188:191], v[124:127]\n"
"ds_read_b128 v[216:219], v13 offset:36864\n"
"ds_read_b64_tr_b16 v[248:249], v20 offset:40960\n"
"v_mfma_f32_16x16x32_bf16 v[120:123], v[156:159], v[184:187], v[120:123]\n"
"ds_read_b64_tr_b16 v[250:251], v20 offset:41984\n"
"v_mfma_f32_16x16x32_bf16 v[116:119], v[156:159], v[180:183], v[116:119]\n"
"ds_read_b128 v[220:223], v13 offset:38912\n"
"ds_read_b64_tr_b16 v[252:253], v21 offset:40960\n"
"v_mfma_f32_16x16x32_bf16 v[112:115], v[156:159], v[176:179], v[112:115]\n"
"ds_read_b64_tr_b16 v[254:255], v21 offset:41984\n"
"s_waitcnt lgkmcnt(12)\n"
"v_mfma_f32_16x16x32_bf16 v[64:67], v[192:195], v[224:227], v[64:67]\n"
"v_mfma_f32_16x16x32_bf16 v[68:71], v[192:195], v[228:231], v[68:71]\n"
"v_mfma_f32_16x16x32_bf16 v[72:75], v[192:195], v[232:235], v[72:75]\n"
"v_mfma_f32_16x16x32_bf16 v[76:79], v[192:195], v[236:239], v[76:79]\n"
"v_mfma_f32_16x16x32_bf16 v[92:95], v[196:199], v[236:239], v[92:95]\n"
"v_mfma_f32_16x16x32_bf16 v[88:91], v[196:199], v[232:235], v[88:91]\n"
"v_mfma_f32_16x16x32_bf16 v[84:87], v[196:199], v[228:231], v[84:87]\n"
"v_mfma_f32_16x16x32_bf16 v[80:83], v[196:199], v[224:227], v[80:83]\n"
"v_mfma_f32_16x16x32_bf16 v[96:99], v[200:203], v[224:227], v[96:99]\n"
"v_mfma_f32_16x16x32_bf16 v[100:103], v[200:203], v[228:231], v[100:103]\n"
"v_mfma_f32_16x16x32_bf16 v[104:107], v[200:203], v[232:235], v[104:107]\n"
"v_mfma_f32_16x16x32_bf16 v[108:111], v[200:203], v[236:239], v[108:111]\n"
"v_mfma_f32_16x16x32_bf16 v[124:127], v[204:207], v[236:239], v[124:127]\n"
"v_mfma_f32_16x16x32_bf16 v[120:123], v[204:207], v[232:235], v[120:123]\n"
"v_mfma_f32_16x16x32_bf16 v[116:119], v[204:207], v[228:231], v[116:119]\n"
"v_mfma_f32_16x16x32_bf16 v[112:115], v[204:207], v[224:227], v[112:115]\n"
"s_waitcnt lgkmcnt(0)\n"
"s_barrier\n"
"s_sleep 1\n"
"v_mfma_f32_16x16x32_bf16 v[64:67], v[208:211], v[240:243], v[64:67]\n"
"ds_read_b128 v[128:131], v10 offset:0\n"
"ds_read_b64_tr_b16 v[160:161], v14 offset:0\n"
"v_mfma_f32_16x16x32_bf16 v[68:71], v[208:211], v[244:247], v[68:71]\n"
"ds_read_b64_tr_b16 v[162:163], v14 offset:1024\n"
"v_mfma_f32_16x16x32_bf16 v[72:75], v[208:211], v[248:251], v[72:75]\n"
"ds_read_b128 v[132:135], v10 offset:2048\n"
"ds_read_b64_tr_b16 v[164:165], v15 offset:0\n"
"v_mfma_f32_16x16x32_bf16 v[76:79], v[208:211], v[252:255], v[76:79]\n"
"ds_read_b64_tr_b16 v[166:167], v15 offset:1024\n"
"v_mfma_f32_16x16x32_bf16 v[92:95], v[212:215], v[252:255], v[92:95]\n"
"ds_read_b128 v[136:139], v10 offset:4096\n"
"ds_read_b64_tr_b16 v[168:169], v16 offset:0\n"
"v_mfma_f32_16x16x32_bf16 v[88:91], v[212:215], v[248:251], v[88:91]\n"
"ds_read_b64_tr_b16 v[170:171], v16 offset:1024\n"
"v_mfma_f32_16x16x32_bf16 v[84:87], v[212:215], v[244:247], v[84:87]\n"
"ds_read_b128 v[140:143], v10 offset:6144\n"
"ds_read_b64_tr_b16 v[172:173], v17 offset:0\n"
"v_mfma_f32_16x16x32_bf16 v[80:83], v[212:215], v[240:243], v[80:83]\n"
"ds_read_b64_tr_b16 v[174:175], v17 offset:1024\n"
"v_mfma_f32_16x16x32_bf16 v[96:99], v[216:219], v[240:243], v[96:99]\n"
"ds_read_b128 v[144:147], v11 offset:0\n"
"ds_read_b64_tr_b16 v[176:177], v14 offset:8192\n"
"v_mfma_f32_16x16x32_bf16 v[100:103], v[216:219], v[244:247], v[100:103]\n"
"ds_read_b64_tr_b16 v[178:179], v14 offset:9216\n"
"v_mfma_f32_16x16x32_bf16 v[104:107], v[216:219], v[248:251], v[104:107]\n"
"ds_read_b128 v[148:151], v11 offset:2048\n"
"ds_read_b64_tr_b16 v[180:181], v15 offset:8192\n"
"v_mfma_f32_16x16x32_bf16 v[108:111], v[216:219], v[252:255], v[108:111]\n"
"ds_read_b64_tr_b16 v[182:183], v15 offset:9216\n"
"v_mfma_f32_16x16x32_bf16 v[124:127], v[220:223], v[252:255], v[124:127]\n"
"ds_read_b128 v[152:155], v11 offset:4096\n"
"ds_read_b64_tr_b16 v[184:185], v16 offset:8192\n"
"v_mfma_f32_16x16x32_bf16 v[120:123], v[220:223], v[248:251], v[120:123]\n"
"ds_read_b64_tr_b16 v[186:187], v16 offset:9216\n"
"v_mfma_f32_16x16x32_bf16 v[116:119], v[220:223], v[244:247], v[116:119]\n"
"ds_read_b128 v[156:159], v11 offset:6144\n"
"ds_read_b64_tr_b16 v[188:189], v17 offset:8192\n"
"v_mfma_f32_16x16x32_bf16 v[112:115], v[220:223], v[240:243], v[112:115]\n"
"ds_read_b64_tr_b16 v[190:191], v17 offset:9216\n"
"s_waitcnt lgkmcnt(12)\n"
"v_mfma_f32_16x16x32_bf16 v[64:67], v[128:131], v[160:163], v[64:67]\n"
"v_mfma_f32_16x16x32_bf16 v[68:71], v[128:131], v[164:167], v[68:71]\n"
"v_mfma_f32_16x16x32_bf16 v[72:75], v[128:131], v[168:171], v[72:75]\n"
"v_mfma_f32_16x16x32_bf16 v[76:79], v[128:131], v[172:175], v[76:79]\n"
"v_mfma_f32_16x16x32_bf16 v[92:95], v[132:135], v[172:175], v[92:95]\n"
"v_mfma_f32_16x16x32_bf16 v[88:91], v[132:135], v[168:171], v[88:91]\n"
"v_mfma_f32_16x16x32_bf16 v[84:87], v[132:135], v[164:167], v[84:87]\n"
"v_mfma_f32_16x16x32_bf16 v[80:83], v[132:135], v[160:163], v[80:83]\n"
"v_mfma_f32_16x16x32_bf16 v[96:99], v[136:139], v[160:163], v[96:99]\n"
"v_mfma_f32_16x16x32_bf16 v[100:103], v[136:139], v[164:167], v[100:103]\n"
"v_mfma_f32_16x16x32_bf16 v[104:107], v[136:139], v[168:171], v[104:107]\n"
"v_mfma_f32_16x16x32_bf16 v[108:111], v[136:139], v[172:175], v[108:111]\n"
"v_mfma_f32_16x16x32_bf16 v[124:127], v[140:143], v[172:175], v[124:127]\n"
"v_mfma_f32_16x16x32_bf16 v[120:123], v[140:143], v[168:171], v[120:123]\n"
"v_mfma_f32_16x16x32_bf16 v[116:119], v[140:143], v[164:167], v[116:119]\n"
"v_mfma_f32_16x16x32_bf16 v[112:115], v[140:143], v[160:163], v[112:115]\n"
"s_waitcnt lgkmcnt(0)\n"
"s_barrier\n"
"s_sleep 1\n"
"v_mfma_f32_16x16x32_bf16 v[64:67], v[144:147], v[176:179], v[64:67]\n"
"ds_read_b128 v[192:195], v10 offset:32768\n"
"ds_read_b64_tr_b16 v[224:225], v14 offset:32768\n"
"v_mfma_f32_16x16x32_bf16 v[68:71], v[144:147], v[180:183], v[68:71]\n"
"ds_read_b64_tr_b16 v[226:227], v14 offset:33792\n"
"v_mfma_f32_16x16x32_bf16 v[72:75], v[144:147], v[184:187], v[72:75]\n"
"ds_read_b128 v[196:199], v10 offset:34816\n"
"ds_read_b64_tr_b16 v[228:229], v15 offset:32768\n"
"v_mfma_f32_16x16x32_bf16 v[76:79], v[144:147], v[188:191], v[76:79]\n"
"ds_read_b64_tr_b16 v[230:231], v15 offset:33792\n"
"v_mfma_f32_16x16x32_bf16 v[92:95], v[148:151], v[188:191], v[92:95]\n"
"ds_read_b128 v[200:203], v10 offset:36864\n"
"ds_read_b64_tr_b16 v[232:233], v16 offset:32768\n"
"v_mfma_f32_16x16x32_bf16 v[88:91], v[148:151], v[184:187], v[88:91]\n"
"ds_read_b64_tr_b16 v[234:235], v16 offset:33792\n"
"v_mfma_f32_16x16x32_bf16 v[84:87], v[148:151], v[180:183], v[84:87]\n"
"ds_read_b128 v[204:207], v10 offset:38912\n"
"ds_read_b64_tr_b16 v[236:237], v17 offset:32768\n"
"v_mfma_f32_16x16x32_bf16 v[80:83], v[148:151], v[176:179], v[80:83]\n"
"ds_read_b64_tr_b16 v[238:239], v17 offset:33792\n"
"v_mfma_f32_16x16x32_bf16 v[96:99], v[152:155], v[176:179], v[96:99]\n"
"ds_read_b128 v[208:211], v11 offset:32768\n"
"ds_read_b64_tr_b16 v[240:241], v14 offset:40960\n"
"v_mfma_f32_16x16x32_bf16 v[100:103], v[152:155], v[180:183], v[100:103]\n"
"ds_read_b64_tr_b16 v[242:243], v14 offset:41984\n"
"v_mfma_f32_16x16x32_bf16 v[104:107], v[152:155], v[184:187], v[104:107]\n"
"ds_read_b128 v[212:215], v11 offset:34816\n"
"ds_read_b64_tr_b16 v[244:245], v15 offset:40960\n"
"v_mfma_f32_16x16x32_bf16 v[108:111], v[152:155], v[188:191], v[108:111]\n"
"ds_read_b64_tr_b16 v[246:247], v15 offset:41984\n"
"v_mfma_f32_16x16x32_bf16 v[124:127], v[156:159], v[188:191], v[124:127]\n"
"ds_read_b128 v[216:219], v11 offset:36864\n"
"ds_read_b64_tr_b16 v[248:249], v16 offset:40960\n"
"v_mfma_f32_16x16x32_bf16 v[120:123], v[156:159], v[184:187], v[120:123]\n"
"ds_read_b64_tr_b16 v[250:251], v16 offset:41984\n"
"v_mfma_f32_16x16x32_bf16 v[116:119], v[156:159], v[180:183], v[116:119]\n"
"ds_read_b128 v[220:223], v11 offset:38912\n"
"ds_read_b64_tr_b16 v[252:253], v17 offset:40960\n"
"v_mfma_f32_16x16x32_bf16 v[112:115], v[156:159], v[176:179], v[112:115]\n"
"ds_read_b64_tr_b16 v[254:255], v17 offset:41984\n"
"s_waitcnt lgkmcnt(12)\n"
"v_mfma_f32_16x16x32_bf16 v[64:67], v[192:195], v[224:227], v[64:67]\n"
"v_mfma_f32_16x16x32_bf16 v[68:71], v[192:195], v[228:231], v[68:71]\n"
"v_mfma_f32_16x16x32_bf16 v[72:75], v[192:195], v[232:235], v[72:75]\n"
"v_mfma_f32_16x16x32_bf16 v[76:79], v[192:195], v[236:239], v[76:79]\n"
"v_mfma_f32_16x16x32_bf16 v[92:95], v[196:199], v[236:239], v[92:95]\n"
"v_mfma_f32_16x16x32_bf16 v[88:91], v[196:199], v[232:235], v[88:91]\n"
"v_mfma_f32_16x16x32_bf16 v[84:87], v[196:199], v[228:231], v[84:87]\n"
"v_mfma_f32_16x16x32_bf16 v[80:83], v[196:199], v[224:227], v[80:83]\n"
"v_mfma_f32_16x16x32_bf16 v[96:99], v[200:203], v[224:227], v[96:99]\n"
"v_mfma_f32_16x16x32_bf16 v[100:103], v[200:203], v[228:231], v[100:103]\n"
"v_mfma_f32_16x16x32_bf16 v[104:107], v[200:203], v[232:235], v[104:107]\n"
"v_mfma_f32_16x16x32_bf16 v[108:111], v[200:203], v[236:239], v[108:111]\n"
"v_mfma_f32_16x16x32_bf16 v[124:127], v[204:207], v[236:239], v[124:127]\n"
"v_mfma_f32_16x16x32_bf16 v[120:123], v[204:207], v[232:235], v[120:123]\n"
"v_mfma_f32_16x16x32_bf16 v[116:119], v[204:207], v[228:231], v[116:119]\n"
"v_mfma_f32_16x16x32_bf16 v[112:115], v[204:207], v[224:227], v[112:115]\n"
"s_waitcnt lgkmcnt(0)\n"
"s_barrier\n"
"s_sleep 1\n"
"v_mfma_f32_16x16x32_bf16 v[64:67], v[208:211], v[240:243], v[64:67]\n"
"ds_read_b128 v[128:131], v12 offset:0\n"
"ds_read_b64_tr_b16 v[160:161], v18 offset:0\n"
"v_mfma_f32_16x16x32_bf16 v[68:71], v[208:211], v[244:247], v[68:71]\n"
"ds_read_b64_tr_b16 v[162:163], v18 offset:1024\n"
"v_mfma_f32_16x16x32_bf16 v[72:75], v[208:211], v[248:251], v[72:75]\n"
"ds_read_b128 v[132:135], v12 offset:2048\n"
"ds_read_b64_tr_b16 v[164:165], v19 offset:0\n"
"v_mfma_f32_16x16x32_bf16 v[76:79], v[208:211], v[252:255], v[76:79]\n"
"ds_read_b64_tr_b16 v[166:167], v19 offset:1024\n"
"v_mfma_f32_16x16x32_bf16 v[92:95], v[212:215], v[252:255], v[92:95]\n"
"ds_read_b128 v[136:139], v12 offset:4096\n"
"ds_read_b64_tr_b16 v[168:169], v20 offset:0\n"
"v_mfma_f32_16x16x32_bf16 v[88:91], v[212:215], v[248:251], v[88:91]\n"
"ds_read_b64_tr_b16 v[170:171], v20 offset:1024\n"
"v_mfma_f32_16x16x32_bf16 v[84:87], v[212:215], v[244:247], v[84:87]\n"
"ds_read_b128 v[140:143], v12 offset:6144\n"
"ds_read_b64_tr_b16 v[172:173], v21 offset:0\n"
"v_mfma_f32_16x16x32_bf16 v[80:83], v[212:215], v[240:243], v[80:83]\n"
"ds_read_b64_tr_b16 v[174:175], v21 offset:1024\n"
"v_mfma_f32_16x16x32_bf16 v[96:99], v[216:219], v[240:243], v[96:99]\n"
"ds_read_b128 v[144:147], v13 offset:0\n"
"ds_read_b64_tr_b16 v[176:177], v18 offset:8192\n"
"v_mfma_f32_16x16x32_bf16 v[100:103], v[216:219], v[244:247], v[100:103]\n"
"ds_read_b64_tr_b16 v[178:179], v18 offset:9216\n"
"v_mfma_f32_16x16x32_bf16 v[104:107], v[216:219], v[248:251], v[104:107]\n"
"ds_read_b128 v[148:151], v13 offset:2048\n"
"ds_read_b64_tr_b16 v[180:181], v19 offset:8192\n"
"v_mfma_f32_16x16x32_bf16 v[108:111], v[216:219], v[252:255], v[108:111]\n"
"ds_read_b64_tr_b16 v[182:183], v19 offset:9216\n"
"v_mfma_f32_16x16x32_bf16 v[124:127], v[220:223], v[252:255], v[124:127]\n"
"ds_read_b128 v[152:155], v13 offset:4096\n"
"ds_read_b64_tr_b16 v[184:185], v20 offset:8192\n"
"v_mfma_f32_16x16x32_bf16 v[120:123], v[220:223], v[248:251], v[120:123]\n"
"ds_read_b64_tr_b16 v[186:187], v20 offset:9216\n"
"v_mfma_f32_16x16x32_bf16 v[116:119], v[220:223], v[244:247], v[116:119]\n"
"ds_read_b128 v[156:159], v13 offset:6144\n"
"ds_read_b64_tr_b16 v[188:189], v21 offset:8192\n"
"v_mfma_f32_16x16x32_bf16 v[112:115], v[220:223], v[240:243], v[112:115]\n"
"ds_read_b64_tr_b16 v[190:191], v21 offset:9216\n"
"s_waitcnt lgkmcnt(12)\n"
"v_mfma_f32_16x16x32_bf16 v[64:67], v[128:131], v[160:163], v[64:67]\n"
"v_mfma_f32_16x16x32_bf16 v[68:71], v[128:131], v[164:167], v[68:71]\n"
"v_mfma_f32_16x16x32_bf16 v[72:75], v[128:131], v[168:171], v[72:75]\n"
"v_mfma_f32_16x16x32_bf16 v[76:79], v[128:131], v[172:175], v[76:79]\n"
"v_mfma_f32_16x16x32_bf16 v[92:95], v[132:135], v[172:175], v[92:95]\n"
"v_mfma_f32_16x16x32_bf16 v[88:91], v[132:135], v[168:171], v[88:91]\n"
"v_mfma_f32_16x16x32_bf16 v[84:87], v[132:135], v[164:167], v[84:87]\n"
"v_mfma_f32_16x16x32_bf16 v[80:83], v[132:135], v[160:163], v[80:83]\n"
"v_mfma_f32_16x16x32_bf16 v[96:99], v[136:139], v[160:163], v[96:99]\n"
"v_mfma_f32_16x16x32_bf16 v[100:103], v[136:139], v[164:167], v[100:103]\n"
"v_mfma_f32_16x16x32_bf16 v[104:107], v[136:139], v[168:171], v[104:107]\n"
"v_mfma_f32_16x16x32_bf16 v[108:111], v[136:139], v[172:175], v[108:111]\n"
"v_mfma_f32_16x16x32_bf16 v[124:127], v[140:143], v[172:175], v[124:127]\n"
"v_mfma_f32_16x16x32_bf16 v[120:123], v[140:143], v[168:171], v[120:123]\n"
"v_mfma_f32_16x16x32_bf16 v[116:119], v[140:143], v[164:167], v[116:119]\n"
"v_mfma_f32_16x16x32_bf16 v[112:115], v[140:143], v[160:163], v[112:115]\n"
"s_waitcnt lgkmcnt(0)\n"
"s_barrier\n"
"s_sleep 1\n"
"v_mfma_f32_16x16x32_bf16 v[64:67], v[144:147], v[176:179], v[64:67]\n"
"ds_read_b128 v[192:195], v12 offset:32768\n"
"ds_read_b64_tr_b16 v[224:225], v18 offset:32768\n"
"v_mfma_f32_16x16x32_bf16 v[68:71], v[144:147], v[180:183], v[68:71]\n"
"ds_read_b64_tr_b16 v[226:227], v18 offset:33792\n"
"v_mfma_f32_16x16x32_bf16 v[72:75], v[144:147], v[184:187], v[72:75]\n"
"ds_read_b128 v[196:199], v12 offset:34816\n"
"ds_read_b64_tr_b16 v[228:229], v19 offset:32768\n"
"v_mfma_f32_16x16x32_bf16 v[76:79], v[144:147], v[188:191], v[76:79]\n"
"ds_read_b64_tr_b16 v[230:231], v19 offset:33792\n"
"v_mfma_f32_16x16x32_bf16 v[92:95], v[148:151], v[188:191], v[92:95]\n"
"ds_read_b128 v[200:203], v12 offset:36864\n"
"ds_read_b64_tr_b16 v[232:233], v20 offset:32768\n"
"v_mfma_f32_16x16x32_bf16 v[88:91], v[148:151], v[184:187], v[88:91]\n"
"ds_read_b64_tr_b16 v[234:235], v20 offset:33792\n"
"v_mfma_f32_16x16x32_bf16 v[84:87], v[148:151], v[180:183], v[84:87]\n"
"ds_read_b128 v[204:207], v12 offset:38912\n"
"ds_read_b64_tr_b16 v[236:237], v21 offset:32768\n"
"v_mfma_f32_16x16x32_bf16 v[80:83], v[148:151], v[176:179], v[80:83]\n"
"ds_read_b64_tr_b16 v[238:239], v21 offset:33792\n"
"v_mfma_f32_16x16x32_bf16 v[96:99], v[152:155], v[176:179], v[96:99]\n"
"ds_read_b128 v[208:211], v13 offset:32768\n"
"ds_read_b64_tr_b16 v[240:241], v18 offset:40960\n"
"v_mfma_f32_16x16x32_bf16 v[100:103], v[152:155], v[180:183], v[100:103]\n"
"ds_read_b64_tr_b16 v[242:243], v18 offset:41984\n"
"v_mfma_f32_16x16x32_bf16 v[104:107], v[152:155], v[184:187], v[104:107]\n"
"ds_read_b128 v[212:215], v13 offset:34816\n"
"ds_read_b64_tr_b16 v[244:245], v19 offset:40960\n"
"v_mfma_f32_16x16x32_bf16 v[108:111], v[152:155], v[188:191], v[108:111]\n"
"ds_read_b64_tr_b16 v[246:247], v19 offset:41984\n"
"v_mfma_f32_16x16x32_bf16 v[124:127], v[156:159], v[188:191], v[124:127]\n"
"ds_read_b128 v[216:219], v13 offset:36864\n"
"ds_read_b64_tr_b16 v[248:249], v20 offset:40960\n"
"v_mfma_f32_16x16x32_bf16 v[120:123], v[156:159], v[184:187], v[120:123]\n"
"ds_read_b64_tr_b16 v[250:251], v20 offset:41984\n"
"v_mfma_f32_16x16x32_bf16 v[116:119], v[156:159], v[180:183], v[116:119]\n"
"ds_read_b128 v[220:223], v13 offset:38912\n"
"ds_read_b64_tr_b16 v[252:253], v21 offset:40960\n"
"v_mfma_f32_16x16x32_bf16 v[112:115], v[156:159], v[176:179], v[112:115]\n"
"ds_read_b64_tr_b16 v[254:255], v21 offset:41984\n"
"s_waitcnt lgkmcnt(12)\n"
"v_mfma_f32_16x16x32_bf16 v[64:67], v[192:195], v[224:227], v[64:67]\n"
"v_mfma_f32_16x16x32_bf16 v[68:71], v[192:195], v[228:231], v[68:71]\n"
"v_mfma_f32_16x16x32_bf16 v[72:75], v[192:195], v[232:235], v[72:75]\n"
"v_mfma_f32_16x16x32_bf16 v[76:79], v[192:195], v[236:239], v[76:79]\n"
"v_mfma_f32_16x16x32_bf16 v[92:95], v[196:199], v[236:239], v[92:95]\n"
"v_mfma_f32_16x16x32_bf16 v[88:91], v[196:199], v[232:235], v[88:91]\n"
"v_mfma_f32_16x16x32_bf16 v[84:87], v[196:199], v[228:231], v[84:87]\n"
"v_mfma_f32_16x16x32_bf16 v[80:83], v[196:199], v[224:227], v[80:83]\n"
"v_mfma_f32_16x16x32_bf16 v[96:99], v[200:203], v[224:227], v[96:99]\n"
"v_mfma_f32_16x16x32_bf16 v[100:103], v[200:203], v[228:231], v[100:103]\n"
"v_mfma_f32_16x16x32_bf16 v[104:107], v[200:203], v[232:235], v[104:107]\n"
"v_mfma_f32_16x16x32_bf16 v[108:111], v[200:203], v[236:239], v[108:111]\n"
"v_mfma_f32_16x16x32_bf16 v[124:127], v[204:207], v[236:239], v[124:127]\n"
"v_mfma_f32_16x16x32_bf16 v[120:123], v[204:207], v[232:235], v[120:123]\n"
"v_mfma_f32_16x16x32_bf16 v[116:119], v[204:207], v[228:231], v[116:119]\n"
"v_mfma_f32_16x16x32_bf16 v[112:115], v[204:207], v[224:227], v[112:115]\n"
"s_waitcnt lgkmcnt(0)\n"
"s_barrier\n"
"s_sleep 1\n"
"v_mfma_f32_16x16x32_bf16 v[64:67], v[208:211], v[240:243], v[64:67]\n"
"ds_read_b128 v[128:131], v10 offset:0\n"
"ds_read_b64_tr_b16 v[160:161], v14 offset:0\n"
"v_mfma_f32_16x16x32_bf16 v[68:71], v[208:211], v[244:247], v[68:71]\n"
"ds_read_b64_tr_b16 v[162:163], v14 offset:1024\n"
"v_mfma_f32_16x16x32_bf16 v[72:75], v[208:211], v[248:251], v[72:75]\n"
"ds_read_b128 v[132:135], v10 offset:2048\n"
"ds_read_b64_tr_b16 v[164:165], v15 offset:0\n"
"v_mfma_f32_16x16x32_bf16 v[76:79], v[208:211], v[252:255], v[76:79]\n"
"ds_read_b64_tr_b16 v[166:167], v15 offset:1024\n"
"v_mfma_f32_16x16x32_bf16 v[92:95], v[212:215], v[252:255], v[92:95]\n"
"ds_read_b128 v[136:139], v10 offset:4096\n"
"ds_read_b64_tr_b16 v[168:169], v16 offset:0\n"
"v_mfma_f32_16x16x32_bf16 v[88:91], v[212:215], v[248:251], v[88:91]\n"
"ds_read_b64_tr_b16 v[170:171], v16 offset:1024\n"
"v_mfma_f32_16x16x32_bf16 v[84:87], v[212:215], v[244:247], v[84:87]\n"
"ds_read_b128 v[140:143], v10 offset:6144\n"
"ds_read_b64_tr_b16 v[172:173], v17 offset:0\n"
"v_mfma_f32_16x16x32_bf16 v[80:83], v[212:215], v[240:243], v[80:83]\n"
"ds_read_b64_tr_b16 v[174:175], v17 offset:1024\n"
"v_mfma_f32_16x16x32_bf16 v[96:99], v[216:219], v[240:243], v[96:99]\n"
"ds_read_b128 v[144:147], v11 offset:0\n"
"ds_read_b64_tr_b16 v[176:177], v14 offset:8192\n"
"v_mfma_f32_16x16x32_bf16 v[100:103], v[216:219], v[244:247], v[100:103]\n"
"ds_read_b64_tr_b16 v[178:179], v14 offset:9216\n"
"v_mfma_f32_16x16x32_bf16 v[104:107], v[216:219], v[248:251], v[104:107]\n"
"ds_read_b128 v[148:151], v11 offset:2048\n"
"ds_read_b64_tr_b16 v[180:181], v15 offset:8192\n"
"v_mfma_f32_16x16x32_bf16 v[108:111], v[216:219], v[252:255], v[108:111]\n"
"ds_read_b64_tr_b16 v[182:183], v15 offset:9216\n"
"v_mfma_f32_16x16x32_bf16 v[124:127], v[220:223], v[252:255], v[124:127]\n"
"ds_read_b128 v[152:155], v11 offset:4096\n"
"ds_read_b64_tr_b16 v[184:185], v16 offset:8192\n"
"v_mfma_f32_16x16x32_bf16 v[120:123], v[220:223], v[248:251], v[120:123]\n"
"ds_read_b64_tr_b16 v[186:187], v16 offset:9216\n"
"v_mfma_f32_16x16x32_bf16 v[116:119], v[220:223], v[244:247], v[116:119]\n"
"ds_read_b128 v[156:159], v11 offset:6144\n"
"ds_read_b64_tr_b16 v[188:189], v17 offset:8192\n"
"v_mfma_f32_16x16x32_bf16 v[112:115], v[220:223], v[240:243], v[112:115]\n"
"ds_read_b64_tr_b16 v[190:191], v17 offset:9216\n"
"s_waitcnt lgkmcnt(12)\n"
"v_mfma_f32_16x16x32_bf16 v[64:67], v[128:131], v[160:163], v[64:67]\n"
"v_mfma_f32_16x16x32_bf16 v[68:71], v[128:131], v[164:167], v[68:71]\n"
"v_mfma_f32_16x16x32_bf16 v[72:75], v[128:131], v[168:171], v[72:75]\n"
"v_mfma_f32_16x16x32_bf16 v[76:79], v[128:131], v[172:175], v[76:79]\n"
"v_mfma_f32_16x16x32_bf16 v[92:95], v[132:135], v[172:175], v[92:95]\n"
"v_mfma_f32_16x16x32_bf16 v[88:91], v[132:135], v[168:171], v[88:91]\n"
"v_mfma_f32_16x16x32_bf16 v[84:87], v[132:135], v[164:167], v[84:87]\n"
"v_mfma_f32_16x16x32_bf16 v[80:83], v[132:135], v[160:163], v[80:83]\n"
"v_mfma_f32_16x16x32_bf16 v[96:99], v[136:139], v[160:163], v[96:99]\n"
"v_mfma_f32_16x16x32_bf16 v[100:103], v[136:139], v[164:167], v[100:103]\n"
"v_mfma_f32_16x16x32_bf16 v[104:107], v[136:139], v[168:171], v[104:107]\n"
"v_mfma_f32_16x16x32_bf16 v[108:111], v[136:139], v[172:175], v[108:111]\n"
"v_mfma_f32_16x16x32_bf16 v[124:127], v[140:143], v[172:175], v[124:127]\n"
"v_mfma_f32_16x16x32_bf16 v[120:123], v[140:143], v[168:171], v[120:123]\n"
"v_mfma_f32_16x16x32_bf16 v[116:119], v[140:143], v[164:167], v[116:119]\n"
"v_mfma_f32_16x16x32_bf16 v[112:115], v[140:143], v[160:163], v[112:115]\n"
"s_waitcnt lgkmcnt(0)\n"
"s_barrier\n"
"s_mov_b64 exec, 0xffff\n"
"ds_read_b64 v[28:29], v1 offset:0\n"
"ds_read_b64 v[30:31], v1 offset:128\n"
"ds_read_b64 v[32:33], v1 offset:256\n"
"ds_read_b64 v[34:35], v1 offset:384\n"
"s_mov_b64 exec, -1\n"
"v_mfma_f32_16x16x32_bf16 v[64:67], v[144:147], v[176:179], v[64:67]\n"
"ds_read_b64 v[52:53], v9 offset:0\n"
"ds_read_b64 v[54:55], v9 offset:128\n"
"v_mfma_f32_16x16x32_bf16 v[68:71], v[144:147], v[180:183], v[68:71]\n"
"s_waitcnt vmcnt(0)\n"
"ds_read_b64 v[56:57], v9 offset:256\n"
"v_mfma_f32_16x16x32_bf16 v[72:75], v[144:147], v[184:187], v[72:75]\n"
"ds_read_b64 v[58:59], v9 offset:384\n"
"ds_read_b64_tr_b16 v[224:225], v14 offset:32768\n"
"v_mfma_f32_16x16x32_bf16 v[76:79], v[144:147], v[188:191], v[76:79]\n"
"ds_read_b64_tr_b16 v[226:227], v14 offset:33792\n"
"v_mfma_f32_16x16x32_bf16 v[92:95], v[148:151], v[188:191], v[92:95]\n"
"ds_read_b128 v[192:195], v10 offset:32768\n"
"ds_read_b128 v[196:199], v10 offset:34816\n"
"v_mfma_f32_16x16x32_bf16 v[88:91], v[148:151], v[184:187], v[88:91]\n"
"ds_read_b128 v[200:203], v10 offset:36864\n"
"v_mfma_f32_16x16x32_bf16 v[84:87], v[148:151], v[180:183], v[84:87]\n"
"ds_read_b128 v[204:207], v10 offset:38912\n"
"ds_read_b64_tr_b16 v[240:241], v14 offset:40960\n"
"v_mfma_f32_16x16x32_bf16 v[80:83], v[148:151], v[176:179], v[80:83]\n"
"ds_read_b64_tr_b16 v[242:243], v14 offset:41984\n"
"v_mfma_f32_16x16x32_bf16 v[96:99], v[152:155], v[176:179], v[96:99]\n"
"ds_read_b128 v[208:211], v11 offset:32768\n"
"ds_read_b128 v[212:215], v11 offset:34816\n"
"v_mfma_f32_16x16x32_bf16 v[100:103], v[152:155], v[180:183], v[100:103]\n"
"ds_read_b128 v[216:219], v11 offset:36864\n"
"v_mfma_f32_16x16x32_bf16 v[104:107], v[152:155], v[184:187], v[104:107]\n"
"ds_read_b128 v[220:223], v11 offset:38912\n"
"ds_read_b64_tr_b16 v[228:229], v15 offset:32768\n"
"v_mfma_f32_16x16x32_bf16 v[108:111], v[152:155], v[188:191], v[108:111]\n"
"ds_read_b64_tr_b16 v[230:231], v15 offset:33792\n"
"v_mfma_f32_16x16x32_bf16 v[124:127], v[156:159], v[188:191], v[124:127]\n"
"ds_read_b64_tr_b16 v[244:245], v15 offset:40960\n"
"ds_read_b64_tr_b16 v[246:247], v15 offset:41984\n"
"v_mfma_f32_16x16x32_bf16 v[120:123], v[156:159], v[184:187], v[120:123]\n"
"ds_read_b64_tr_b16 v[232:233], v16 offset:32768\n"
"v_mfma_f32_16x16x32_bf16 v[116:119], v[156:159], v[180:183], v[116:119]\n"
"ds_read_b64_tr_b16 v[234:235], v16 offset:33792\n"
"ds_read_b64_tr_b16 v[248:249], v16 offset:40960\n"
"v_mfma_f32_16x16x32_bf16 v[112:115], v[156:159], v[176:179], v[112:115]\n"
"ds_read_b64_tr_b16 v[250:251], v16 offset:41984\n"
"s_waitcnt lgkmcnt(15)\n"
"v_mfma_f32_16x16x16_bf16 v[64:67], v[28:29], v[52:53], v[64:67]\n"
"ds_read_b64_tr_b16 v[236:237], v17 offset:32768\n"
"ds_read_b64_tr_b16 v[238:239], v17 offset:33792\n"
"v_mfma_f32_16x16x16_bf16 v[80:83], v[30:31], v[52:53], v[80:83]\n"
"ds_read_b64_tr_b16 v[252:253], v17 offset:40960\n"
"v_mfma_f32_16x16x16_bf16 v[96:99], v[32:33], v[52:53], v[96:99]\n"
"ds_read_b64_tr_b16 v[254:255], v17 offset:41984\n"
"ds_read_b128 v[128:131], v26 offset:0\n"
"v_mfma_f32_16x16x16_bf16 v[112:115], v[34:35], v[52:53], v[112:115]\n"
"ds_read_b128 v[132:135], v26 offset:64\n"
"v_mfma_f32_16x16x32_bf16 v[64:67], v[192:195], v[224:227], v[64:67]\n"
"ds_read_b128 v[136:139], v26 offset:128\n"
"ds_read_b128 v[140:143], v26 offset:192\n"
"v_mfma_f32_16x16x32_bf16 v[80:83], v[196:199], v[224:227], v[80:83]\n"
"v_mfma_f32_16x16x32_bf16 v[96:99], v[200:203], v[224:227], v[96:99]\n"
"s_waitcnt lgkmcnt(15)\n"
"v_mfma_f32_16x16x32_bf16 v[112:115], v[204:207], v[224:227], v[112:115]\n"
"v_mfma_f32_16x16x32_bf16 v[64:67], v[208:211], v[240:243], v[64:67]\n"
"v_mfma_f32_16x16x32_bf16 v[80:83], v[212:215], v[240:243], v[80:83]\n"
"v_mfma_f32_16x16x32_bf16 v[96:99], v[216:219], v[240:243], v[96:99]\n"
"v_mfma_f32_16x16x32_bf16 v[112:115], v[220:223], v[240:243], v[112:115]\n"
"v_mfma_f32_16x16x16_bf16 v[68:71], v[28:29], v[54:55], v[68:71]\n"
"v_mfma_f32_16x16x16_bf16 v[84:87], v[30:31], v[54:55], v[84:87]\n"
"v_mfma_f32_16x16x16_bf16 v[100:103], v[32:33], v[54:55], v[100:103]\n"
"v_mfma_f32_16x16x16_bf16 v[116:119], v[34:35], v[54:55], v[116:119]\n"
"s_waitcnt lgkmcnt(3)\n"
"v_mul_f32 v64, v64, v128\n"
"v_mul_f32 v65, v65, v129\n"
"v_mfma_f32_16x16x32_bf16 v[68:71], v[192:195], v[228:231], v[68:71]\n"
"v_mul_f32 v66, v66, v130\n"
"v_mul_f32 v67, v67, v131\n"
"v_mfma_f32_16x16x32_bf16 v[84:87], v[196:199], v[228:231], v[84:87]\n"
"s_waitcnt lgkmcnt(2)\n"
"v_mul_f32 v80, v80, v132\n"
"v_mul_f32 v81, v81, v133\n"
"v_mfma_f32_16x16x32_bf16 v[100:103], v[200:203], v[228:231], v[100:103]\n"
"v_mul_f32 v82, v82, v134\n"
"v_mul_f32 v83, v83, v135\n"
"v_mfma_f32_16x16x32_bf16 v[116:119], v[204:207], v[228:231], v[116:119]\n"
"v_cvt_pk_bf16_f32 v36, v64, v65\n"
"v_cvt_pk_bf16_f32 v37, v66, v67\n"
"v_mfma_f32_16x16x32_bf16 v[68:71], v[208:211], v[244:247], v[68:71]\n"
"v_cvt_pk_bf16_f32 v38, v80, v81\n"
"v_cvt_pk_bf16_f32 v39, v82, v83\n"
"v_mfma_f32_16x16x32_bf16 v[84:87], v[212:215], v[244:247], v[84:87]\n"
"s_waitcnt lgkmcnt(1)\n"
"v_mul_f32 v96, v96, v136\n"
"v_mul_f32 v97, v97, v137\n"
"v_mfma_f32_16x16x32_bf16 v[100:103], v[216:219], v[244:247], v[100:103]\n"
"v_permlane16_swap_b32 v36, v38\n"
"v_permlane16_swap_b32 v37, v39\n"
"v_mfma_f32_16x16x32_bf16 v[116:119], v[220:223], v[244:247], v[116:119]\n"
"buffer_store_dwordx4 v[36:39], v22, s[4:7], s49 offen offset:0 sc1\n"
"v_mul_f32 v98, v98, v138\n"
"v_mul_f32 v99, v99, v139\n"
"v_mfma_f32_16x16x16_bf16 v[72:75], v[28:29], v[56:57], v[72:75]\n"
"s_waitcnt lgkmcnt(0)\n"
"v_mul_f32 v112, v112, v140\n"
"v_mul_f32 v113, v113, v141\n"
"v_mfma_f32_16x16x16_bf16 v[88:91], v[30:31], v[56:57], v[88:91]\n"
"v_mul_f32 v114, v114, v142\n"
"v_mul_f32 v115, v115, v143\n"
"v_mfma_f32_16x16x16_bf16 v[104:107], v[32:33], v[56:57], v[104:107]\n"
"v_cvt_pk_bf16_f32 v40, v96, v97\n"
"v_cvt_pk_bf16_f32 v41, v98, v99\n"
"v_mfma_f32_16x16x16_bf16 v[120:123], v[34:35], v[56:57], v[120:123]\n"
"v_cvt_pk_bf16_f32 v42, v112, v113\n"
"v_cvt_pk_bf16_f32 v43, v114, v115\n"
"v_mfma_f32_16x16x32_bf16 v[72:75], v[192:195], v[232:235], v[72:75]\n"
"v_permlane16_swap_b32 v40, v42\n"
"v_permlane16_swap_b32 v41, v43\n"
"v_mfma_f32_16x16x32_bf16 v[88:91], v[196:199], v[232:235], v[88:91]\n"
"buffer_store_dwordx4 v[40:43], v22, s[4:7], s49 offen offset:1280 sc1\n"
"v_mul_f32 v68, v68, v128\n"
"v_mul_f32 v69, v69, v129\n"
"v_mfma_f32_16x16x32_bf16 v[104:107], v[200:203], v[232:235], v[104:107]\n"
"v_mul_f32 v70, v70, v130\n"
"v_mul_f32 v71, v71, v131\n"
"v_mfma_f32_16x16x32_bf16 v[120:123], v[204:207], v[232:235], v[120:123]\n"
"v_mul_f32 v84, v84, v132\n"
"v_mul_f32 v85, v85, v133\n"
"v_mfma_f32_16x16x32_bf16 v[72:75], v[208:211], v[248:251], v[72:75]\n"
"v_mul_f32 v86, v86, v134\n"
"v_mul_f32 v87, v87, v135\n"
"v_mfma_f32_16x16x32_bf16 v[88:91], v[212:215], v[248:251], v[88:91]\n"
"v_cvt_pk_bf16_f32 v44, v68, v69\n"
"v_cvt_pk_bf16_f32 v45, v70, v71\n"
"v_mfma_f32_16x16x32_bf16 v[104:107], v[216:219], v[248:251], v[104:107]\n"
"v_cvt_pk_bf16_f32 v46, v84, v85\n"
"v_cvt_pk_bf16_f32 v47, v86, v87\n"
"v_mfma_f32_16x16x32_bf16 v[120:123], v[220:223], v[248:251], v[120:123]\n"
"v_mul_f32 v100, v100, v136\n"
"v_mul_f32 v101, v101, v137\n"
"v_mfma_f32_16x16x16_bf16 v[76:79], v[28:29], v[58:59], v[76:79]\n"
"v_permlane16_swap_b32 v44, v46\n"
"v_permlane16_swap_b32 v45, v47\n"
"v_mfma_f32_16x16x16_bf16 v[92:95], v[30:31], v[58:59], v[92:95]\n"
"buffer_store_dwordx4 v[44:47], v23, s[4:7], s49 offen offset:0 sc1\n"
"v_mul_f32 v102, v102, v138\n"
"v_mul_f32 v103, v103, v139\n"
"v_mfma_f32_16x16x16_bf16 v[108:111], v[32:33], v[58:59], v[108:111]\n"
"v_mul_f32 v116, v116, v140\n"
"v_mul_f32 v117, v117, v141\n"
"v_mfma_f32_16x16x16_bf16 v[124:127], v[34:35], v[58:59], v[124:127]\n"
"v_mul_f32 v118, v118, v142\n"
"v_mul_f32 v119, v119, v143\n"
"v_mfma_f32_16x16x32_bf16 v[76:79], v[192:195], v[236:239], v[76:79]\n"
"v_cvt_pk_bf16_f32 v48, v100, v101\n"
"v_cvt_pk_bf16_f32 v49, v102, v103\n"
"v_mfma_f32_16x16x32_bf16 v[92:95], v[196:199], v[236:239], v[92:95]\n"
"v_cvt_pk_bf16_f32 v50, v116, v117\n"
"v_cvt_pk_bf16_f32 v51, v118, v119\n"
"v_mfma_f32_16x16x32_bf16 v[108:111], v[200:203], v[236:239], v[108:111]\n"
"v_permlane16_swap_b32 v48, v50\n"
"v_permlane16_swap_b32 v49, v51\n"
"v_mfma_f32_16x16x32_bf16 v[124:127], v[204:207], v[236:239], v[124:127]\n"
"buffer_store_dwordx4 v[48:51], v23, s[4:7], s49 offen offset:1280 sc1\n"
"v_mul_f32 v72, v72, v128\n"
"v_mul_f32 v73, v73, v129\n"
"v_mfma_f32_16x16x32_bf16 v[76:79], v[208:211], v[252:255], v[76:79]\n"
"v_mul_f32 v74, v74, v130\n"
"v_mul_f32 v75, v75, v131\n"
"v_mfma_f32_16x16x32_bf16 v[92:95], v[212:215], v[252:255], v[92:95]\n"
"v_mul_f32 v88, v88, v132\n"
"v_mul_f32 v89, v89, v133\n"
"v_mfma_f32_16x16x32_bf16 v[108:111], v[216:219], v[252:255], v[108:111]\n"
"v_mul_f32 v90, v90, v134\n"
"v_mul_f32 v91, v91, v135\n"
"s_cbranch_execz .Lk_tramp\n"
"s_nop 0\n"
"v_mfma_f32_16x16x32_bf16 v[124:127], v[220:223], v[252:255], v[124:127]\n"
"v_cvt_pk_bf16_f32 v36, v72, v73\n"
"v_cvt_pk_bf16_f32 v37, v74, v75\n"
"v_cvt_pk_bf16_f32 v38, v88, v89\n"
"v_cvt_pk_bf16_f32 v39, v90, v91\n"
"v_mul_f32 v104, v104, v136\n"
"v_mul_f32 v105, v105, v137\n"
"v_permlane16_swap_b32 v36, v38\n"
"v_permlane16_swap_b32 v37, v39\n"
"buffer_store_dwordx4 v[36:39], v24, s[4:7], s49 offen offset:0 sc1\n"
"v_mul_f32 v106, v106, v138\n"
"v_mul_f32 v107, v107, v139\n"
"v_mul_f32 v120, v120, v140\n"
"v_mul_f32 v121, v121, v141\n"
"v_mul_f32 v122, v122, v142\n"
"v_mul_f32 v123, v123, v143\n"
"v_cvt_pk_bf16_f32 v40, v104, v105\n"
"v_cvt_pk_bf16_f32 v41, v106, v107\n"
"v_cvt_pk_bf16_f32 v42, v120, v121\n"
"v_cvt_pk_bf16_f32 v43, v122, v123\n"
"s_nop 0\n"
"v_permlane16_swap_b32 v40, v42\n"
"v_permlane16_swap_b32 v41, v43\n"
"buffer_store_dwordx4 v[40:43], v24, s[4:7], s49 offen offset:1280 sc1\n"
"v_mul_f32 v76, v76, v128\n"
"v_mul_f32 v77, v77, v129\n"
"v_mul_f32 v78, v78, v130\n"
"v_mul_f32 v79, v79, v131\n"
"v_mul_f32 v92, v92, v132\n"
"v_mul_f32 v93, v93, v133\n"
"v_mul_f32 v94, v94, v134\n"
"v_mul_f32 v95, v95, v135\n"
"v_cvt_pk_bf16_f32 v44, v76, v77\n"
"v_cvt_pk_bf16_f32 v45, v78, v79\n"
"v_cvt_pk_bf16_f32 v46, v92, v93\n"
"v_cvt_pk_bf16_f32 v47, v94, v95\n"
"v_mul_f32 v108, v108, v136\n"
"v_mul_f32 v109, v109, v137\n"
"v_permlane16_swap_b32 v44, v46\n"
"v_permlane16_swap_b32 v45, v47\n"
"buffer_store_dwordx4 v[44:47], v25, s[4:7], s49 offen offset:0 sc1\n"
"v_mul_f32 v110, v110, v138\n"
"v_mul_f32 v111, v111, v139\n"
"v_mul_f32 v124, v124, v140\n"
"v_mul_f32 v125, v125, v141\n"
"v_mul_f32 v126, v126, v142\n"
"v_mul_f32 v127, v127, v143\n"
"v_cvt_pk_bf16_f32 v48, v108, v109\n"
"v_cvt_pk_bf16_f32 v49, v110, v111\n"
"v_cvt_pk_bf16_f32 v50, v124, v125\n"
"v_cvt_pk_bf16_f32 v51, v126, v127\n"
"s_nop 0\n"
"v_permlane16_swap_b32 v48, v50\n"
"v_permlane16_swap_b32 v49, v51\n"
"buffer_store_dwordx4 v[48:51], v25, s[4:7], s49 offen offset:1280 sc1\n"
"v_lshlrev_b32 v14, 4, v0\n"
"s_waitcnt vmcnt(0)\n"
"s_barrier\n"
"s_setprio 0\n"
"s_cmp_lg_u32 s3, 0\n"
"s_cbranch_scc1 .Lk_p1np_20\n"
"s_mov_b64 exec, 0xff\n"
"buffer_store_dwordx4 v[60:63], v14, s[4:7], s12 offen sc1\n"
"s_mov_b64 exec, -1\n"
".Lk_p1np_20:\n"
".Lk_cdone_3:\n"
"s_lshl_b32 s56, s35, 2\n"
"s_lshr_b32 s57, s36, 3\n"
"s_add_u32 s40, s56, s57\n"
"s_and_b32 s56, s35, 0\n"
"s_lshl_b32 s57, s56, 3\n"
"s_and_b32 s58, s36, 7\n"
"s_add_u32 s41, s57, s58\n"
"s_lshl_b32 s56, s35, 2\n"
"s_lshr_b32 s57, s36, 3\n"
"s_add_u32 s62, s56, s57\n"
"s_and_b32 s56, s35, 0\n"
"s_lshl_b32 s57, s56, 3\n"
"s_and_b32 s58, s36, 7\n"
"s_add_u32 s63, s57, s58\n"
"v_lshlrev_b32 v14, 4, v0\n"
"s_mul_i32 s56, s40, 0x14000\n"
"s_add_u32 s57, 0x311000, s56\n"
"s_lshl_b32 s58, s41, 1\n"
"s_lshr_b32 s59, s34, 2\n"
"s_add_u32 s60, s58, s59\n"
"s_mul_i32 s61, s60, 0x1400\n"
"s_add_u32 s20, s57, s61\n"
"v_lshrrev_b32 v2, 5, v0\n"
"v_lshlrev_b32 v3, 11, v2\n"
"v_and_b32 v4, 31, v0\n"
"v_lshlrev_b32 v5, 4, v4\n"
"v_add_u32 v10, v3, v5\n"
"s_lshl_b32 s56, s3, 9\n"
"v_add_u32 v6, s56, v10\n"
"s_lshl_b32 s56, s3, 5\n"
"v_and_b32 v2, 31, v0\n"
"v_add_u32 v3, s56, v2\n"
"v_lshlrev_b32 v4, 3, v3\n"
"v_add_u32 v7, 0x1000, v4\n"
"v_and_b32 v2, 31, v0\n"
"v_lshlrev_b32 v3, 3, v2\n"
"v_add_u32 v11, 0x1000, v3\n"
"v_lshrrev_b32 v2, 5, v0\n"
"v_mul_u32_u24 v3, 0x140, v2\n"
"v_add_u32 v4, 0x1400, v3\n"
"v_and_b32 v5, 19, v0\n"
"v_and_b32 v2, 4, v0\n"
"v_lshlrev_b32 v3, 1, v2\n"
"v_or_b32 v2, v5, v3\n"
"v_and_b32 v5, 8, v0\n"
"v_lshrrev_b32 v3, 1, v5\n"
"v_or_b32 v5, v2, v3\n"
"v_lshlrev_b32 v2, 4, v5\n"
"v_add_u32 v12, v4, v2\n"
"s_lshl_b32 s17, s34, 10\n"
"s_mul_i32 s56, s40, 0x28000\n"
"s_add_u32 s57, 0x591000, s56\n"
"s_add_u32 s16, s57, s17\n"
"v_mov_b32 v13, 0xdddd\n"
"s_mov_b32 s18, 0xfffff\n"
"s_mov_b32 s19, 0xfffff\n"
"v_and_b32 v2, 27, v0\n"
"v_cmp_eq_u32 vcc, 24, v2\n"
"v_mov_b32 v2, 0x3f803f80\n"
"s_nop 0\n"
"v_cndmask_b32 v80, 0, v2, vcc\n"
"v_cndmask_b32 v81, 0, v2, vcc\n"
"v_cndmask_b32 v82, 0, v2, vcc\n"
"v_cndmask_b32 v83, 0, v2, vcc\n"
"v_cndmask_b32 v84, 0, v2, vcc\n"
"v_cndmask_b32 v85, 0, v2, vcc\n"
"v_cndmask_b32 v86, 0, v2, vcc\n"
"v_cndmask_b32 v87, 0, v2, vcc\n"
"v_cndmask_b32 v88, 0, v2, vcc\n"
"v_cndmask_b32 v89, 0, v2, vcc\n"
"v_cndmask_b32 v90, 0, v2, vcc\n"
"v_cndmask_b32 v91, 0, v2, vcc\n"
"v_cndmask_b32 v92, 0, v2, vcc\n"
"v_cndmask_b32 v93, 0, v2, vcc\n"
"v_cndmask_b32 v94, 0, v2, vcc\n"
"v_cndmask_b32 v95, 0, v2, vcc\n"
"v_mov_b32 v48, 0\n"
"v_mov_b32 v49, 0\n"
"v_mov_b32 v50, 0\n"
"v_mov_b32 v51, 0\n"
"v_mov_b32 v52, 0\n"
"v_mov_b32 v53, 0\n"
"v_mov_b32 v54, 0\n"
"v_mov_b32 v55, 0\n"
"v_mov_b32 v56, 0\n"
"v_mov_b32 v57, 0\n"
"v_mov_b32 v58, 0\n"
"v_mov_b32 v59, 0\n"
"v_mov_b32 v60, 0\n"
"v_mov_b32 v61, 0\n"
"v_mov_b32 v62, 0\n"
"v_mov_b32 v63, 0\n"
"s_lshl_b32 s56, s34, 3\n"
"s_and_b32 s57, s63, 7\n"
"s_add_u32 s58, s56, s57\n"
"s_mul_i32 s59, s58, 0xa000\n"
"s_add_u32 s13, 0x1762800, s59\n"
"s_lshl_b32 s56, s34, 3\n"
"s_add_u32 s38, s56, s63\n"
"s_cmp_lt_u32 s38, 54\n"
"s_cselect_b32 s13, s13, 0x7ff00000\n"
"s_cmp_eq_u32 s34, 7\n"
"s_movk_i32 s24, 0x800\n"
"s_cselect_b32 s24, 0x5000, s24\n"
"s_and_b32 s56, s63, 7\n"
"s_mul_i32 s57, s56, 0x19000\n"
"s_add_u32 s58, 0x1691000, s57\n"
"s_lshl_b32 s59, s34, 10\n"
"s_add_u32 s14, s58, s59\n"
"s_lshl_b32 s15, s34, 10\n"
"s_lshl_b32 s56, s41, 2\n"
"s_lshr_b32 s57, s34, 1\n"
"s_add_u32 s58, s56, s57\n"
"s_mul_i32 s59, s58, 0x14000\n"
"s_add_u32 s22, 0xa91000, s59\n"
"s_lshl_b32 s56, s41, 5\n"
"s_add_u32 s57, s56, s40\n"
"s_lshl_b32 s58, s57, 10\n"
"s_add_u32 s23, 0x81000, s58\n"
"s_branch .Lk_p1done_4\n"
".Lk_idle_5:\n"
"s_and_b32 s56, s35, 1\n"
"s_lshl_b32 s57, s56, 3\n"
"s_and_b32 s58, s35, 6\n"
"s_add_u32 s59, s57, s58\n"
"s_and_b32 s60, s36, 1\n"
"s_add_u32 s40, s59, s60\n"
"s_mov_b32 s18, 0xc8000\n"
"s_mov_b32 s19, 0x20000\n"
"s_mov_b32 s22, 0x21c000\n"
"s_mov_b32 s23, 0x20000\n"
"s_mov_b32 s26, 0x21c000\n"
"s_mov_b32 s27, 0x20000\n"
"s_mov_b32 s30, 0x280000\n"
"s_mov_b32 s31, 0x20000\n"
"s_mov_b32 s37, 1048576\n"
"s_waitcnt lgkmcnt(0)\n"
"buffer_load_dwordx2 v[6:7], off, s[4:7], 0 sc1\n"
"s_cmp_ge_u32 s34, 4\n"
"s_cbranch_scc1 .Lk_id0_24\n"
"s_lshr_b32 s56, s35, 1\n"
"s_lshl_b32 s57, s56, 9\n"
"s_and_b32 s58, s35, 1\n"
"s_lshl_b32 s59, s58, 5\n"
"s_add_u32 s60, s59, s36\n"
"s_lshl_b32 s61, s60, 3\n"
"s_add_u32 s56, s57, s61\n"
"s_lshl_b32 s58, s3, 1\n"
"s_add_u32 s42, s56, s58\n"
"s_mul_i32 s43, s42, 0x500\n"
"v_lshlrev_b32 v8, 4, v0\n"
"v_lshlrev_b32 v9, 2, v0\n"
"buffer_load_dwordx4 v[10:13], v8, s[8:11], s43 offen offset:0\n"
"buffer_load_dword v14, v9, s[8:11], s43 offen offset:1024\n"
"buffer_load_dwordx4 v[16:19], v8, s[8:11], s43 offen offset:1280\n"
"buffer_load_dword v20, v9, s[8:11], s43 offen offset:2304\n"
".Lk_id0_24:\n"
"s_lshr_b32 s56, s34, 2\n"
"s_sub_u32 s57, 1, s56\n"
"s_mul_i32 s58, s57, 0x140\n"
"s_lshl_b32 s59, s3, 2\n"
"s_add_u32 s60, s58, s59\n"
"v_lshrrev_b32 v2, 4, v0\n"
"v_add_u32 v3, s60, v2\n"
"v_mul_u32_u24 v4, 0xf00, v3\n"
"s_min_u32 s61, s40, 14\n"
"s_lshl_b32 s56, s61, 8\n"
"v_add_u32 v5, s56, v4\n"
"v_and_b32 v2, 15, v0\n"
"v_lshlrev_b32 v3, 4, v2\n"
"v_add_u32 v62, v5, v3\n"
"s_mov_b32 s39, 0\n"
"buffer_load_dwordx4 v[64:67], v62, s[12:15], s39 offen\n"
"s_mov_b32 s39, 61440\n"
"buffer_load_dwordx4 v[68:71], v62, s[12:15], s39 offen\n"
"s_mov_b32 s39, 122880\n"
"buffer_load_dwordx4 v[72:75], v62, s[12:15], s39 offen\n"
"s_mov_b32 s39, 184320\n"
"buffer_load_dwordx4 v[76:79], v62, s[12:15], s39 offen\n"
"s_mov_b32 s39, 245760\n"
"buffer_load_dwordx4 v[80:83], v62, s[12:15], s39 offen\n"
"s_mov_b32 s39, 307200\n"
"buffer_load_dwordx4 v[84:87], v62, s[12:15], s39 offen\n"
"s_mov_b32 s39, 368640\n"
"buffer_load_dwordx4 v[88:91], v62, s[12:15], s39 offen\n"
"s_mov_b32 s39, 430080\n"
"buffer_load_dwordx4 v[92:95], v62, s[12:15], s39 offen\n"
"s_mov_b32 s39, 491520\n"
"buffer_load_dwordx4 v[96:99], v62, s[12:15], s39 offen\n"
"s_mov_b32 s39, 552960\n"
"buffer_load_dwordx4 v[100:103], v62, s[12:15], s39 offen\n"
"s_mov_b32 s39, 614400\n"
"buffer_load_dwordx4 v[104:107], v62, s[12:15], s39 offen\n"
"s_mov_b32 s39, 675840\n"
"buffer_load_dwordx4 v[108:111], v62, s[12:15], s39 offen\n"
"s_mov_b32 s39, 737280\n"
"buffer_load_dwordx4 v[112:115], v62, s[12:15], s39 offen\n"
"s_mov_b32 s39, 798720\n"
"buffer_load_dwordx4 v[116:119], v62, s[12:15], s39 offen\n"
"s_mov_b32 s39, 860160\n"
"buffer_load_dwordx4 v[120:123], v62, s[12:15], s39 offen\n"
"s_mov_b32 s39, 921600\n"
"buffer_load_dwordx4 v[124:127], v62, s[12:15], s39 offen\n"
"s_mov_b32 s39, 983040\n"
"buffer_load_dwordx4 v[128:131], v62, s[12:15], s39 offen\n"
"s_mov_b32 s39, 1044480\n"
"buffer_load_dwordx4 v[132:135], v62, s[12:15], s39 offen\n"
"s_mov_b32 s39, 1105920\n"
"buffer_load_dwordx4 v[136:139], v62, s[12:15], s39 offen\n"
"s_mov_b32 s39, 1167360\n"
"buffer_load_dwordx4 v[140:143], v62, s[12:15], s39 offen\n"
"v_and_b32 v2, 1, v0\n"
"v_lshlrev_b32 v60, 4, v2\n"
"v_mov_b32 v61, 0x3f80\n"
"v_lshlrev_b32 v61, v60, v61\n"
"v_and_b32 v2, 15, v0\n"
"v_lshrrev_b32 v60, 1, v2\n"
"v_cmp_eq_u32 vcc, 0, v60\n"
"s_nop 1\n"
"v_cndmask_b32 v56, 0, v61, vcc\n"
"v_cmp_eq_u32 vcc, 1, v60\n"
"s_nop 1\n"
"v_cndmask_b32 v57, 0, v61, vcc\n"
"v_cmp_eq_u32 vcc, 2, v60\n"
"s_nop 1\n"
"v_cndmask_b32 v58, 0, v61, vcc\n"
"v_cmp_eq_u32 vcc, 3, v60\n"
"s_nop 1\n"
"v_cndmask_b32 v59, 0, v61, vcc\n"
"s_waitcnt vmcnt(20)\n"
"v_readfirstlane_b32 s0, v6\n"
"v_readfirstlane_b32 s1, v7\n"
"s_mul_hi_u32 s33, s0, 0x4c957f2d\n"
"s_mul_i32 s32, s0, 0x4c957f2d\n"
"s_mul_i32 s0, s0, 0x5851f42d\n"
"s_mul_i32 s1, s1, 0x4c957f2d\n"
"s_add_u32 s33, s33, s0\n"
"s_add_u32 s33, s33, s1\n"
"s_add_u32 s32, s32, 0xf767814f\n"
"s_add_u32 s33, s33, 0x14057b7e\n"
"s_cmp_ge_u32 s34, 4\n"
"s_cbranch_scc1 .Lk_idc_21\n"
"v_lshlrev_b32 v32, 16, v10\n"
"v_and_b32 v33, 0xffff0000, v10\n"
"v_lshlrev_b32 v34, 16, v11\n"
"v_and_b32 v35, 0xffff0000, v11\n"
"v_lshlrev_b32 v36, 16, v12\n"
"v_and_b32 v37, 0xffff0000, v12\n"
"v_lshlrev_b32 v38, 16, v13\n"
"v_and_b32 v39, 0xffff0000, v13\n"
"v_lshlrev_b32 v40, 16, v14\n"
"v_and_b32 v41, 0xffff0000, v14\n"
"v_lshlrev_b32 v42, 16, v16\n"
"v_and_b32 v43, 0xffff0000, v16\n"
"v_lshlrev_b32 v44, 16, v17\n"
"v_and_b32 v45, 0xffff0000, v17\n"
"v_lshlrev_b32 v46, 16, v18\n"
"v_and_b32 v47, 0xffff0000, v18\n"
"v_lshlrev_b32 v48, 16, v19\n"
"v_and_b32 v49, 0xffff0000, v19\n"
"v_lshlrev_b32 v50, 16, v20\n"
"v_and_b32 v51, 0xffff0000, v20\n"
"v_add_f32 v24, v32, v33\n"
"v_add_f32 v28, v42, v43\n"
"v_add_f32 v24, v24, v34\n"
"v_add_f32 v28, v28, v44\n"
"v_add_f32 v24, v24, v35\n"
"v_add_f32 v28, v28, v45\n"
"v_add_f32 v24, v24, v36\n"
"v_add_f32 v28, v28, v46\n"
"v_add_f32 v24, v24, v37\n"
"v_add_f32 v28, v28, v47\n"
"v_add_f32 v24, v24, v38\n"
"v_add_f32 v28, v28, v48\n"
"v_add_f32 v24, v24, v39\n"
"v_add_f32 v28, v28, v49\n"
"v_add_f32 v24, v24, v40\n"
"v_add_f32 v28, v28, v50\n"
"v_add_f32 v24, v24, v41\n"
"v_add_f32 v28, v28, v51\n"
"s_nop 0\n"
"v_add_f32_dpp v24, v24, v24 quad_perm:[1,0,3,2] row_mask:0xf bank_mask:0xf\n"
"v_add_f32_dpp v28, v28, v28 quad_perm:[1,0,3,2] row_mask:0xf bank_mask:0xf\n"
"s_nop 0\n"
"v_add_f32_dpp v24, v24, v24 quad_perm:[2,3,0,1] row_mask:0xf bank_mask:0xf\n"
"v_add_f32_dpp v28, v28, v28 quad_perm:[2,3,0,1] row_mask:0xf bank_mask:0xf\n"
"s_nop 0\n"
"v_add_f32_dpp v24, v24, v24 row_half_mirror row_mask:0xf bank_mask:0xf\n"
"v_add_f32_dpp v28, v28, v28 row_half_mirror row_mask:0xf bank_mask:0xf\n"
"s_nop 0\n"
"v_add_f32_dpp v24, v24, v24 row_mirror row_mask:0xf bank_mask:0xf\n"
"v_add_f32_dpp v28, v28, v28 row_mirror row_mask:0xf bank_mask:0xf\n"
"v_mov_b32 v22, v24\n"
"v_mov_b32 v23, v28\n"
"s_nop 0\n"
"v_permlane16_swap_b32 v24, v22\n"
"v_permlane16_swap_b32 v28, v23\n"
"v_add_f32 v24, v24, v22\n"
"v_add_f32 v28, v28, v23\n"
"v_mov_b32 v22, v24\n"
"v_mov_b32 v23, v28\n"
"s_nop 0\n"
"v_permlane32_swap_b32 v24, v22\n"
"v_permlane32_swap_b32 v28, v23\n"
"v_add_f32 v24, v24, v22\n"
"v_add_f32 v28, v28, v23\n"
"v_mul_f32 v24, 0x3acccccd, v24\n"
"v_mul_f32 v28, 0x3acccccd, v28\n"
"v_sub_f32 v32, v32, v24\n"
"v_sub_f32 v42, v42, v28\n"
"v_sub_f32 v33, v33, v24\n"
"v_sub_f32 v43, v43, v28\n"
"v_sub_f32 v34, v34, v24\n"
"v_sub_f32 v44, v44, v28\n"
"v_sub_f32 v35, v35, v24\n"
"v_sub_f32 v45, v45, v28\n"
"v_sub_f32 v36, v36, v24\n"
"v_sub_f32 v46, v46, v28\n"
"v_sub_f32 v37, v37, v24\n"
"v_sub_f32 v47, v47, v28\n"
"v_sub_f32 v38, v38, v24\n"
"v_sub_f32 v48, v48, v28\n"
"v_sub_f32 v39, v39, v24\n"
"v_sub_f32 v49, v49, v28\n"
"v_sub_f32 v40, v40, v24\n"
"v_sub_f32 v50, v50, v28\n"
"v_sub_f32 v41, v41, v24\n"
"v_sub_f32 v51, v51, v28\n"
"v_mul_f32 v26, v32, v32\n"
"v_mul_f32 v30, v42, v42\n"
"v_fmac_f32 v26, v33, v33\n"
"v_fmac_f32 v30, v43, v43\n"
"v_fmac_f32 v26, v34, v34\n"
"v_fmac_f32 v30, v44, v44\n"
"v_fmac_f32 v26, v35, v35\n"
"v_fmac_f32 v30, v45, v45\n"
"v_fmac_f32 v26, v36, v36\n"
"v_fmac_f32 v30, v46, v46\n"
"v_fmac_f32 v26, v37, v37\n"
"v_fmac_f32 v30, v47, v47\n"
"v_fmac_f32 v26, v38, v38\n"
"v_fmac_f32 v30, v48, v48\n"
"v_fmac_f32 v26, v39, v39\n"
"v_fmac_f32 v30, v49, v49\n"
"v_fmac_f32 v26, v40, v40\n"
"v_fmac_f32 v30, v50, v50\n"
"v_fmac_f32 v26, v41, v41\n"
"v_fmac_f32 v30, v51, v51\n"
"s_nop 0\n"
"v_add_f32_dpp v26, v26, v26 quad_perm:[1,0,3,2] row_mask:0xf bank_mask:0xf\n"
"v_add_f32_dpp v30, v30, v30 quad_perm:[1,0,3,2] row_mask:0xf bank_mask:0xf\n"
"s_nop 0\n"
"v_add_f32_dpp v26, v26, v26 quad_perm:[2,3,0,1] row_mask:0xf bank_mask:0xf\n"
"v_add_f32_dpp v30, v30, v30 quad_perm:[2,3,0,1] row_mask:0xf bank_mask:0xf\n"
"s_nop 0\n"
"v_add_f32_dpp v26, v26, v26 row_half_mirror row_mask:0xf bank_mask:0xf\n"
"v_add_f32_dpp v30, v30, v30 row_half_mirror row_mask:0xf bank_mask:0xf\n"
"s_nop 0\n"
"v_add_f32_dpp v26, v26, v26 row_mirror row_mask:0xf bank_mask:0xf\n"
"v_add_f32_dpp v30, v30, v30 row_mirror row_mask:0xf bank_mask:0xf\n"
"v_mov_b32 v22, v26\n"
"v_mov_b32 v23, v30\n"
"s_nop 0\n"
"v_permlane16_swap_b32 v26, v22\n"
"v_permlane16_swap_b32 v30, v23\n"
"v_add_f32 v26, v26, v22\n"
"v_add_f32 v30, v30, v23\n"
"v_mov_b32 v22, v26\n"
"v_mov_b32 v23, v30\n"
"s_nop 0\n"
"v_permlane32_swap_b32 v26, v22\n"
"v_permlane32_swap_b32 v30, v23\n"
"v_add_f32 v26, v26, v22\n"
"v_add_f32 v30, v30, v23\n"
"v_cmp_eq_u32 vcc, 0, v0\n"
"s_nop 1\n"
"v_cndmask_b32 v10, v28, v24, vcc\n"
"v_cndmask_b32 v12, v30, v26, vcc\n"
"v_mul_f32 v12, 0x3acccccd, v12\n"
"v_cvt_pk_bf16_f32 v12, v12, v12\n"
"v_and_b32 v12, 0xffff0000, v12\n"
"v_add_f32 v12, 0x3727c5ac, v12\n"
"v_cvt_pk_bf16_f32 v12, v12, v12\n"
"v_and_b32 v12, 0xffff0000, v12\n"
"v_sqrt_f32 v12, v12\n"
"v_cvt_pk_bf16_f32 v10, v10, v10\n"
"v_cvt_pk_bf16_f32 v12, v12, v12\n"
"s_mov_b32 s38, 0xffff\n"
"v_mov_b32 v11, s32\n"
"v_bfi_b32 v10, s38, v10, v11\n"
"v_bfi_b32 v12, s38, v12, v11\n"
"v_mov_b32 v11, s33\n"
"v_mov_b32 v13, s33\n"
"s_lshl_b32 s56, s42, 4\n"
"s_add_u32 s43, 0x301000, s56\n"
"v_cmp_gt_u32 vcc, 2, v0\n"
"v_mov_b32 v9, 0x7ff00000\n"
"s_nop 0\n"
"v_cndmask_b32 v9, v9, v8, vcc\n"
"buffer_store_dwordx4 v[10:13], v9, s[4:7], s43 offen sc1\n"
".Lk_idc_21:\n"
"s_waitcnt vmcnt(19)\n"
"v_mfma_f32_16x16x32_bf16 v[52:55], v[56:59], v[64:67], 0\n"
"s_waitcnt vmcnt(18)\n"
"v_mfma_f32_16x16x32_bf16 v[52:55], v[56:59], v[68:71], v[52:55]\n"
"s_waitcnt vmcnt(17)\n"
"v_mfma_f32_16x16x32_bf16 v[52:55], v[56:59], v[72:75], v[52:55]\n"
"s_waitcnt vmcnt(16)\n"
"v_mfma_f32_16x16x32_bf16 v[52:55], v[56:59], v[76:79], v[52:55]\n"
"s_waitcnt vmcnt(15)\n"
"v_mfma_f32_16x16x32_bf16 v[52:55], v[56:59], v[80:83], v[52:55]\n"
"s_waitcnt vmcnt(14)\n"
"v_mfma_f32_16x16x32_bf16 v[52:55], v[56:59], v[84:87], v[52:55]\n"
"s_waitcnt vmcnt(13)\n"
"v_mfma_f32_16x16x32_bf16 v[52:55], v[56:59], v[88:91], v[52:55]\n"
"s_waitcnt vmcnt(12)\n"
"v_mfma_f32_16x16x32_bf16 v[52:55], v[56:59], v[92:95], v[52:55]\n"
"s_waitcnt vmcnt(11)\n"
"v_mfma_f32_16x16x32_bf16 v[52:55], v[56:59], v[96:99], v[52:55]\n"
"s_waitcnt vmcnt(10)\n"
"v_mfma_f32_16x16x32_bf16 v[52:55], v[56:59], v[100:103], v[52:55]\n"
"s_waitcnt vmcnt(9)\n"
"v_mfma_f32_16x16x32_bf16 v[52:55], v[56:59], v[104:107], v[52:55]\n"
"s_waitcnt vmcnt(8)\n"
"v_mfma_f32_16x16x32_bf16 v[52:55], v[56:59], v[108:111], v[52:55]\n"
"s_waitcnt vmcnt(7)\n"
"v_mfma_f32_16x16x32_bf16 v[52:55], v[56:59], v[112:115], v[52:55]\n"
"s_waitcnt vmcnt(6)\n"
"v_mfma_f32_16x16x32_bf16 v[52:55], v[56:59], v[116:119], v[52:55]\n"
"s_waitcnt vmcnt(5)\n"
"v_mfma_f32_16x16x32_bf16 v[52:55], v[56:59], v[120:123], v[52:55]\n"
"s_waitcnt vmcnt(4)\n"
"v_mfma_f32_16x16x32_bf16 v[52:55], v[56:59], v[124:127], v[52:55]\n"
"s_waitcnt vmcnt(3)\n"
"v_mfma_f32_16x16x32_bf16 v[52:55], v[56:59], v[128:131], v[52:55]\n"
"s_waitcnt vmcnt(2)\n"
"v_mfma_f32_16x16x32_bf16 v[52:55], v[56:59], v[132:135], v[52:55]\n"
"s_waitcnt vmcnt(1)\n"
"v_mfma_f32_16x16x32_bf16 v[52:55], v[56:59], v[136:139], v[52:55]\n"
"s_waitcnt vmcnt(0)\n"
"v_mfma_f32_16x16x32_bf16 v[52:55], v[56:59], v[140:143], v[52:55]\n"
"s_lshl_b32 s56, s34, 10\n"
"s_add_u32 s57, 0x8000, s56\n"
"v_lshrrev_b32 v2, 5, v0\n"
"v_lshlrev_b32 v3, 9, v2\n"
"v_add_u32 v4, s57, v3\n"
"v_and_b32 v5, 15, v0\n"
"v_lshlrev_b32 v2, 5, v5\n"
"v_add_u32 v3, v4, v2\n"
"v_lshrrev_b32 v5, 4, v0\n"
"v_and_b32 v4, 1, v5\n"
"v_lshlrev_b32 v2, 4, v4\n"
"v_add_u32 v60, v3, v2\n"
"ds_write_b128 v60, v[52:55]\n"
"s_waitcnt lgkmcnt(0)\n"
"s_barrier\n"
"s_cmp_ge_u32 s34, 4\n"
"s_cbranch_scc1 .Lk_ide_22\n"
"s_cmp_ge_u32 s3, 2\n"
"s_cbranch_scc1 .Lk_idx_23\n"
"s_lshl_b32 s56, s3, 6\n"
"v_add_u32 v2, s56, v0\n"
"v_lshlrev_b32 v60, 2, v2\n"
"ds_read_b32 v64, v60 offset:32768\n"
"ds_read_b32 v65, v60 offset:33792\n"
"ds_read_b32 v66, v60 offset:34816\n"
"ds_read_b32 v67, v60 offset:35840\n"
"ds_read_b32 v68, v60 offset:36864\n"
"ds_read_b32 v69, v60 offset:37888\n"
"ds_read_b32 v70, v60 offset:38912\n"
"ds_read_b32 v71, v60 offset:39936\n"
"s_waitcnt lgkmcnt(6)\n"
"v_add_f32 v64, v64, v65\n"
"s_waitcnt lgkmcnt(5)\n"
"v_add_f32 v64, v64, v66\n"
"s_waitcnt lgkmcnt(4)\n"
"v_add_f32 v64, v64, v67\n"
"s_waitcnt lgkmcnt(3)\n"
"v_add_f32 v64, v64, v68\n"
"s_waitcnt lgkmcnt(2)\n"
"v_add_f32 v64, v64, v69\n"
"s_waitcnt lgkmcnt(1)\n"
"v_add_f32 v64, v64, v70\n"
"s_waitcnt lgkmcnt(0)\n"
"v_add_f32 v64, v64, v71\n"
"s_mov_b32 s38, 0xffff\n"
"v_mov_b32 v11, s32\n"
"v_lshrrev_b32 v12, 16, v64\n"
"v_bfi_b32 v10, s38, v64, v11\n"
"v_bfi_b32 v12, s38, v12, v11\n"
"v_mov_b32 v11, s33\n"
"v_mov_b32 v13, s33\n"
"v_lshlrev_b32 v60, 2, v60\n"
"s_lshl_b32 s56, s40, 11\n"
"s_add_u32 s42, 0x309000, s56\n"
"s_cmp_lt_u32 s40, 15\n"
"s_cselect_b32 s42, s42, 0x7ff00000\n"
"buffer_store_dwordx4 v[10:13], v60, s[4:7], s42 offen sc1\n"
".Lk_idx_23:\n"
"s_lshl_b32 s56, s35, 2\n"
"s_lshr_b32 s57, s36, 3\n"
"s_add_u32 s40, s56, s57\n"
"s_and_b32 s56, s35, 0\n"
"s_lshl_b32 s57, s56, 3\n"
"s_and_b32 s58, s36, 7\n"
"s_add_u32 s41, s57, s58\n"
"s_lshl_b32 s56, s35, 2\n"
"s_lshr_b32 s57, s36, 3\n"
"s_add_u32 s62, s56, s57\n"
"s_and_b32 s56, s35, 0\n"
"s_lshl_b32 s57, s56, 3\n"
"s_and_b32 s58, s36, 7\n"
"s_add_u32 s63, s57, s58\n"
"v_lshlrev_b32 v14, 4, v0\n"
"s_mul_i32 s56, s40, 0x14000\n"
"s_add_u32 s57, 0x311000, s56\n"
"s_lshl_b32 s58, s41, 1\n"
"s_lshr_b32 s59, s34, 2\n"
"s_add_u32 s60, s58, s59\n"
"s_mul_i32 s61, s60, 0x1400\n"
"s_add_u32 s20, s57, s61\n"
"v_lshrrev_b32 v2, 5, v0\n"
"v_lshlrev_b32 v3, 11, v2\n"
"v_and_b32 v4, 31, v0\n"
"v_lshlrev_b32 v5, 4, v4\n"
"v_add_u32 v10, v3, v5\n"
"s_lshl_b32 s56, s3, 9\n"
"v_add_u32 v6, s56, v10\n"
"s_lshl_b32 s56, s3, 5\n"
"v_and_b32 v2, 31, v0\n"
"v_add_u32 v3, s56, v2\n"
"v_lshlrev_b32 v4, 3, v3\n"
"v_add_u32 v7, 0x1000, v4\n"
"v_and_b32 v2, 31, v0\n"
"v_lshlrev_b32 v3, 3, v2\n"
"v_add_u32 v11, 0x1000, v3\n"
"v_lshrrev_b32 v2, 5, v0\n"
"v_mul_u32_u24 v3, 0x140, v2\n"
"v_add_u32 v4, 0x1400, v3\n"
"v_and_b32 v5, 19, v0\n"
"v_and_b32 v2, 4, v0\n"
"v_lshlrev_b32 v3, 1, v2\n"
"v_or_b32 v2, v5, v3\n"
"v_and_b32 v5, 8, v0\n"
"v_lshrrev_b32 v3, 1, v5\n"
"v_or_b32 v5, v2, v3\n"
"v_lshlrev_b32 v2, 4, v5\n"
"v_add_u32 v12, v4, v2\n"
"s_lshl_b32 s17, s34, 10\n"
"s_mul_i32 s56, s40, 0x28000\n"
"s_add_u32 s57, 0x591000, s56\n"
"s_add_u32 s16, s57, s17\n"
"v_mov_b32 v13, 0xdddd\n"
"s_mov_b32 s18, 0xfffff\n"
"s_mov_b32 s19, 0xfffff\n"
"v_and_b32 v2, 27, v0\n"
"v_cmp_eq_u32 vcc, 24, v2\n"
"v_mov_b32 v2, 0x3f803f80\n"
"s_nop 0\n"
"v_cndmask_b32 v80, 0, v2, vcc\n"
"v_cndmask_b32 v81, 0, v2, vcc\n"
"v_cndmask_b32 v82, 0, v2, vcc\n"
"v_cndmask_b32 v83, 0, v2, vcc\n"
"v_cndmask_b32 v84, 0, v2, vcc\n"
"v_cndmask_b32 v85, 0, v2, vcc\n"
"v_cndmask_b32 v86, 0, v2, vcc\n"
"v_cndmask_b32 v87, 0, v2, vcc\n"
"v_cndmask_b32 v88, 0, v2, vcc\n"
"v_cndmask_b32 v89, 0, v2, vcc\n"
"v_cndmask_b32 v90, 0, v2, vcc\n"
"v_cndmask_b32 v91, 0, v2, vcc\n"
"v_cndmask_b32 v92, 0, v2, vcc\n"
"v_cndmask_b32 v93, 0, v2, vcc\n"
"v_cndmask_b32 v94, 0, v2, vcc\n"
"v_cndmask_b32 v95, 0, v2, vcc\n"
"v_mov_b32 v48, 0\n"
"v_mov_b32 v49, 0\n"
"v_mov_b32 v50, 0\n"
"v_mov_b32 v51, 0\n"
"v_mov_b32 v52, 0\n"
"v_mov_b32 v53, 0\n"
"v_mov_b32 v54, 0\n"
"v_mov_b32 v55, 0\n"
"v_mov_b32 v56, 0\n"
"v_mov_b32 v57, 0\n"
"v_mov_b32 v58, 0\n"
"v_mov_b32 v59, 0\n"
"v_mov_b32 v60, 0\n"
"v_mov_b32 v61, 0\n"
"v_mov_b32 v62, 0\n"
"v_mov_b32 v63, 0\n"
"s_lshl_b32 s56, s34, 3\n"
"s_and_b32 s57, s63, 7\n"
"s_add_u32 s58, s56, s57\n"
"s_mul_i32 s59, s58, 0xa000\n"
"s_add_u32 s13, 0x1762800, s59\n"
"s_lshl_b32 s56, s34, 3\n"
"s_add_u32 s38, s56, s63\n"
"s_cmp_lt_u32 s38, 54\n"
"s_cselect_b32 s13, s13, 0x7ff00000\n"
"s_cmp_eq_u32 s34, 7\n"
"s_movk_i32 s24, 0x800\n"
"s_cselect_b32 s24, 0x5000, s24\n"
"s_and_b32 s56, s63, 7\n"
"s_mul_i32 s57, s56, 0x19000\n"
"s_add_u32 s58, 0x1691000, s57\n"
"s_lshl_b32 s59, s34, 10\n"
"s_add_u32 s14, s58, s59\n"
"s_lshl_b32 s15, s34, 10\n"
"s_lshl_b32 s56, s41, 2\n"
"s_lshr_b32 s57, s34, 1\n"
"s_add_u32 s58, s56, s57\n"
"s_mul_i32 s59, s58, 0x14000\n"
"s_add_u32 s22, 0xa91000, s59\n"
"s_lshl_b32 s56, s41, 5\n"
"s_add_u32 s57, s56, s40\n"
"s_lshl_b32 s58, s57, 10\n"
"s_add_u32 s23, 0x81000, s58\n"
"s_barrier\n"
"s_branch .Lk_p1done_4\n"
".Lk_ide_22:\n"
".Lk_rpst_6:\n"
"s_cmp_lt_u32 s2, 108\n"
"s_cbranch_scc1 .Lk_rpw1_25\n"
"s_cmp_ge_u32 s2, 216\n"
"s_cbranch_scc1 .Lk_rpwo_26\n"
"s_sub_u32 s56, s2, 0x6c\n"
"s_lshr_b32 s57, s56, 1\n"
"s_lshl_b32 s58, s57, 5\n"
"s_lshl_b32 s59, s3, 3\n"
"s_add_u32 s60, s58, s59\n"
"v_lshrrev_b32 v2, 3, v0\n"
"v_add_u32 v8, s60, v2\n"
"s_mov_b64 s[52:53], s[24:25]\n"
"s_mov_b64 s[54:55], s[26:27]\n"
"s_branch .Lk_rpp_27\n"
".Lk_rpwo_26:\n"
"s_sub_u32 s56, s2, 0xd8\n"
"s_lshr_b32 s45, s56, 1\n"
"s_mul_i32 s56, s45, 40\n"
"s_lshr_b32 s57, s3, 1\n"
"s_mul_i32 s58, s57, 20\n"
"s_add_u32 s59, s56, s58\n"
"s_and_b32 s60, s3, 1\n"
"s_lshl_b32 s61, s60, 3\n"
"s_add_u32 s46, s59, s61\n"
"s_mul_i32 s56, s45, 0xa0\n"
"s_mul_i32 s57, s3, 40\n"
"s_add_u32 s58, s56, s57\n"
"s_sub_u32 s47, s58, 0x9f0\n"
"s_cmp_lt_u32 s45, 16\n"
"s_cselect_b32 s46, s46, s47\n"
"s_cselect_b32 s47, 4, 20\n"
"v_lshrrev_b32 v2, 5, v0\n"
"v_mul_u32_u24 v3, s47, v2\n"
"v_add_u32 v4, s46, v3\n"
"v_lshrrev_b32 v5, 3, v0\n"
"v_and_b32 v2, 3, v5\n"
"v_add_u32 v8, v4, v2\n"
"s_mov_b64 s[52:53], s[16:17]\n"
"s_mov_b64 s[54:55], s[18:19]\n"
".Lk_rpp_27:\n"
"v_mul_u32_u24 v2, 0x500, v8\n"
"s_and_b32 s56, s2, 1\n"
"s_mul_i32 s57, s56, 0x280\n"
"v_add_u32 v3, s57, v2\n"
"v_and_b32 v4, 7, v0\n"
"v_lshlrev_b32 v5, 4, v4\n"
"v_add_u32 v8, v3, v5\n"
"s_movk_i32 s44, 128\n"
"s_branch .Lk_rpd_28\n"
".Lk_rpw1_25:\n"
"s_mov_b64 s[52:53], s[20:21]\n"
"s_mov_b64 s[54:55], s[22:23]\n"
"s_and_b32 s56, s2, 3\n"
"s_mul_i32 s57, s56, 0xa0\n"
"s_mul_i32 s58, s3, 40\n"
"s_add_u32 s59, s57, s58\n"
"v_lshrrev_b32 v2, 3, v0\n"
"v_add_u32 v3, s59, v2\n"
"v_mul_u32_u24 v4, 0xd80, v3\n"
"s_lshr_b32 s60, s2, 2\n"
"s_lshl_b32 s61, s60, 7\n"
"v_add_u32 v5, s61, v4\n"
"v_and_b32 v2, 7, v0\n"
"v_lshlrev_b32 v3, 4, v2\n"
"v_add_u32 v8, v5, v3\n"
"s_movk_i32 s44, 27648\n"
".Lk_rpd_28:\n"
"v_mov_b32 v205, v8\n"
"s_mov_b32 s49, s44\n"
"s_mul_i32 s56, s3, 0x1400\n"
"s_add_u32 s42, s56, 0x22000\n"
"s_mov_b32 s43, 0\n"
"s_cmp_lt_u32 s2, 108\n"
"s_cbranch_scc1 .Lk_rpw1_29\n"
"s_and_b32 s56, s2, 1\n"
"s_lshl_b32 s57, s56, 2\n"
"s_add_u32 s58, s57, s3\n"
"s_mul_i32 s59, s58, 0x43800\n"
"s_add_u32 s60, 0x1975000, s59\n"
"s_sub_u32 s61, s2, 0x6c\n"
"s_lshr_b32 s56, s61, 1\n"
"s_mul_i32 s57, s56, 0x1400\n"
"s_add_u32 s50, s60, s57\n"
"s_and_b32 s56, s2, 1\n"
"s_lshl_b32 s57, s56, 2\n"
"s_add_u32 s58, s57, s3\n"
"s_mul_i32 s59, s58, 0x19000\n"
"s_add_u32 s60, 0x160a000, s59\n"
"s_lshr_b32 s61, s2, 1\n"
"s_mul_i32 s56, s61, 0x1400\n"
"s_add_u32 s51, s60, s56\n"
"s_cmp_ge_u32 s2, 216\n"
"s_cselect_b32 s50, s51, s50\n"
"v_lshrrev_b32 v2, 4, v0\n"
"v_mul_u32_u24 v3, 0x1400, v2\n"
"v_add_u32 v4, 0x22000, v3\n"
"v_and_b32 v5, 15, v0\n"
"v_lshrrev_b32 v2, 2, v5\n"
"v_lshlrev_b32 v3, 7, v2\n"
"v_add_u32 v8, v4, v3\n"
"s_mul_i32 s56, s3, 0xa0\n"
"v_and_b32 v2, 3, v0\n"
"v_lshlrev_b32 v3, 4, v2\n"
"v_add_u32 v9, s56, v3\n"
"v_lshrrev_b32 v2, 7, v9\n"
"v_lshlrev_b32 v3, 10, v2\n"
"v_add_u32 v4, v8, v3\n"
"v_and_b32 v5, 0x7f, v9\n"
"v_add_u32 v206, v4, v5\n"
"s_add_u32 s44, s50, 0\n"
"s_mul_i32 s56, s3, 0xa0\n"
"s_add_u32 s57, s56, 8\n"
"s_cbranch_execz .Lk_tramp\n"
"s_nop 0\n"
"v_and_b32 v2, 3, v0\n"
"v_lshlrev_b32 v3, 4, v2\n"
"v_add_u32 v9, s57, v3\n"
"v_lshrrev_b32 v2, 7, v9\n"
"v_lshlrev_b32 v3, 10, v2\n"
"v_add_u32 v4, v8, v3\n"
"v_and_b32 v5, 0x7f, v9\n"
"v_add_u32 v207, v4, v5\n"
"s_add_u32 s45, s50, 1024\n"
"s_mul_i32 s56, s3, 0xa0\n"
"s_add_u32 s57, s56, 64\n"
"v_and_b32 v2, 3, v0\n"
"v_lshlrev_b32 v3, 4, v2\n"
"v_add_u32 v9, s57, v3\n"
"v_lshrrev_b32 v2, 7, v9\n"
"v_lshlrev_b32 v3, 10, v2\n"
"v_add_u32 v4, v8, v3\n"
"v_and_b32 v5, 0x7f, v9\n"
"v_add_u32 v208, v4, v5\n"
"s_add_u32 s46, s50, 3072\n"
"s_mul_i32 s56, s3, 0xa0\n"
"s_add_u32 s57, s56, 0x48\n"
"v_and_b32 v2, 3, v0\n"
"v_lshlrev_b32 v3, 4, v2\n"
"v_add_u32 v9, s57, v3\n"
"v_lshrrev_b32 v2, 7, v9\n"
"v_lshlrev_b32 v3, 10, v2\n"
"v_add_u32 v4, v8, v3\n"
"v_and_b32 v5, 0x7f, v9\n"
"v_add_u32 v209, v4, v5\n"
"s_add_u32 s47, s50, 4096\n"
"s_mul_i32 s56, s3, 0xa0\n"
"s_add_u32 s57, s56, 0x80\n"
"v_and_b32 v2, 3, v0\n"
"v_lshlrev_b32 v3, 3, v2\n"
"v_add_u32 v9, s57, v3\n"
"v_lshrrev_b32 v2, 7, v9\n"
"v_lshlrev_b32 v3, 10, v2\n"
"v_add_u32 v4, v8, v3\n"
"v_and_b32 v5, 0x7f, v9\n"
"v_add_u32 v210, v4, v5\n"
"s_add_u32 s48, s50, 2048\n"
"s_branch .Lk_rpd_30\n"
".Lk_rpw1_29:\n"
"s_lshr_b32 s56, s3, 1\n"
"s_lshl_b32 s57, s56, 6\n"
"s_add_u32 s58, 0x22000, s57\n"
"s_and_b32 s59, s3, 1\n"
"s_lshl_b32 s60, s59, 3\n"
"s_add_u32 s61, s58, s60\n"
"v_lshrrev_b32 v2, 4, v0\n"
"v_lshlrev_b32 v3, 3, v2\n"
"v_and_b32 v4, 15, v0\n"
"v_lshrrev_b32 v5, 2, v4\n"
"v_add_u32 v2, v3, v5\n"
"v_lshlrev_b32 v4, 7, v2\n"
"v_add_u32 v3, s61, v4\n"
"v_and_b32 v5, 3, v0\n"
"v_lshlrev_b32 v2, 4, v5\n"
"v_add_u32 v206, v3, v2\n"
"s_lshr_b32 s56, s2, 2\n"
"s_lshl_b32 s57, s56, 1\n"
"s_lshr_b32 s58, s3, 1\n"
"s_add_u32 s59, s57, s58\n"
"s_mul_i32 s60, s59, 0xa000\n"
"s_add_u32 s61, 0x1759000, s60\n"
"s_and_b32 s56, s2, 3\n"
"s_mul_i32 s57, s56, 10\n"
"s_and_b32 s58, s3, 1\n"
"s_add_u32 s59, s57, s58\n"
"s_lshl_b32 s60, s59, 10\n"
"s_add_u32 s50, s61, s60\n"
"v_add_u32 v207, 0x1000, v206\n"
"v_add_u32 v208, 0x2000, v206\n"
"v_add_u32 v209, 0x3000, v206\n"
"v_add_u32 v210, 0x4000, v206\n"
"s_add_u32 s44, s50, 0\n"
"s_add_u32 s45, s50, 2048\n"
"s_add_u32 s46, s50, 4096\n"
"s_add_u32 s47, s50, 6144\n"
"s_add_u32 s48, s50, 8192\n"
".Lk_rpd_30:\n"
"s_lshl_b32 s56, s35, 2\n"
"s_lshr_b32 s57, s36, 3\n"
"s_add_u32 s40, s56, s57\n"
"s_and_b32 s56, s35, 0\n"
"s_lshl_b32 s57, s56, 3\n"
"s_and_b32 s58, s36, 7\n"
"s_add_u32 s41, s57, s58\n"
"s_lshl_b32 s56, s35, 2\n"
"s_lshr_b32 s57, s36, 3\n"
"s_add_u32 s62, s56, s57\n"
"s_and_b32 s56, s35, 0\n"
"s_lshl_b32 s57, s56, 3\n"
"s_and_b32 s58, s36, 7\n"
"s_add_u32 s63, s57, s58\n"
"v_lshlrev_b32 v14, 4, v0\n"
"s_mul_i32 s56, s40, 0x14000\n"
"s_add_u32 s57, 0x311000, s56\n"
"s_lshl_b32 s58, s41, 1\n"
"s_lshr_b32 s59, s34, 2\n"
"s_add_u32 s60, s58, s59\n"
"s_mul_i32 s61, s60, 0x1400\n"
"s_add_u32 s20, s57, s61\n"
"v_lshrrev_b32 v2, 5, v0\n"
"v_lshlrev_b32 v3, 11, v2\n"
"v_and_b32 v4, 31, v0\n"
"v_lshlrev_b32 v5, 4, v4\n"
"v_add_u32 v10, v3, v5\n"
"s_lshl_b32 s56, s3, 9\n"
"v_add_u32 v6, s56, v10\n"
"s_lshl_b32 s56, s3, 5\n"
"v_and_b32 v2, 31, v0\n"
"v_add_u32 v3, s56, v2\n"
"v_lshlrev_b32 v4, 3, v3\n"
"v_add_u32 v7, 0x1000, v4\n"
"v_and_b32 v2, 31, v0\n"
"v_lshlrev_b32 v3, 3, v2\n"
"v_add_u32 v11, 0x1000, v3\n"
"v_lshrrev_b32 v2, 5, v0\n"
"v_mul_u32_u24 v3, 0x140, v2\n"
"v_add_u32 v4, 0x1400, v3\n"
"v_and_b32 v5, 19, v0\n"
"v_and_b32 v2, 4, v0\n"
"v_lshlrev_b32 v3, 1, v2\n"
"v_or_b32 v2, v5, v3\n"
"v_and_b32 v5, 8, v0\n"
"v_lshrrev_b32 v3, 1, v5\n"
"v_or_b32 v5, v2, v3\n"
"v_lshlrev_b32 v2, 4, v5\n"
"v_add_u32 v12, v4, v2\n"
"s_lshl_b32 s17, s34, 10\n"
"s_mul_i32 s56, s40, 0x28000\n"
"s_add_u32 s57, 0x591000, s56\n"
"s_add_u32 s16, s57, s17\n"
"v_mov_b32 v13, 0xdddd\n"
"s_mov_b32 s18, 0xfffff\n"
"s_mov_b32 s19, 0xfffff\n"
"v_and_b32 v2, 27, v0\n"
"v_cmp_eq_u32 vcc, 24, v2\n"
"v_mov_b32 v2, 0x3f803f80\n"
"s_nop 0\n"
"v_cndmask_b32 v80, 0, v2, vcc\n"
"v_cndmask_b32 v81, 0, v2, vcc\n"
"v_cndmask_b32 v82, 0, v2, vcc\n"
"v_cndmask_b32 v83, 0, v2, vcc\n"
"v_cndmask_b32 v84, 0, v2, vcc\n"
"v_cndmask_b32 v85, 0, v2, vcc\n"
"v_cndmask_b32 v86, 0, v2, vcc\n"
"v_cndmask_b32 v87, 0, v2, vcc\n"
"v_cndmask_b32 v88, 0, v2, vcc\n"
"v_cndmask_b32 v89, 0, v2, vcc\n"
"v_cndmask_b32 v90, 0, v2, vcc\n"
"v_cndmask_b32 v91, 0, v2, vcc\n"
"v_cndmask_b32 v92, 0, v2, vcc\n"
"v_cndmask_b32 v93, 0, v2, vcc\n"
"v_cndmask_b32 v94, 0, v2, vcc\n"
"v_cndmask_b32 v95, 0, v2, vcc\n"
"v_mov_b32 v48, 0\n"
"v_mov_b32 v49, 0\n"
"v_mov_b32 v50, 0\n"
"v_mov_b32 v51, 0\n"
"v_mov_b32 v52, 0\n"
"v_mov_b32 v53, 0\n"
"v_mov_b32 v54, 0\n"
"v_mov_b32 v55, 0\n"
"v_mov_b32 v56, 0\n"
"v_mov_b32 v57, 0\n"
"v_mov_b32 v58, 0\n"
"v_mov_b32 v59, 0\n"
"v_mov_b32 v60, 0\n"
"v_mov_b32 v61, 0\n"
"v_mov_b32 v62, 0\n"
"v_mov_b32 v63, 0\n"
"s_lshl_b32 s56, s34, 3\n"
"s_and_b32 s57, s63, 7\n"
"s_add_u32 s58, s56, s57\n"
"s_mul_i32 s59, s58, 0xa000\n"
"s_add_u32 s13, 0x1762800, s59\n"
"s_lshl_b32 s56, s34, 3\n"
"s_add_u32 s38, s56, s63\n"
"s_cmp_lt_u32 s38, 54\n"
"s_cselect_b32 s13, s13, 0x7ff00000\n"
"s_cmp_eq_u32 s34, 7\n"
"s_movk_i32 s24, 0x800\n"
"s_cselect_b32 s24, 0x5000, s24\n"
"s_and_b32 s56, s63, 7\n"
"s_mul_i32 s57, s56, 0x19000\n"
"s_add_u32 s58, 0x1691000, s57\n"
"s_lshl_b32 s59, s34, 10\n"
"s_add_u32 s14, s58, s59\n"
"s_lshl_b32 s15, s34, 10\n"
"s_lshl_b32 s56, s41, 2\n"
"s_lshr_b32 s57, s34, 1\n"
"s_add_u32 s58, s56, s57\n"
"s_mul_i32 s59, s58, 0x14000\n"
"s_add_u32 s22, 0xa91000, s59\n"
"s_lshl_b32 s56, s41, 5\n"
"s_add_u32 s57, s56, s40\n"
"s_lshl_b32 s58, s57, 10\n"
"s_add_u32 s23, 0x81000, s58\n"
"v_lshrrev_b32 v2, 5, v0\n"
"v_mul_u32_u24 v3, 5, v2\n"
"v_add_u32 v4, 5, v3\n"
"s_mul_i32 s56, s40, 20\n"
"v_lshrrev_b32 v5, 4, v0\n"
"v_and_b32 v2, 1, v5\n"
"v_mul_u32_u24 v3, 19, v2\n"
"v_add_u32 v5, s56, v3\n"
"v_lshrrev_b32 v2, 7, v5\n"
"v_add_u32 v3, v4, v2\n"
"v_lshlrev_b32 v5, 4, v3\n"
"v_and_b32 v4, 15, v0\n"
"v_add_u32 v2, v5, v4\n"
"v_lshlrev_b32 v20, 10, v2\n"
"s_mul_i32 s56, s40, 20\n"
"v_and_b32 v2, 1, v0\n"
"v_mul_u32_u24 v3, 19, v2\n"
"v_add_u32 v4, s56, v3\n"
"v_lshrrev_b32 v5, 7, v4\n"
"v_lshlrev_b32 v2, 4, v5\n"
"s_lshl_b32 s57, s41, 1\n"
"v_add_u32 v3, s57, v2\n"
"v_lshrrev_b32 v4, 1, v0\n"
"v_and_b32 v5, 1, v4\n"
"v_add_u32 v2, v3, v5\n"
"v_lshlrev_b32 v21, 10, v2\n"
"s_mov_b32 s21, 4096\n"
"s_lshl_b32 s38, s3, 12\n"
"s_add_u32 s38, s38, ((.Lk_bh-.Lk_top)&0xfffff000)+36864\n"
"s_min_u32 s38, s38, (.Lk_last-.Lk_top)&0xfffff000\n"
"s_getpc_b64 s[0:1]\n"
".Lk_ith_31:\n"
"s_sub_u32 s0, s0, .Lk_ith_31-.Lk_top\n"
"s_subb_u32 s1, s1, 0\n"
"s_add_u32 s0, s0, s38\n"
"s_addc_u32 s1, s1, 0\n"
"s_mov_b64 exec, 0\n"
"s_swappc_b64 s[38:39], s[0:1]\n"
"s_mov_b64 exec, -1\n"
"s_barrier\n"
"s_lshl_b32 s38, s3, 12\n"
"s_add_u32 s38, s38, ((.Lk_bh-.Lk_top)&0xfffff000)+4096\n"
"s_min_u32 s38, s38, (.Lk_last-.Lk_top)&0xfffff000\n"
"s_getpc_b64 s[0:1]\n"
".Lk_ith_32:\n"
"s_sub_u32 s0, s0, .Lk_ith_32-.Lk_top\n"
"s_subb_u32 s1, s1, 0\n"
"s_add_u32 s0, s0, s38\n"
"s_addc_u32 s1, s1, 0\n"
"s_mov_b64 exec, 0\n"
"s_swappc_b64 s[38:39], s[0:1]\n"
"s_mov_b64 exec, -1\n"
"s_lshl_b32 s38, s3, 12\n"
"s_add_u32 s38, s38, ((.Lk_bh-.Lk_top)&0xfffff000)+20480\n"
"s_min_u32 s38, s38, (.Lk_last-.Lk_top)&0xfffff000\n"
"s_getpc_b64 s[0:1]\n"
".Lk_ith_33:\n"
"s_sub_u32 s0, s0, .Lk_ith_33-.Lk_top\n"
"s_subb_u32 s1, s1, 0\n"
"s_add_u32 s0, s0, s38\n"
"s_addc_u32 s1, s1, 0\n"
"s_mov_b64 exec, 0\n"
"s_swappc_b64 s[38:39], s[0:1]\n"
"s_mov_b64 exec, -1\n"
".Lk_p1done_4:\n"
".Lk_bh:\n"
"s_cmp_ge_u32 s34, 4\n"
"s_cbranch_scc1 .Lk_late_34\n"
"s_barrier\n"
"s_mov_b32 m0, s17\n"
"s_nop 0\n"
"buffer_load_dwordx4 v14, s[4:7], s16 offen sc1 lds\n"
"s_add_u32 s16, s16, 0x2000\n"
"s_add_u32 s17, s17, 0x2000\n"
"buffer_load_dwordx4 v[64:67], v6, s[4:7], s20 offen sc1\n"
"buffer_load_dwordx2 v[68:69], v7, s[4:7], s20 offen sc1\n"
"s_mov_b32 m0, s17\n"
"s_nop 0\n"
"buffer_load_dwordx4 v14, s[4:7], s16 offen sc1 lds\n"
"s_add_u32 s16, s16, 0x2000\n"
"s_add_u32 s17, s17, 0x2000\n"
"v_cmp_gt_u32 vcc, 32, v0\n"
"s_waitcnt vmcnt(1)\n"
"s_nop 0\n"
"v_cndmask_b32 v68, 0, v68, vcc\n"
"v_cndmask_b32 v69, 0, v69, vcc\n"
"s_waitcnt vmcnt(0)\n"
"s_barrier\n"
"ds_read_b64 v[70:71], v11\n"
"ds_read_b128 v[72:75], v10\n"
"ds_read_b64 v[8:9], v11 offset:256\n"
"ds_read_b128 v[76:79], v10 offset:512\n"
"s_setprio 2\n"
"s_mov_b64 exec, s[18:19]\n"
"ds_read_b128 v[80:83], v12\n"
"ds_read_b128 v[84:87], v12 offset:640\n"
"s_mov_b64 exec, -1\n"
"s_waitcnt lgkmcnt(5)\n"
"v_mfma_f32_32x32x8_bf16 v[16:31], v[70:71], v[68:69], 0\n"
"ds_read_b64 v[70:71], v11 offset:512\n"
"s_mov_b32 m0, s17\n"
"s_nop 0\n"
"buffer_load_dwordx4 v14, s[4:7], s16 offen sc1 lds\n"
"s_add_u32 s16, s16, 0x2000\n"
"s_add_u32 s17, s17, 0x2000\n"
"s_waitcnt lgkmcnt(5)\n"
"v_mfma_f32_32x32x16_bf16 v[16:31], v[72:75], v[64:67], v[16:31]\n"
"ds_read_b128 v[72:75], v10 offset:1024\n"
"s_mov_b64 exec, s[18:19]\n"
"ds_read_b128 v[88:91], v12 offset:1280\n"
"ds_read_b128 v[92:95], v12 offset:1920\n"
"s_mov_b64 exec, -1\n"
"s_waitcnt lgkmcnt(7)\n"
"v_mfma_f32_32x32x8_bf16 v[32:47], v[8:9], v[68:69], 0\n"
"ds_read_b64 v[8:9], v11 offset:768\n"
"s_mov_b32 m0, s17\n"
"s_nop 0\n"
"buffer_load_dwordx4 v14, s[4:7], s16 offen sc1 lds\n"
"s_add_u32 s16, s16, 0x2000\n"
"s_add_u32 s17, s17, 0x2000\n"
"s_waitcnt lgkmcnt(7)\n"
"v_mfma_f32_32x32x16_bf16 v[32:47], v[76:79], v[64:67], v[32:47]\n"
"ds_read_b128 v[76:79], v10 offset:1536\n"
"s_mov_b32 m0, s17\n"
"s_nop 0\n"
"buffer_load_dwordx4 v14, s[4:7], s16 offen sc1 lds\n"
"s_add_u32 s16, s16, 0x2000\n"
"s_add_u32 s17, s17, 0x2000\n"
"s_setprio 0\n"
"v_exp_f32 v16, v16\n"
"v_exp_f32 v17, v17\n"
"s_setprio 1\n"
"v_exp_f32 v18, v18\n"
"v_exp_f32 v19, v19\n"
"v_exp_f32 v20, v20\n"
"v_exp_f32 v21, v21\n"
"v_exp_f32 v22, v22\n"
"v_exp_f32 v23, v23\n"
"v_exp_f32 v24, v24\n"
"v_exp_f32 v25, v25\n"
"v_exp_f32 v26, v26\n"
"v_exp_f32 v27, v27\n"
"v_exp_f32 v28, v28\n"
"v_exp_f32 v29, v29\n"
"v_exp_f32 v30, v30\n"
"v_exp_f32 v31, v31\n"
"v_exp_f32 v32, v32\n"
"v_exp_f32 v33, v33\n"
"v_exp_f32 v34, v34\n"
"v_exp_f32 v35, v35\n"
"v_exp_f32 v36, v36\n"
"v_exp_f32 v37, v37\n"
"v_exp_f32 v38, v38\n"
"v_exp_f32 v39, v39\n"
"v_exp_f32 v40, v40\n"
"v_exp_f32 v41, v41\n"
"v_exp_f32 v42, v42\n"
"v_exp_f32 v43, v43\n"
"v_exp_f32 v44, v44\n"
"v_exp_f32 v45, v45\n"
"v_exp_f32 v46, v46\n"
"v_exp_f32 v47, v47\n"
"s_setprio 2\n"
"s_waitcnt lgkmcnt(0)\n"
"v_smfmac_f32_32x32x32_bf16 v[48:63], v[80:83], v[16:23], v13\n"
"v_smfmac_f32_32x32x32_bf16 v[48:63], v[84:87], v[24:31], v13\n"
"s_mov_b64 exec, s[18:19]\n"
"ds_read_b128 v[80:83], v12 offset:2560\n"
"ds_read_b128 v[84:87], v12 offset:3200\n"
"s_mov_b64 exec, -1\n"
"v_mfma_f32_32x32x8_bf16 v[16:31], v[70:71], v[68:69], 0\n"
"ds_read_b64 v[70:71], v11 offset:10240\n"
"v_mfma_f32_32x32x16_bf16 v[16:31], v[72:75], v[64:67], v[16:31]\n"
"ds_read_b128 v[72:75], v10 offset:10240\n"
"v_smfmac_f32_32x32x32_bf16 v[48:63], v[88:91], v[32:39], v13\n"
"v_smfmac_f32_32x32x32_bf16 v[48:63], v[92:95], v[40:47], v13\n"
"s_mov_b64 exec, s[18:19]\n"
"ds_read_b128 v[88:91], v12 offset:3840\n"
"ds_read_b128 v[92:95], v12 offset:4480\n"
"s_mov_b64 exec, -1\n"
"v_mfma_f32_32x32x8_bf16 v[32:47], v[8:9], v[68:69], 0\n"
"ds_read_b64 v[8:9], v11 offset:10496\n"
"v_mfma_f32_32x32x16_bf16 v[32:47], v[76:79], v[64:67], v[32:47]\n"
"ds_read_b128 v[76:79], v10 offset:10752\n"
"s_setprio 0\n"
"v_exp_f32 v16, v16\n"
"v_exp_f32 v17, v17\n"
"s_setprio 1\n"
"v_exp_f32 v18, v18\n"
"v_exp_f32 v19, v19\n"
"v_exp_f32 v20, v20\n"
"v_exp_f32 v21, v21\n"
"v_exp_f32 v22, v22\n"
"v_exp_f32 v23, v23\n"
"v_exp_f32 v24, v24\n"
"v_exp_f32 v25, v25\n"
"v_exp_f32 v26, v26\n"
"v_exp_f32 v27, v27\n"
"v_exp_f32 v28, v28\n"
"v_exp_f32 v29, v29\n"
"v_exp_f32 v30, v30\n"
"v_exp_f32 v31, v31\n"
"v_exp_f32 v32, v32\n"
"v_exp_f32 v33, v33\n"
"v_exp_f32 v34, v34\n"
"v_exp_f32 v35, v35\n"
"v_exp_f32 v36, v36\n"
"v_exp_f32 v37, v37\n"
"v_exp_f32 v38, v38\n"
"v_exp_f32 v39, v39\n"
"v_exp_f32 v40, v40\n"
"v_exp_f32 v41, v41\n"
"v_exp_f32 v42, v42\n"
"v_exp_f32 v43, v43\n"
"v_exp_f32 v44, v44\n"
"v_exp_f32 v45, v45\n"
"v_exp_f32 v46, v46\n"
"v_exp_f32 v47, v47\n"
"s_setprio 2\n"
"s_waitcnt vmcnt(0)\n"
"s_barrier\n"
"s_waitcnt lgkmcnt(0)\n"
"v_smfmac_f32_32x32x32_bf16 v[48:63], v[80:83], v[16:23], v13\n"
"v_smfmac_f32_32x32x32_bf16 v[48:63], v[84:87], v[24:31], v13\n"
"s_mov_b64 exec, s[18:19]\n"
"ds_read_b128 v[80:83], v12 offset:10240\n"
"ds_read_b128 v[84:87], v12 offset:10880\n"
"s_mov_b64 exec, -1\n"
"v_mfma_f32_32x32x8_bf16 v[16:31], v[70:71], v[68:69], 0\n"
"ds_read_b64 v[70:71], v11 offset:10752\n"
"v_mfma_f32_32x32x16_bf16 v[16:31], v[72:75], v[64:67], v[16:31]\n"
"ds_read_b128 v[72:75], v10 offset:11264\n"
"v_smfmac_f32_32x32x32_bf16 v[48:63], v[88:91], v[32:39], v13\n"
"v_smfmac_f32_32x32x32_bf16 v[48:63], v[92:95], v[40:47], v13\n"
"s_mov_b64 exec, s[18:19]\n"
"ds_read_b128 v[88:91], v12 offset:11520\n"
"ds_read_b128 v[92:95], v12 offset:12160\n"
"s_mov_b64 exec, -1\n"
"v_mfma_f32_32x32x8_bf16 v[32:47], v[8:9], v[68:69], 0\n"
"ds_read_b64 v[8:9], v11 offset:11008\n"
"v_mfma_f32_32x32x16_bf16 v[32:47], v[76:79], v[64:67], v[32:47]\n"
"ds_read_b128 v[76:79], v10 offset:11776\n"
"s_setprio 0\n"
"v_exp_f32 v16, v16\n"
"v_exp_f32 v17, v17\n"
"s_setprio 1\n"
"v_exp_f32 v18, v18\n"
"v_exp_f32 v19, v19\n"
"v_exp_f32 v20, v20\n"
"v_exp_f32 v21, v21\n"
"v_exp_f32 v22, v22\n"
"v_exp_f32 v23, v23\n"
"v_exp_f32 v24, v24\n"
"v_exp_f32 v25, v25\n"
"v_exp_f32 v26, v26\n"
"v_exp_f32 v27, v27\n"
"v_exp_f32 v28, v28\n"
"v_exp_f32 v29, v29\n"
"v_exp_f32 v30, v30\n"
"v_exp_f32 v31, v31\n"
"v_exp_f32 v32, v32\n"
"v_exp_f32 v33, v33\n"
"v_exp_f32 v34, v34\n"
"v_exp_f32 v35, v35\n"
"v_exp_f32 v36, v36\n"
"v_exp_f32 v37, v37\n"
"v_exp_f32 v38, v38\n"
"v_exp_f32 v39, v39\n"
"v_exp_f32 v40, v40\n"
"v_exp_f32 v41, v41\n"
"v_exp_f32 v42, v42\n"
"v_exp_f32 v43, v43\n"
"v_exp_f32 v44, v44\n"
"v_exp_f32 v45, v45\n"
"v_exp_f32 v46, v46\n"
"v_exp_f32 v47, v47\n"
"s_setprio 2\n"
"s_waitcnt lgkmcnt(0)\n"
"v_smfmac_f32_32x32x32_bf16 v[48:63], v[80:83], v[16:23], v13\n"
"s_lshl_b32 s56, s34, 3\n"
"s_add_u32 s57, s56, s63\n"
"s_lshr_b32 s58, s57, 1\n"
"s_min_u32 s59, s58, 26\n"
"s_lshl_b32 s60, s59, 2\n"
"s_lshl_b32 s61, s60, 10\n"
"s_add_u32 s21, 0x41000, s61\n"
"v_and_b32 v2, 3, v0\n"
"v_lshlrev_b32 v15, 10, v2\n"
"v_smfmac_f32_32x32x32_bf16 v[48:63], v[84:87], v[24:31], v13\n"
"s_mov_b64 exec, s[18:19]\n"
"ds_read_b128 v[80:83], v12 offset:12800\n"
"ds_read_b128 v[84:87], v12 offset:13440\n"
"s_mov_b64 exec, -1\n"
"v_mfma_f32_32x32x8_bf16 v[16:31], v[70:71], v[68:69], 0\n"
"ds_read_b64 v[70:71], v11 offset:20480\n"
"v_mfma_f32_32x32x16_bf16 v[16:31], v[72:75], v[64:67], v[16:31]\n"
"ds_read_b128 v[72:75], v10 offset:20480\n"
"v_smfmac_f32_32x32x32_bf16 v[48:63], v[88:91], v[32:39], v13\n"
"v_smfmac_f32_32x32x32_bf16 v[48:63], v[92:95], v[40:47], v13\n"
"s_mov_b64 exec, s[18:19]\n"
"ds_read_b128 v[88:91], v12 offset:14080\n"
"ds_read_b128 v[92:95], v12 offset:14720\n"
"s_mov_b64 exec, -1\n"
"v_mfma_f32_32x32x8_bf16 v[32:47], v[8:9], v[68:69], 0\n"
"ds_read_b64 v[8:9], v11 offset:20736\n"
"v_mfma_f32_32x32x16_bf16 v[32:47], v[76:79], v[64:67], v[32:47]\n"
"ds_read_b128 v[76:79], v10 offset:20992\n"
"s_setprio 0\n"
"v_exp_f32 v16, v16\n"
"v_exp_f32 v17, v17\n"
"s_setprio 1\n"
"v_exp_f32 v18, v18\n"
"v_exp_f32 v19, v19\n"
"v_exp_f32 v20, v20\n"
"v_exp_f32 v21, v21\n"
"v_exp_f32 v22, v22\n"
"v_exp_f32 v23, v23\n"
"v_exp_f32 v24, v24\n"
"v_exp_f32 v25, v25\n"
"v_exp_f32 v26, v26\n"
"v_exp_f32 v27, v27\n"
"v_exp_f32 v28, v28\n"
"v_exp_f32 v29, v29\n"
"v_exp_f32 v30, v30\n"
"v_exp_f32 v31, v31\n"
"v_exp_f32 v32, v32\n"
"v_exp_f32 v33, v33\n"
"v_exp_f32 v34, v34\n"
"v_exp_f32 v35, v35\n"
"v_exp_f32 v36, v36\n"
"v_exp_f32 v37, v37\n"
"v_exp_f32 v38, v38\n"
"v_exp_f32 v39, v39\n"
"v_exp_f32 v40, v40\n"
"v_exp_f32 v41, v41\n"
"v_exp_f32 v42, v42\n"
"v_exp_f32 v43, v43\n"
"v_exp_f32 v44, v44\n"
"v_exp_f32 v45, v45\n"
"v_exp_f32 v46, v46\n"
"v_exp_f32 v47, v47\n"
"s_setprio 2\n"
"s_waitcnt lgkmcnt(0)\n"
"v_smfmac_f32_32x32x32_bf16 v[48:63], v[80:83], v[16:23], v13\n"
"s_mov_b32 m0, s17\n"
"s_nop 0\n"
"buffer_load_dwordx4 v14, s[4:7], s16 offen sc1 lds\n"
"s_add_u32 s16, s16, 0x2000\n"
"s_add_u32 s17, s17, 0x2000\n"
"v_smfmac_f32_32x32x32_bf16 v[48:63], v[84:87], v[24:31], v13\n"
"s_mov_b64 exec, s[18:19]\n"
"ds_read_b128 v[80:83], v12 offset:20480\n"
"ds_read_b128 v[84:87], v12 offset:21120\n"
"s_mov_b64 exec, -1\n"
"v_mfma_f32_32x32x8_bf16 v[16:31], v[70:71], v[68:69], 0\n"
"ds_read_b64 v[70:71], v11 offset:20992\n"
"s_mov_b32 m0, s17\n"
"s_nop 0\n"
"buffer_load_dwordx4 v14, s[4:7], s16 offen sc1 lds\n"
"s_add_u32 s16, s16, 0x2000\n"
"s_add_u32 s17, s17, 0x2000\n"
"v_mfma_f32_32x32x16_bf16 v[16:31], v[72:75], v[64:67], v[16:31]\n"
"ds_read_b128 v[72:75], v10 offset:21504\n"
"v_smfmac_f32_32x32x32_bf16 v[48:63], v[88:91], v[32:39], v13\n"
"s_mov_b32 m0, s17\n"
"s_nop 0\n"
"buffer_load_dwordx4 v14, s[4:7], s16 offen sc1 lds\n"
"s_add_u32 s16, s16, 0x2000\n"
"s_add_u32 s17, s17, 0x2000\n"
"v_smfmac_f32_32x32x32_bf16 v[48:63], v[92:95], v[40:47], v13\n"
"s_mov_b64 exec, s[18:19]\n"
"ds_read_b128 v[88:91], v12 offset:21760\n"
"ds_read_b128 v[92:95], v12 offset:22400\n"
"s_mov_b64 exec, -1\n"
"v_mfma_f32_32x32x8_bf16 v[32:47], v[8:9], v[68:69], 0\n"
"ds_read_b64 v[8:9], v11 offset:21248\n"
"v_mfma_f32_32x32x16_bf16 v[32:47], v[76:79], v[64:67], v[32:47]\n"
"ds_read_b128 v[76:79], v10 offset:22016\n"
"s_setprio 0\n"
"v_exp_f32 v16, v16\n"
"v_exp_f32 v17, v17\n"
"s_setprio 1\n"
"v_exp_f32 v18, v18\n"
"v_exp_f32 v19, v19\n"
"v_exp_f32 v20, v20\n"
"v_exp_f32 v21, v21\n"
"v_exp_f32 v22, v22\n"
"v_exp_f32 v23, v23\n"
"v_exp_f32 v24, v24\n"
"v_exp_f32 v25, v25\n"
"v_exp_f32 v26, v26\n"
"v_exp_f32 v27, v27\n"
"v_exp_f32 v28, v28\n"
"v_exp_f32 v29, v29\n"
"v_exp_f32 v30, v30\n"
"v_exp_f32 v31, v31\n"
"v_exp_f32 v32, v32\n"
"v_exp_f32 v33, v33\n"
"v_exp_f32 v34, v34\n"
"v_exp_f32 v35, v35\n"
"v_exp_f32 v36, v36\n"
"v_exp_f32 v37, v37\n"
"v_exp_f32 v38, v38\n"
"v_exp_f32 v39, v39\n"
"v_exp_f32 v40, v40\n"
"v_exp_f32 v41, v41\n"
"v_exp_f32 v42, v42\n"
"v_exp_f32 v43, v43\n"
"v_exp_f32 v44, v44\n"
"v_exp_f32 v45, v45\n"
"v_exp_f32 v46, v46\n"
"v_exp_f32 v47, v47\n"
"s_setprio 2\n"
"s_waitcnt lgkmcnt(0)\n"
"v_smfmac_f32_32x32x32_bf16 v[48:63], v[80:83], v[16:23], v13\n"
"s_mov_b32 m0, s17\n"
"s_nop 0\n"
"buffer_load_dwordx4 v14, s[4:7], s16 offen sc1 lds\n"
"s_add_u32 s16, s16, 0x2000\n"
"s_add_u32 s17, s17, 0x2000\n"
"v_smfmac_f32_32x32x32_bf16 v[48:63], v[84:87], v[24:31], v13\n"
"s_mov_b64 exec, s[18:19]\n"
"ds_read_b128 v[80:83], v12 offset:23040\n"
"ds_read_b128 v[84:87], v12 offset:23680\n"
"s_mov_b64 exec, -1\n"
"v_mfma_f32_32x32x8_bf16 v[16:31], v[70:71], v[68:69], 0\n"
"ds_read_b64 v[70:71], v11 offset:30720\n"
"s_mov_b32 m0, s17\n"
"s_nop 0\n"
"buffer_load_dwordx4 v14, s[4:7], s16 offen sc1 lds\n"
"s_add_u32 s16, s16, 0x2000\n"
"s_add_u32 s17, s17, 0x2000\n"
"v_mfma_f32_32x32x16_bf16 v[16:31], v[72:75], v[64:67], v[16:31]\n"
"ds_read_b128 v[72:75], v10 offset:30720\n"
"v_smfmac_f32_32x32x32_bf16 v[48:63], v[88:91], v[32:39], v13\n"
"s_mov_b32 m0, s17\n"
"s_nop 0\n"
"buffer_load_dwordx4 v14, s[4:7], s16 offen sc1 lds\n"
"s_add_u32 s16, s16, 0x2000\n"
"s_add_u32 s17, s17, 0x2000\n"
"v_smfmac_f32_32x32x32_bf16 v[48:63], v[92:95], v[40:47], v13\n"
"s_mov_b64 exec, s[18:19]\n"
"ds_read_b128 v[88:91], v12 offset:24320\n"
"ds_read_b128 v[92:95], v12 offset:24960\n"
"s_mov_b64 exec, -1\n"
"v_mfma_f32_32x32x8_bf16 v[32:47], v[8:9], v[68:69], 0\n"
"ds_read_b64 v[8:9], v11 offset:30976\n"
"v_mfma_f32_32x32x16_bf16 v[32:47], v[76:79], v[64:67], v[32:47]\n"
"ds_read_b128 v[76:79], v10 offset:31232\n"
"s_setprio 0\n"
"v_exp_f32 v16, v16\n"
"v_exp_f32 v17, v17\n"
"s_setprio 1\n"
"v_exp_f32 v18, v18\n"
"v_exp_f32 v19, v19\n"
"v_exp_f32 v20, v20\n"
"v_exp_f32 v21, v21\n"
"v_exp_f32 v22, v22\n"
"v_exp_f32 v23, v23\n"
"v_exp_f32 v24, v24\n"
"v_exp_f32 v25, v25\n"
"v_exp_f32 v26, v26\n"
"v_exp_f32 v27, v27\n"
"v_exp_f32 v28, v28\n"
"v_exp_f32 v29, v29\n"
"v_exp_f32 v30, v30\n"
"v_exp_f32 v31, v31\n"
"v_exp_f32 v32, v32\n"
"v_exp_f32 v33, v33\n"
"v_exp_f32 v34, v34\n"
"v_exp_f32 v35, v35\n"
"v_exp_f32 v36, v36\n"
"v_exp_f32 v37, v37\n"
"v_exp_f32 v38, v38\n"
"v_exp_f32 v39, v39\n"
"v_exp_f32 v40, v40\n"
"v_exp_f32 v41, v41\n"
"v_exp_f32 v42, v42\n"
"v_exp_f32 v43, v43\n"
"v_exp_f32 v44, v44\n"
"v_exp_f32 v45, v45\n"
"v_exp_f32 v46, v46\n"
"v_exp_f32 v47, v47\n"
"s_setprio 2\n"
"s_waitcnt lgkmcnt(0)\n"
"v_smfmac_f32_32x32x32_bf16 v[48:63], v[80:83], v[16:23], v13\n"
"v_smfmac_f32_32x32x32_bf16 v[48:63], v[84:87], v[24:31], v13\n"
"s_mov_b64 exec, s[18:19]\n"
"ds_read_b128 v[80:83], v12 offset:30720\n"
"ds_read_b128 v[84:87], v12 offset:31360\n"
"s_mov_b64 exec, -1\n"
"v_mfma_f32_32x32x8_bf16 v[16:31], v[70:71], v[68:69], 0\n"
"ds_read_b64 v[70:71], v11 offset:31232\n"
"v_mfma_f32_32x32x16_bf16 v[16:31], v[72:75], v[64:67], v[16:31]\n"
"ds_read_b128 v[72:75], v10 offset:31744\n"
"v_smfmac_f32_32x32x32_bf16 v[48:63], v[88:91], v[32:39], v13\n"
"v_smfmac_f32_32x32x32_bf16 v[48:63], v[92:95], v[40:47], v13\n"
"s_mov_b64 exec, s[18:19]\n"
"ds_read_b128 v[88:91], v12 offset:32000\n"
"ds_read_b128 v[92:95], v12 offset:32640\n"
"s_mov_b64 exec, -1\n"
"v_mfma_f32_32x32x8_bf16 v[32:47], v[8:9], v[68:69], 0\n"
"ds_read_b64 v[8:9], v11 offset:31488\n"
"v_mfma_f32_32x32x16_bf16 v[32:47], v[76:79], v[64:67], v[32:47]\n"
"ds_read_b128 v[76:79], v10 offset:32256\n"
"s_setprio 0\n"
"v_exp_f32 v16, v16\n"
"v_exp_f32 v17, v17\n"
"s_setprio 1\n"
"v_exp_f32 v18, v18\n"
"v_exp_f32 v19, v19\n"
"v_exp_f32 v20, v20\n"
"v_exp_f32 v21, v21\n"
"v_exp_f32 v22, v22\n"
"v_exp_f32 v23, v23\n"
"v_exp_f32 v24, v24\n"
"v_exp_f32 v25, v25\n"
"v_exp_f32 v26, v26\n"
"v_exp_f32 v27, v27\n"
"v_exp_f32 v28, v28\n"
"v_exp_f32 v29, v29\n"
"v_exp_f32 v30, v30\n"
"v_exp_f32 v31, v31\n"
"v_exp_f32 v32, v32\n"
"v_exp_f32 v33, v33\n"
"v_exp_f32 v34, v34\n"
"v_exp_f32 v35, v35\n"
"v_exp_f32 v36, v36\n"
"v_exp_f32 v37, v37\n"
"v_exp_f32 v38, v38\n"
"v_exp_f32 v39, v39\n"
"v_exp_f32 v40, v40\n"
"v_exp_f32 v41, v41\n"
"v_exp_f32 v42, v42\n"
"v_exp_f32 v43, v43\n"
"v_exp_f32 v44, v44\n"
"v_exp_f32 v45, v45\n"
"v_exp_f32 v46, v46\n"
"v_exp_f32 v47, v47\n"
"s_setprio 2\n"
"s_waitcnt vmcnt(0)\n"
"s_barrier\n"
"s_waitcnt lgkmcnt(0)\n"
"v_smfmac_f32_32x32x32_bf16 v[48:63], v[80:83], v[16:23], v13\n"
"v_smfmac_f32_32x32x32_bf16 v[48:63], v[84:87], v[24:31], v13\n"
"s_mov_b64 exec, s[18:19]\n"
"ds_read_b128 v[80:83], v12 offset:33280\n"
"ds_read_b128 v[84:87], v12 offset:33920\n"
"s_mov_b64 exec, -1\n"
"v_mfma_f32_32x32x8_bf16 v[16:31], v[70:71], v[68:69], 0\n"
"ds_read_b64 v[70:71], v11 offset:40960\n"
"v_mfma_f32_32x32x16_bf16 v[16:31], v[72:75], v[64:67], v[16:31]\n"
"ds_read_b128 v[72:75], v10 offset:40960\n"
"v_smfmac_f32_32x32x32_bf16 v[48:63], v[88:91], v[32:39], v13\n"
"v_smfmac_f32_32x32x32_bf16 v[48:63], v[92:95], v[40:47], v13\n"
"s_mov_b64 exec, s[18:19]\n"
"ds_read_b128 v[88:91], v12 offset:34560\n"
"ds_read_b128 v[92:95], v12 offset:35200\n"
"s_mov_b64 exec, -1\n"
"v_mfma_f32_32x32x8_bf16 v[32:47], v[8:9], v[68:69], 0\n"
"ds_read_b64 v[8:9], v11 offset:41216\n"
"v_mfma_f32_32x32x16_bf16 v[32:47], v[76:79], v[64:67], v[32:47]\n"
"ds_read_b128 v[76:79], v10 offset:41472\n"
"s_setprio 0\n"
"v_exp_f32 v16, v16\n"
"v_exp_f32 v17, v17\n"
"s_setprio 1\n"
"v_exp_f32 v18, v18\n"
"v_exp_f32 v19, v19\n"
"v_exp_f32 v20, v20\n"
"v_exp_f32 v21, v21\n"
"v_exp_f32 v22, v22\n"
"v_exp_f32 v23, v23\n"
"v_exp_f32 v24, v24\n"
"v_exp_f32 v25, v25\n"
"v_exp_f32 v26, v26\n"
"v_exp_f32 v27, v27\n"
"v_exp_f32 v28, v28\n"
"v_exp_f32 v29, v29\n"
"v_exp_f32 v30, v30\n"
"v_exp_f32 v31, v31\n"
"v_exp_f32 v32, v32\n"
"v_exp_f32 v33, v33\n"
"v_exp_f32 v34, v34\n"
"v_exp_f32 v35, v35\n"
"v_exp_f32 v36, v36\n"
"v_exp_f32 v37, v37\n"
"v_exp_f32 v38, v38\n"
"v_exp_f32 v39, v39\n"
"v_exp_f32 v40, v40\n"
"v_exp_f32 v41, v41\n"
"v_exp_f32 v42, v42\n"
"v_exp_f32 v43, v43\n"
"v_exp_f32 v44, v44\n"
"v_exp_f32 v45, v45\n"
"v_exp_f32 v46, v46\n"
"v_exp_f32 v47, v47\n"
"s_setprio 2\n"
"s_waitcnt lgkmcnt(0)\n"
"v_smfmac_f32_32x32x32_bf16 v[48:63], v[80:83], v[16:23], v13\n"
"v_smfmac_f32_32x32x32_bf16 v[48:63], v[84:87], v[24:31], v13\n"
"s_mov_b64 exec, s[18:19]\n"
"ds_read_b128 v[80:83], v12 offset:40960\n"
"ds_read_b128 v[84:87], v12 offset:41600\n"
"s_cbranch_execz .Lk_tramp\n"
"s_nop 0\n"
"s_mov_b64 exec, -1\n"
"v_mfma_f32_32x32x8_bf16 v[16:31], v[70:71], v[68:69], 0\n"
"ds_read_b64 v[70:71], v11 offset:41472\n"
"v_mfma_f32_32x32x16_bf16 v[16:31], v[72:75], v[64:67], v[16:31]\n"
"ds_read_b128 v[72:75], v10 offset:41984\n"
"v_smfmac_f32_32x32x32_bf16 v[48:63], v[88:91], v[32:39], v13\n"
"v_smfmac_f32_32x32x32_bf16 v[48:63], v[92:95], v[40:47], v13\n"
"s_mov_b64 exec, s[18:19]\n"
"ds_read_b128 v[88:91], v12 offset:42240\n"
"ds_read_b128 v[92:95], v12 offset:42880\n"
"s_mov_b64 exec, -1\n"
"v_mfma_f32_32x32x8_bf16 v[32:47], v[8:9], v[68:69], 0\n"
"ds_read_b64 v[8:9], v11 offset:41728\n"
"v_mfma_f32_32x32x16_bf16 v[32:47], v[76:79], v[64:67], v[32:47]\n"
"ds_read_b128 v[76:79], v10 offset:42496\n"
"s_setprio 0\n"
"v_exp_f32 v16, v16\n"
"v_exp_f32 v17, v17\n"
"s_setprio 1\n"
"v_exp_f32 v18, v18\n"
"v_exp_f32 v19, v19\n"
"v_exp_f32 v20, v20\n"
"v_exp_f32 v21, v21\n"
"v_exp_f32 v22, v22\n"
"v_exp_f32 v23, v23\n"
"v_exp_f32 v24, v24\n"
"v_exp_f32 v25, v25\n"
"v_exp_f32 v26, v26\n"
"v_exp_f32 v27, v27\n"
"v_exp_f32 v28, v28\n"
"v_exp_f32 v29, v29\n"
"v_exp_f32 v30, v30\n"
"v_exp_f32 v31, v31\n"
"v_exp_f32 v32, v32\n"
"v_exp_f32 v33, v33\n"
"v_exp_f32 v34, v34\n"
"v_exp_f32 v35, v35\n"
"v_exp_f32 v36, v36\n"
"v_exp_f32 v37, v37\n"
"v_exp_f32 v38, v38\n"
"v_exp_f32 v39, v39\n"
"v_exp_f32 v40, v40\n"
"v_exp_f32 v41, v41\n"
"v_exp_f32 v42, v42\n"
"v_exp_f32 v43, v43\n"
"v_exp_f32 v44, v44\n"
"v_exp_f32 v45, v45\n"
"v_exp_f32 v46, v46\n"
"v_exp_f32 v47, v47\n"
"s_setprio 2\n"
"s_waitcnt lgkmcnt(0)\n"
"v_smfmac_f32_32x32x32_bf16 v[48:63], v[80:83], v[16:23], v13\n"
"v_smfmac_f32_32x32x32_bf16 v[48:63], v[84:87], v[24:31], v13\n"
"s_mov_b64 exec, s[18:19]\n"
"ds_read_b128 v[80:83], v12 offset:43520\n"
"ds_read_b128 v[84:87], v12 offset:44160\n"
"s_mov_b64 exec, -1\n"
"v_mfma_f32_32x32x8_bf16 v[16:31], v[70:71], v[68:69], 0\n"
"ds_read_b64 v[70:71], v11 offset:51200\n"
"v_mfma_f32_32x32x16_bf16 v[16:31], v[72:75], v[64:67], v[16:31]\n"
"ds_read_b128 v[72:75], v10 offset:51200\n"
"v_smfmac_f32_32x32x32_bf16 v[48:63], v[88:91], v[32:39], v13\n"
"v_smfmac_f32_32x32x32_bf16 v[48:63], v[92:95], v[40:47], v13\n"
"s_mov_b64 exec, s[18:19]\n"
"ds_read_b128 v[88:91], v12 offset:44800\n"
"ds_read_b128 v[92:95], v12 offset:45440\n"
"s_mov_b64 exec, -1\n"
"v_mfma_f32_32x32x8_bf16 v[32:47], v[8:9], v[68:69], 0\n"
"ds_read_b64 v[8:9], v11 offset:51456\n"
"v_mfma_f32_32x32x16_bf16 v[32:47], v[76:79], v[64:67], v[32:47]\n"
"ds_read_b128 v[76:79], v10 offset:51712\n"
"s_setprio 0\n"
"v_exp_f32 v16, v16\n"
"v_exp_f32 v17, v17\n"
"s_setprio 1\n"
"v_exp_f32 v18, v18\n"
"v_exp_f32 v19, v19\n"
"v_exp_f32 v20, v20\n"
"v_exp_f32 v21, v21\n"
"v_exp_f32 v22, v22\n"
"v_exp_f32 v23, v23\n"
"v_exp_f32 v24, v24\n"
"v_exp_f32 v25, v25\n"
"v_exp_f32 v26, v26\n"
"v_exp_f32 v27, v27\n"
"v_exp_f32 v28, v28\n"
"v_exp_f32 v29, v29\n"
"v_exp_f32 v30, v30\n"
"v_exp_f32 v31, v31\n"
"v_exp_f32 v32, v32\n"
"v_exp_f32 v33, v33\n"
"v_exp_f32 v34, v34\n"
"v_exp_f32 v35, v35\n"
"v_exp_f32 v36, v36\n"
"v_exp_f32 v37, v37\n"
"v_exp_f32 v38, v38\n"
"v_exp_f32 v39, v39\n"
"v_exp_f32 v40, v40\n"
"v_exp_f32 v41, v41\n"
"v_exp_f32 v42, v42\n"
"v_exp_f32 v43, v43\n"
"v_exp_f32 v44, v44\n"
"v_exp_f32 v45, v45\n"
"v_exp_f32 v46, v46\n"
"v_exp_f32 v47, v47\n"
"s_setprio 2\n"
"s_waitcnt lgkmcnt(0)\n"
"v_add_u32 v10, 0xc800, v10\n"
"v_add_u32 v11, 0xc800, v11\n"
"v_add_u32 v12, 0xc800, v12\n"
"v_smfmac_f32_32x32x32_bf16 v[48:63], v[80:83], v[16:23], v13\n"
"s_mov_b32 m0, s17\n"
"s_nop 0\n"
"buffer_load_dwordx4 v14, s[4:7], s16 offen sc1 lds\n"
"s_add_u32 s16, s16, 0x2000\n"
"s_add_u32 s17, s17, 0x2000\n"
"v_smfmac_f32_32x32x32_bf16 v[48:63], v[84:87], v[24:31], v13\n"
"s_mov_b64 exec, s[18:19]\n"
"ds_read_b128 v[80:83], v12\n"
"ds_read_b128 v[84:87], v12 offset:640\n"
"s_mov_b64 exec, -1\n"
"v_mfma_f32_32x32x8_bf16 v[16:31], v[70:71], v[68:69], 0\n"
"ds_read_b64 v[70:71], v11 offset:512\n"
"s_mov_b32 m0, s17\n"
"s_nop 0\n"
"buffer_load_dwordx4 v14, s[4:7], s16 offen sc1 lds\n"
"s_add_u32 s16, s16, 0x2000\n"
"s_add_u32 s17, s17, 0x2000\n"
"v_mfma_f32_32x32x16_bf16 v[16:31], v[72:75], v[64:67], v[16:31]\n"
"ds_read_b128 v[72:75], v10 offset:1024\n"
"v_smfmac_f32_32x32x32_bf16 v[48:63], v[88:91], v[32:39], v13\n"
"s_mov_b32 m0, s17\n"
"s_nop 0\n"
"buffer_load_dwordx4 v14, s[4:7], s16 offen sc1 lds\n"
"s_add_u32 s16, s16, 0x2000\n"
"s_add_u32 s17, s17, 0x2000\n"
"v_smfmac_f32_32x32x32_bf16 v[48:63], v[92:95], v[40:47], v13\n"
"s_mov_b64 exec, s[18:19]\n"
"ds_read_b128 v[88:91], v12 offset:1280\n"
"ds_read_b128 v[92:95], v12 offset:1920\n"
"s_mov_b64 exec, -1\n"
"v_mfma_f32_32x32x8_bf16 v[32:47], v[8:9], v[68:69], 0\n"
"ds_read_b64 v[8:9], v11 offset:768\n"
"v_mfma_f32_32x32x16_bf16 v[32:47], v[76:79], v[64:67], v[32:47]\n"
"ds_read_b128 v[76:79], v10 offset:1536\n"
"s_setprio 0\n"
"v_exp_f32 v16, v16\n"
"v_exp_f32 v17, v17\n"
"s_setprio 1\n"
"v_exp_f32 v18, v18\n"
"v_exp_f32 v19, v19\n"
"v_exp_f32 v20, v20\n"
"v_exp_f32 v21, v21\n"
"v_exp_f32 v22, v22\n"
"v_exp_f32 v23, v23\n"
"v_exp_f32 v24, v24\n"
"v_exp_f32 v25, v25\n"
"v_exp_f32 v26, v26\n"
"v_exp_f32 v27, v27\n"
"v_exp_f32 v28, v28\n"
"v_exp_f32 v29, v29\n"
"v_exp_f32 v30, v30\n"
"v_exp_f32 v31, v31\n"
"v_exp_f32 v32, v32\n"
"v_exp_f32 v33, v33\n"
"v_exp_f32 v34, v34\n"
"v_exp_f32 v35, v35\n"
"v_exp_f32 v36, v36\n"
"v_exp_f32 v37, v37\n"
"v_exp_f32 v38, v38\n"
"v_exp_f32 v39, v39\n"
"v_exp_f32 v40, v40\n"
"v_exp_f32 v41, v41\n"
"v_exp_f32 v42, v42\n"
"v_exp_f32 v43, v43\n"
"v_exp_f32 v44, v44\n"
"v_exp_f32 v45, v45\n"
"v_exp_f32 v46, v46\n"
"v_exp_f32 v47, v47\n"
"s_setprio 2\n"
"s_waitcnt lgkmcnt(0)\n"
"v_smfmac_f32_32x32x32_bf16 v[48:63], v[80:83], v[16:23], v13\n"
"s_mov_b32 m0, s17\n"
"s_nop 0\n"
"buffer_load_dwordx4 v14, s[4:7], s16 offen sc1 lds\n"
"s_add_u32 s16, s16, 0x2000\n"
"s_add_u32 s17, s17, 0x2000\n"
"v_smfmac_f32_32x32x32_bf16 v[48:63], v[84:87], v[24:31], v13\n"
"s_mov_b64 exec, s[18:19]\n"
"ds_read_b128 v[80:83], v12 offset:2560\n"
"ds_read_b128 v[84:87], v12 offset:3200\n"
"s_mov_b64 exec, -1\n"
"v_mfma_f32_32x32x8_bf16 v[16:31], v[70:71], v[68:69], 0\n"
"ds_read_b64 v[70:71], v11 offset:10240\n"
"s_mov_b32 m0, s17\n"
"s_nop 0\n"
"buffer_load_dwordx4 v14, s[4:7], s16 offen sc1 lds\n"
"s_add_u32 s16, s16, 0x2000\n"
"s_add_u32 s17, s17, 0x2000\n"
"v_mfma_f32_32x32x16_bf16 v[16:31], v[72:75], v[64:67], v[16:31]\n"
"ds_read_b128 v[72:75], v10 offset:10240\n"
"v_smfmac_f32_32x32x32_bf16 v[48:63], v[88:91], v[32:39], v13\n"
"s_mov_b32 m0, s17\n"
"s_nop 0\n"
"buffer_load_dwordx4 v14, s[4:7], s16 offen sc1 lds\n"
"s_add_u32 s16, s16, 0x2000\n"
"s_add_u32 s17, s17, 0x2000\n"
"v_smfmac_f32_32x32x32_bf16 v[48:63], v[92:95], v[40:47], v13\n"
"s_mov_b64 exec, s[18:19]\n"
"ds_read_b128 v[88:91], v12 offset:3840\n"
"ds_read_b128 v[92:95], v12 offset:4480\n"
"s_mov_b64 exec, -1\n"
"v_mfma_f32_32x32x8_bf16 v[32:47], v[8:9], v[68:69], 0\n"
"ds_read_b64 v[8:9], v11 offset:10496\n"
"v_mfma_f32_32x32x16_bf16 v[32:47], v[76:79], v[64:67], v[32:47]\n"
"ds_read_b128 v[76:79], v10 offset:10752\n"
"s_setprio 0\n"
"v_exp_f32 v16, v16\n"
"v_exp_f32 v17, v17\n"
"s_setprio 1\n"
"v_exp_f32 v18, v18\n"
"v_exp_f32 v19, v19\n"
"v_exp_f32 v20, v20\n"
"v_exp_f32 v21, v21\n"
"v_exp_f32 v22, v22\n"
"v_exp_f32 v23, v23\n"
"v_exp_f32 v24, v24\n"
"v_exp_f32 v25, v25\n"
"v_exp_f32 v26, v26\n"
"v_exp_f32 v27, v27\n"
"v_exp_f32 v28, v28\n"
"v_exp_f32 v29, v29\n"
"v_exp_f32 v30, v30\n"
"v_exp_f32 v31, v31\n"
"v_exp_f32 v32, v32\n"
"v_exp_f32 v33, v33\n"
"v_exp_f32 v34, v34\n"
"v_exp_f32 v35, v35\n"
"v_exp_f32 v36, v36\n"
"v_exp_f32 v37, v37\n"
"v_exp_f32 v38, v38\n"
"v_exp_f32 v39, v39\n"
"v_exp_f32 v40, v40\n"
"v_exp_f32 v41, v41\n"
"v_exp_f32 v42, v42\n"
"v_exp_f32 v43, v43\n"
"v_exp_f32 v44, v44\n"
"v_exp_f32 v45, v45\n"
"v_exp_f32 v46, v46\n"
"v_exp_f32 v47, v47\n"
"s_setprio 2\n"
"s_barrier\n"
"s_waitcnt lgkmcnt(0)\n"
"v_smfmac_f32_32x32x32_bf16 v[48:63], v[80:83], v[16:23], v13\n"
"s_cmp_lg_u32 s34, 0\n"
"s_cbranch_scc1 .Lk_nof2_36\n"
"s_lshl_b32 s56, s2, 10\n"
"s_add_u32 s38, 0x41000, s56\n"
"v_mov_b32 v2, s32\n"
"v_mov_b32 v3, s33\n"
"s_mov_b64 exec, 0xff\n"
"buffer_store_dwordx4 v[2:5], v14, s[4:7], s38 offen sc1\n"
"s_mov_b64 exec, -1\n"
".Lk_nof2_36:\n"
"v_smfmac_f32_32x32x32_bf16 v[48:63], v[84:87], v[24:31], v13\n"
"s_mov_b64 exec, s[18:19]\n"
"ds_read_b128 v[80:83], v12 offset:10240\n"
"ds_read_b128 v[84:87], v12 offset:10880\n"
"s_mov_b64 exec, -1\n"
"v_mfma_f32_32x32x8_bf16 v[16:31], v[70:71], v[68:69], 0\n"
"ds_read_b64 v[70:71], v11 offset:10752\n"
"s_mov_b32 m0, s17\n"
"s_nop 0\n"
"buffer_load_dwordx4 v14, s[4:7], s16 offen sc1 lds\n"
"s_add_u32 s16, s16, 0x2000\n"
"s_add_u32 s17, s17, 0x2000\n"
"v_mfma_f32_32x32x16_bf16 v[16:31], v[72:75], v[64:67], v[16:31]\n"
"ds_read_b128 v[72:75], v10 offset:11264\n"
"v_smfmac_f32_32x32x32_bf16 v[48:63], v[88:91], v[32:39], v13\n"
"s_mov_b32 m0, s17\n"
"s_nop 0\n"
"buffer_load_dwordx4 v14, s[4:7], s16 offen sc1 lds\n"
"s_add_u32 s16, s16, 0x2000\n"
"s_add_u32 s17, s17, 0x2000\n"
"v_smfmac_f32_32x32x32_bf16 v[48:63], v[92:95], v[40:47], v13\n"
"s_mov_b64 exec, s[18:19]\n"
"ds_read_b128 v[88:91], v12 offset:11520\n"
"ds_read_b128 v[92:95], v12 offset:12160\n"
"s_mov_b64 exec, -1\n"
"v_mfma_f32_32x32x8_bf16 v[32:47], v[8:9], v[68:69], 0\n"
"ds_read_b64 v[8:9], v11 offset:11008\n"
"s_mov_b32 m0, s17\n"
"s_nop 0\n"
"buffer_load_dwordx4 v14, s[4:7], s16 offen sc1 lds\n"
"v_mfma_f32_32x32x16_bf16 v[32:47], v[76:79], v[64:67], v[32:47]\n"
"ds_read_b128 v[76:79], v10 offset:11776\n"
"s_setprio 0\n"
"v_exp_f32 v16, v16\n"
"v_exp_f32 v17, v17\n"
"s_setprio 1\n"
"v_exp_f32 v18, v18\n"
"v_exp_f32 v19, v19\n"
"v_exp_f32 v20, v20\n"
"v_exp_f32 v21, v21\n"
"v_exp_f32 v22, v22\n"
"v_exp_f32 v23, v23\n"
"v_exp_f32 v24, v24\n"
"v_exp_f32 v25, v25\n"
"v_exp_f32 v26, v26\n"
"v_exp_f32 v27, v27\n"
"v_exp_f32 v28, v28\n"
"v_exp_f32 v29, v29\n"
"v_exp_f32 v30, v30\n"
"v_exp_f32 v31, v31\n"
"v_exp_f32 v32, v32\n"
"v_exp_f32 v33, v33\n"
"v_exp_f32 v34, v34\n"
"v_exp_f32 v35, v35\n"
"v_exp_f32 v36, v36\n"
"v_exp_f32 v37, v37\n"
"v_exp_f32 v38, v38\n"
"v_exp_f32 v39, v39\n"
"v_exp_f32 v40, v40\n"
"v_exp_f32 v41, v41\n"
"v_exp_f32 v42, v42\n"
"v_exp_f32 v43, v43\n"
"v_exp_f32 v44, v44\n"
"v_exp_f32 v45, v45\n"
"v_exp_f32 v46, v46\n"
"v_exp_f32 v47, v47\n"
"s_setprio 2\n"
"s_waitcnt lgkmcnt(0)\n"
"v_smfmac_f32_32x32x32_bf16 v[48:63], v[80:83], v[16:23], v13\n"
"s_and_b32 s56, s63, 7\n"
"s_mul_i32 s57, s56, 0x43800\n"
"s_add_u32 s58, 0x1975000, s57\n"
"s_mul_i32 s59, s3, 0x1400\n"
"s_add_u32 s25, s58, s59\n"
"s_add_u32 s26, s25, 3072\n"
"s_add_u32 s38, s26, 87040\n"
"s_mul_i32 s56, s3, 0x2800\n"
"s_add_u32 s27, 0x19000, s56\n"
"s_cmp_eq_u32 s3, 3\n"
"s_cselect_b32 s26, s38, s26\n"
"s_cselect_b32 s27, 133120, s27\n"
"s_movk_i32 s12, 0x5000\n"
"s_cselect_b32 s12, 0x1400, s12\n"
"s_movk_i32 s20, 0x800\n"
"s_cselect_b32 s20, 0x400, s20\n"
"v_smfmac_f32_32x32x32_bf16 v[48:63], v[84:87], v[24:31], v13\n"
"s_mov_b64 exec, s[18:19]\n"
"ds_read_b128 v[80:83], v12 offset:12800\n"
"ds_read_b128 v[84:87], v12 offset:13440\n"
"s_mov_b64 exec, -1\n"
"v_mfma_f32_32x32x8_bf16 v[16:31], v[70:71], v[68:69], 0\n"
"ds_read_b64 v[70:71], v11 offset:20480\n"
"v_mfma_f32_32x32x16_bf16 v[16:31], v[72:75], v[64:67], v[16:31]\n"
"ds_read_b128 v[72:75], v10 offset:20480\n"
"v_smfmac_f32_32x32x32_bf16 v[48:63], v[88:91], v[32:39], v13\n"
"v_smfmac_f32_32x32x32_bf16 v[48:63], v[92:95], v[40:47], v13\n"
"s_mov_b64 exec, s[18:19]\n"
"ds_read_b128 v[88:91], v12 offset:14080\n"
"ds_read_b128 v[92:95], v12 offset:14720\n"
"s_mov_b64 exec, -1\n"
"v_mfma_f32_32x32x8_bf16 v[32:47], v[8:9], v[68:69], 0\n"
"ds_read_b64 v[8:9], v11 offset:20736\n"
"v_mfma_f32_32x32x16_bf16 v[32:47], v[76:79], v[64:67], v[32:47]\n"
"ds_read_b128 v[76:79], v10 offset:20992\n"
"s_setprio 0\n"
"v_exp_f32 v16, v16\n"
"v_exp_f32 v17, v17\n"
"s_setprio 1\n"
"v_exp_f32 v18, v18\n"
"v_exp_f32 v19, v19\n"
"v_exp_f32 v20, v20\n"
"v_exp_f32 v21, v21\n"
"v_exp_f32 v22, v22\n"
"v_exp_f32 v23, v23\n"
"v_exp_f32 v24, v24\n"
"v_exp_f32 v25, v25\n"
"v_exp_f32 v26, v26\n"
"v_exp_f32 v27, v27\n"
"v_exp_f32 v28, v28\n"
"v_exp_f32 v29, v29\n"
"v_exp_f32 v30, v30\n"
"v_exp_f32 v31, v31\n"
"v_exp_f32 v32, v32\n"
"v_exp_f32 v33, v33\n"
"v_exp_f32 v34, v34\n"
"v_exp_f32 v35, v35\n"
"v_exp_f32 v36, v36\n"
"v_exp_f32 v37, v37\n"
"v_exp_f32 v38, v38\n"
"v_exp_f32 v39, v39\n"
"v_exp_f32 v40, v40\n"
"v_exp_f32 v41, v41\n"
"v_exp_f32 v42, v42\n"
"v_exp_f32 v43, v43\n"
"v_exp_f32 v44, v44\n"
"v_exp_f32 v45, v45\n"
"v_exp_f32 v46, v46\n"
"v_exp_f32 v47, v47\n"
"s_setprio 2\n"
"s_waitcnt lgkmcnt(0)\n"
"v_smfmac_f32_32x32x32_bf16 v[48:63], v[80:83], v[16:23], v13\n"
"buffer_load_dwordx2 v[6:7], v15, s[4:7], s21 offen sc1\n"
"v_smfmac_f32_32x32x32_bf16 v[48:63], v[84:87], v[24:31], v13\n"
"s_mov_b64 exec, s[18:19]\n"
"ds_read_b128 v[80:83], v12 offset:20480\n"
"ds_read_b128 v[84:87], v12 offset:21120\n"
"s_mov_b64 exec, -1\n"
"v_mfma_f32_32x32x8_bf16 v[16:31], v[70:71], v[68:69], 0\n"
"ds_read_b64 v[70:71], v11 offset:20992\n"
"v_not_b32 v2, s32\n"
"s_lshl_b32 s56, s62, 6\n"
"s_lshl_b32 s57, s34, 3\n"
"s_add_u32 s58, s56, s57\n"
"s_add_u32 s59, s58, s63\n"
"s_lshl_b32 s60, s59, 10\n"
"s_add_u32 s38, 0x101000, s60\n"
"v_mfma_f32_32x32x16_bf16 v[16:31], v[72:75], v[64:67], v[16:31]\n"
"ds_read_b128 v[72:75], v10 offset:21504\n"
"v_smfmac_f32_32x32x32_bf16 v[48:63], v[88:91], v[32:39], v13\n"
"s_mov_b64 exec, 0xff\n"
"buffer_store_dwordx4 v[2:5], v14, s[4:7], s38 offen sc1\n"
"s_mov_b64 exec, -1\n"
"v_smfmac_f32_32x32x32_bf16 v[48:63], v[92:95], v[40:47], v13\n"
"s_mov_b64 exec, s[18:19]\n"
"ds_read_b128 v[88:91], v12 offset:21760\n"
"ds_read_b128 v[92:95], v12 offset:22400\n"
"s_mov_b64 exec, -1\n"
"v_mfma_f32_32x32x8_bf16 v[32:47], v[8:9], v[68:69], 0\n"
"ds_read_b64 v[8:9], v11 offset:21248\n"
"s_lshl_b32 s56, s62, 3\n"
"s_add_u32 s57, s56, s63\n"
"s_lshl_b32 s58, s57, 10\n"
"s_add_u32 s38, 0xc1000, s58\n"
"s_cmp_eq_u32 s34, 0\n"
"s_cselect_b32 s38, s23, s38\n"
"v_mfma_f32_32x32x16_bf16 v[32:47], v[76:79], v[64:67], v[32:47]\n"
"ds_read_b128 v[76:79], v10 offset:22016\n"
"s_mov_b64 exec, 0xff\n"
"buffer_store_dwordx4 v[2:5], v14, s[4:7], s38 offen sc1\n"
"s_mov_b64 exec, -1\n"
"s_setprio 0\n"
"v_exp_f32 v16, v16\n"
"v_exp_f32 v17, v17\n"
"s_setprio 1\n"
"v_exp_f32 v18, v18\n"
"v_exp_f32 v19, v19\n"
"v_exp_f32 v20, v20\n"
"v_exp_f32 v21, v21\n"
"v_exp_f32 v22, v22\n"
"v_exp_f32 v23, v23\n"
"v_exp_f32 v24, v24\n"
"v_exp_f32 v25, v25\n"
"v_exp_f32 v26, v26\n"
"v_exp_f32 v27, v27\n"
"v_exp_f32 v28, v28\n"
"v_exp_f32 v29, v29\n"
"v_exp_f32 v30, v30\n"
"v_exp_f32 v31, v31\n"
"v_exp_f32 v32, v32\n"
"v_exp_f32 v33, v33\n"
"v_exp_f32 v34, v34\n"
"v_exp_f32 v35, v35\n"
"v_exp_f32 v36, v36\n"
"v_exp_f32 v37, v37\n"
"v_exp_f32 v38, v38\n"
"v_exp_f32 v39, v39\n"
"v_exp_f32 v40, v40\n"
"v_exp_f32 v41, v41\n"
"v_exp_f32 v42, v42\n"
"v_exp_f32 v43, v43\n"
"v_exp_f32 v44, v44\n"
"v_exp_f32 v45, v45\n"
"v_exp_f32 v46, v46\n"
"v_exp_f32 v47, v47\n"
"s_setprio 2\n"
"s_waitcnt vmcnt(3)\n"
"s_barrier\n"
"s_waitcnt lgkmcnt(0)\n"
"v_smfmac_f32_32x32x32_bf16 v[48:63], v[80:83], v[16:23], v13\n"
"s_waitcnt vmcnt(2)\n"
"v_cmp_ne_u64 vcc, s[32:33], v[6:7]\n"
"s_nop 0\n"
"s_cmp_eq_u64 vcc, 0\n"
"s_cbranch_scc1 .Lk_polld_38\n"
".Lk_poll_37:\n"
"s_sleep 4\n"
"buffer_load_dwordx2 v[6:7], v15, s[4:7], s21 offen sc1\n"
"s_waitcnt vmcnt(0)\n"
"v_cmp_ne_u64 vcc, s[32:33], v[6:7]\n"
"s_nop 0\n"
"s_cmp_eq_u64 vcc, 0\n"
"s_cbranch_scc1 .Lk_polld_38\n"
"s_sub_u32 s37, s37, 1\n"
"s_cmp_gt_i32 s37, 0\n"
"s_cbranch_scc1 .Lk_poll_37\n"
".Lk_polld_38:\n"
"v_smfmac_f32_32x32x32_bf16 v[48:63], v[84:87], v[24:31], v13\n"
"s_mov_b64 exec, s[18:19]\n"
"ds_read_b128 v[80:83], v12 offset:23040\n"
"ds_read_b128 v[84:87], v12 offset:23680\n"
"s_mov_b64 exec, -1\n"
"v_mfma_f32_32x32x8_bf16 v[16:31], v[70:71], v[68:69], 0\n"
"ds_read_b64 v[70:71], v11 offset:30720\n"
"buffer_load_dwordx4 v[248:251], v14, s[4:7], s13 offen sc1\n"
"v_mfma_f32_32x32x16_bf16 v[16:31], v[72:75], v[64:67], v[16:31]\n"
"ds_read_b128 v[72:75], v10 offset:30720\n"
"v_smfmac_f32_32x32x32_bf16 v[48:63], v[88:91], v[32:39], v13\n"
"buffer_load_dwordx4 v[252:255], v14, s[4:7], s13 offen offset:1024 sc1\n"
"s_sub_u32 s13, s13, s24\n"
"v_smfmac_f32_32x32x32_bf16 v[48:63], v[92:95], v[40:47], v13\n"
"s_mov_b64 exec, s[18:19]\n"
"ds_read_b128 v[88:91], v12 offset:24320\n"
"ds_read_b128 v[92:95], v12 offset:24960\n"
"s_mov_b64 exec, -1\n"
"v_mfma_f32_32x32x8_bf16 v[32:47], v[8:9], v[68:69], 0\n"
"ds_read_b64 v[8:9], v11 offset:30976\n"
"buffer_load_dwordx4 v[240:243], v14, s[4:7], s13 offen sc1\n"
"v_mfma_f32_32x32x16_bf16 v[32:47], v[76:79], v[64:67], v[32:47]\n"
"ds_read_b128 v[76:79], v10 offset:31232\n"
"s_setprio 0\n"
"v_exp_f32 v16, v16\n"
"v_exp_f32 v17, v17\n"
"s_setprio 1\n"
"v_exp_f32 v18, v18\n"
"v_exp_f32 v19, v19\n"
"v_exp_f32 v20, v20\n"
"v_exp_f32 v21, v21\n"
"v_exp_f32 v22, v22\n"
"v_exp_f32 v23, v23\n"
"v_exp_f32 v24, v24\n"
"v_exp_f32 v25, v25\n"
"v_exp_f32 v26, v26\n"
"v_exp_f32 v27, v27\n"
"v_exp_f32 v28, v28\n"
"v_exp_f32 v29, v29\n"
"v_exp_f32 v30, v30\n"
"v_exp_f32 v31, v31\n"
"v_exp_f32 v32, v32\n"
"v_exp_f32 v33, v33\n"
"v_exp_f32 v34, v34\n"
"v_exp_f32 v35, v35\n"
"v_exp_f32 v36, v36\n"
"v_exp_f32 v37, v37\n"
"v_exp_f32 v38, v38\n"
"v_exp_f32 v39, v39\n"
"v_exp_f32 v40, v40\n"
"v_exp_f32 v41, v41\n"
"v_exp_f32 v42, v42\n"
"v_exp_f32 v43, v43\n"
"v_exp_f32 v44, v44\n"
"v_exp_f32 v45, v45\n"
"v_exp_f32 v46, v46\n"
"v_exp_f32 v47, v47\n"
"s_setprio 2\n"
"s_waitcnt lgkmcnt(0)\n"
"v_smfmac_f32_32x32x32_bf16 v[48:63], v[80:83], v[16:23], v13\n"
"buffer_load_dwordx4 v[244:247], v14, s[4:7], s13 offen offset:1024 sc1\n"
"s_sub_u32 s13, s13, s24\n"
"v_smfmac_f32_32x32x32_bf16 v[48:63], v[84:87], v[24:31], v13\n"
"s_mov_b64 exec, s[18:19]\n"
"ds_read_b128 v[80:83], v12 offset:30720\n"
"ds_read_b128 v[84:87], v12 offset:31360\n"
"s_mov_b64 exec, -1\n"
"v_mfma_f32_32x32x8_bf16 v[16:31], v[70:71], v[68:69], 0\n"
"ds_read_b64 v[70:71], v11 offset:31232\n"
"buffer_load_dwordx4 v[232:235], v14, s[4:7], s13 offen sc1\n"
"v_mfma_f32_32x32x16_bf16 v[16:31], v[72:75], v[64:67], v[16:31]\n"
"ds_read_b128 v[72:75], v10 offset:31744\n"
"v_smfmac_f32_32x32x32_bf16 v[48:63], v[88:91], v[32:39], v13\n"
"buffer_load_dwordx4 v[236:239], v14, s[4:7], s13 offen offset:1024 sc1\n"
"s_sub_u32 s13, s13, s24\n"
"v_smfmac_f32_32x32x32_bf16 v[48:63], v[92:95], v[40:47], v13\n"
"s_mov_b64 exec, s[18:19]\n"
"ds_read_b128 v[88:91], v12 offset:32000\n"
"ds_read_b128 v[92:95], v12 offset:32640\n"
"s_mov_b64 exec, -1\n"
"v_mfma_f32_32x32x8_bf16 v[32:47], v[8:9], v[68:69], 0\n"
"ds_read_b64 v[8:9], v11 offset:31488\n"
"buffer_load_dwordx4 v[224:227], v14, s[4:7], s13 offen sc1\n"
"v_mfma_f32_32x32x16_bf16 v[32:47], v[76:79], v[64:67], v[32:47]\n"
"ds_read_b128 v[76:79], v10 offset:32256\n"
"s_setprio 0\n"
"v_exp_f32 v16, v16\n"
"v_exp_f32 v17, v17\n"
"s_setprio 1\n"
"v_exp_f32 v18, v18\n"
"v_exp_f32 v19, v19\n"
"v_exp_f32 v20, v20\n"
"v_exp_f32 v21, v21\n"
"v_exp_f32 v22, v22\n"
"v_exp_f32 v23, v23\n"
"v_exp_f32 v24, v24\n"
"v_exp_f32 v25, v25\n"
"v_exp_f32 v26, v26\n"
"v_exp_f32 v27, v27\n"
"v_exp_f32 v28, v28\n"
"v_exp_f32 v29, v29\n"
"v_exp_f32 v30, v30\n"
"v_exp_f32 v31, v31\n"
"v_exp_f32 v32, v32\n"
"v_exp_f32 v33, v33\n"
"v_exp_f32 v34, v34\n"
"v_exp_f32 v35, v35\n"
"v_exp_f32 v36, v36\n"
"v_exp_f32 v37, v37\n"
"v_exp_f32 v38, v38\n"
"v_exp_f32 v39, v39\n"
"v_exp_f32 v40, v40\n"
"v_exp_f32 v41, v41\n"
"v_exp_f32 v42, v42\n"
"v_exp_f32 v43, v43\n"
"v_exp_f32 v44, v44\n"
"v_exp_f32 v45, v45\n"
"v_exp_f32 v46, v46\n"
"v_exp_f32 v47, v47\n"
"s_setprio 2\n"
"s_waitcnt lgkmcnt(0)\n"
"v_smfmac_f32_32x32x32_bf16 v[48:63], v[80:83], v[16:23], v13\n"
"buffer_load_dwordx4 v[228:231], v14, s[4:7], s13 offen offset:1024 sc1\n"
"s_sub_u32 s13, s13, s24\n"
"v_smfmac_f32_32x32x32_bf16 v[48:63], v[84:87], v[24:31], v13\n"
"s_mov_b64 exec, s[18:19]\n"
"ds_read_b128 v[80:83], v12 offset:33280\n"
"ds_read_b128 v[84:87], v12 offset:33920\n"
"s_mov_b64 exec, -1\n"
"v_mfma_f32_32x32x8_bf16 v[16:31], v[70:71], v[68:69], 0\n"
"ds_read_b64 v[70:71], v11 offset:40960\n"
"buffer_load_dwordx4 v[216:219], v14, s[4:7], s13 offen sc1\n"
"v_mfma_f32_32x32x16_bf16 v[16:31], v[72:75], v[64:67], v[16:31]\n"
"ds_read_b128 v[72:75], v10 offset:40960\n"
"v_smfmac_f32_32x32x32_bf16 v[48:63], v[88:91], v[32:39], v13\n"
"buffer_load_dwordx4 v[220:223], v14, s[4:7], s13 offen offset:1024 sc1\n"
"s_sub_u32 s13, s13, s24\n"
"v_smfmac_f32_32x32x32_bf16 v[48:63], v[92:95], v[40:47], v13\n"
"s_mov_b64 exec, s[18:19]\n"
"ds_read_b128 v[88:91], v12 offset:34560\n"
"ds_read_b128 v[92:95], v12 offset:35200\n"
"s_mov_b64 exec, -1\n"
"v_mfma_f32_32x32x8_bf16 v[32:47], v[8:9], v[68:69], 0\n"
"ds_read_b64 v[8:9], v11 offset:41216\n"
"buffer_load_dwordx4 v[208:211], v14, s[4:7], s13 offen sc1\n"
"v_mfma_f32_32x32x16_bf16 v[32:47], v[76:79], v[64:67], v[32:47]\n"
"ds_read_b128 v[76:79], v10 offset:41472\n"
"s_setprio 0\n"
"v_exp_f32 v16, v16\n"
"v_exp_f32 v17, v17\n"
"s_setprio 1\n"
"v_exp_f32 v18, v18\n"
"v_exp_f32 v19, v19\n"
"v_exp_f32 v20, v20\n"
"v_exp_f32 v21, v21\n"
"v_exp_f32 v22, v22\n"
"v_exp_f32 v23, v23\n"
"v_exp_f32 v24, v24\n"
"v_exp_f32 v25, v25\n"
"v_exp_f32 v26, v26\n"
"v_exp_f32 v27, v27\n"
"v_exp_f32 v28, v28\n"
"v_exp_f32 v29, v29\n"
"v_exp_f32 v30, v30\n"
"v_exp_f32 v31, v31\n"
"v_exp_f32 v32, v32\n"
"v_exp_f32 v33, v33\n"
"v_exp_f32 v34, v34\n"
"v_exp_f32 v35, v35\n"
"v_exp_f32 v36, v36\n"
"v_exp_f32 v37, v37\n"
"v_exp_f32 v38, v38\n"
"v_exp_f32 v39, v39\n"
"v_exp_f32 v40, v40\n"
"v_exp_f32 v41, v41\n"
"v_exp_f32 v42, v42\n"
"v_exp_f32 v43, v43\n"
"v_exp_f32 v44, v44\n"
"v_exp_f32 v45, v45\n"
"v_exp_f32 v46, v46\n"
"v_exp_f32 v47, v47\n"
"s_setprio 2\n"
"s_waitcnt lgkmcnt(0)\n"
"v_smfmac_f32_32x32x32_bf16 v[48:63], v[80:83], v[16:23], v13\n"
"v_and_b32 v2, 31, v0\n"
"v_lshlrev_b32 v15, 10, v2\n"
"s_lshl_b32 s56, s34, 5\n"
"s_lshl_b32 s57, s56, 10\n"
"s_add_u32 s21, 0x41000, s57\n"
"buffer_load_dwordx2 v[6:7], v15, s[4:7], s21 offen sc1\n"
"v_smfmac_f32_32x32x32_bf16 v[48:63], v[84:87], v[24:31], v13\n"
"s_mov_b64 exec, s[18:19]\n"
"ds_read_b128 v[80:83], v12 offset:40960\n"
"ds_read_b128 v[84:87], v12 offset:41600\n"
"s_mov_b64 exec, -1\n"
"v_mfma_f32_32x32x8_bf16 v[16:31], v[70:71], v[68:69], 0\n"
"ds_read_b64 v[70:71], v11 offset:41472\n"
"buffer_load_dwordx4 v[212:215], v14, s[4:7], s13 offen offset:1024 sc1\n"
"s_sub_u32 s13, s13, s24\n"
"v_mfma_f32_32x32x16_bf16 v[16:31], v[72:75], v[64:67], v[16:31]\n"
"ds_read_b128 v[72:75], v10 offset:41984\n"
"v_smfmac_f32_32x32x32_bf16 v[48:63], v[88:91], v[32:39], v13\n"
"buffer_load_dwordx4 v[200:203], v14, s[4:7], s13 offen sc1\n"
"v_smfmac_f32_32x32x32_bf16 v[48:63], v[92:95], v[40:47], v13\n"
"s_mov_b64 exec, s[18:19]\n"
"ds_read_b128 v[88:91], v12 offset:42240\n"
"ds_read_b128 v[92:95], v12 offset:42880\n"
"s_mov_b64 exec, -1\n"
"v_mfma_f32_32x32x8_bf16 v[32:47], v[8:9], v[68:69], 0\n"
"ds_read_b64 v[8:9], v11 offset:41728\n"
"buffer_load_dwordx4 v[204:207], v14, s[4:7], s13 offen offset:1024 sc1\n"
"s_sub_u32 s13, s13, s24\n"
"s_add_u32 s38, s25, 248832\n"
"s_cmp_eq_u32 s34, 7\n"
"s_cselect_b32 s13, s38, s13\n"
"v_mfma_f32_32x32x16_bf16 v[32:47], v[76:79], v[64:67], v[32:47]\n"
"ds_read_b128 v[76:79], v10 offset:42496\n"
"s_setprio 0\n"
"v_exp_f32 v16, v16\n"
"v_exp_f32 v17, v17\n"
"s_setprio 1\n"
"v_exp_f32 v18, v18\n"
"v_exp_f32 v19, v19\n"
"v_exp_f32 v20, v20\n"
"v_exp_f32 v21, v21\n"
"v_exp_f32 v22, v22\n"
"v_exp_f32 v23, v23\n"
"v_exp_f32 v24, v24\n"
"v_exp_f32 v25, v25\n"
"v_exp_f32 v26, v26\n"
"v_exp_f32 v27, v27\n"
"v_exp_f32 v28, v28\n"
"v_exp_f32 v29, v29\n"
"v_exp_f32 v30, v30\n"
"v_exp_f32 v31, v31\n"
"v_exp_f32 v32, v32\n"
"v_exp_f32 v33, v33\n"
"v_exp_f32 v34, v34\n"
"v_exp_f32 v35, v35\n"
"v_exp_f32 v36, v36\n"
"v_exp_f32 v37, v37\n"
"v_exp_f32 v38, v38\n"
"v_exp_f32 v39, v39\n"
"v_exp_f32 v40, v40\n"
"v_exp_f32 v41, v41\n"
"v_exp_f32 v42, v42\n"
"v_exp_f32 v43, v43\n"
"v_exp_f32 v44, v44\n"
"v_exp_f32 v45, v45\n"
"v_exp_f32 v46, v46\n"
"v_exp_f32 v47, v47\n"
"s_setprio 2\n"
"s_waitcnt lgkmcnt(0)\n"
"v_smfmac_f32_32x32x32_bf16 v[48:63], v[80:83], v[16:23], v13\n"
"buffer_load_dwordx4 v[192:195], v14, s[4:7], s13 offen sc1\n"
"v_smfmac_f32_32x32x32_bf16 v[48:63], v[84:87], v[24:31], v13\n"
"s_mov_b64 exec, s[18:19]\n"
"ds_read_b128 v[80:83], v12 offset:43520\n"
"ds_read_b128 v[84:87], v12 offset:44160\n"
"s_mov_b64 exec, -1\n"
"v_mfma_f32_32x32x8_bf16 v[16:31], v[70:71], v[68:69], 0\n"
"ds_read_b64 v[70:71], v11 offset:51200\n"
"buffer_load_dwordx4 v[196:199], v14, s[4:7], s13 offen offset:1024 sc1\n"
"s_sub_u32 s13, s13, s24\n"
"v_mfma_f32_32x32x16_bf16 v[16:31], v[72:75], v[64:67], v[16:31]\n"
"ds_read_b128 v[72:75], v10 offset:51200\n"
"v_smfmac_f32_32x32x32_bf16 v[48:63], v[88:91], v[32:39], v13\n"
"buffer_load_dwordx4 v[184:187], v14, s[4:7], s13 offen sc1\n"
"v_smfmac_f32_32x32x32_bf16 v[48:63], v[92:95], v[40:47], v13\n"
"s_mov_b64 exec, s[18:19]\n"
"ds_read_b128 v[88:91], v12 offset:44800\n"
"ds_read_b128 v[92:95], v12 offset:45440\n"
"s_cbranch_execz .Lk_tramp\n"
"s_nop 0\n"
"s_mov_b64 exec, -1\n"
"v_mfma_f32_32x32x8_bf16 v[32:47], v[8:9], v[68:69], 0\n"
"ds_read_b64 v[8:9], v11 offset:51456\n"
"buffer_load_dwordx4 v[188:191], v14, s[4:7], s13 offen offset:1024 sc1\n"
"s_sub_u32 s13, s13, s24\n"
"v_mfma_f32_32x32x16_bf16 v[32:47], v[76:79], v[64:67], v[32:47]\n"
"ds_read_b128 v[76:79], v10 offset:51712\n"
"s_setprio 0\n"
"v_exp_f32 v16, v16\n"
"v_exp_f32 v17, v17\n"
"s_setprio 1\n"
"v_exp_f32 v18, v18\n"
"v_exp_f32 v19, v19\n"
"v_exp_f32 v20, v20\n"
"v_exp_f32 v21, v21\n"
"v_exp_f32 v22, v22\n"
"v_exp_f32 v23, v23\n"
"v_exp_f32 v24, v24\n"
"v_exp_f32 v25, v25\n"
"v_exp_f32 v26, v26\n"
"v_exp_f32 v27, v27\n"
"v_exp_f32 v28, v28\n"
"v_exp_f32 v29, v29\n"
"v_exp_f32 v30, v30\n"
"v_exp_f32 v31, v31\n"
"v_exp_f32 v32, v32\n"
"v_exp_f32 v33, v33\n"
"v_exp_f32 v34, v34\n"
"v_exp_f32 v35, v35\n"
"v_exp_f32 v36, v36\n"
"v_exp_f32 v37, v37\n"
"v_exp_f32 v38, v38\n"
"v_exp_f32 v39, v39\n"
"v_exp_f32 v40, v40\n"
"v_exp_f32 v41, v41\n"
"v_exp_f32 v42, v42\n"
"v_exp_f32 v43, v43\n"
"v_exp_f32 v44, v44\n"
"v_exp_f32 v45, v45\n"
"v_exp_f32 v46, v46\n"
"v_exp_f32 v47, v47\n"
"s_setprio 2\n"
"s_waitcnt lgkmcnt(0)\n"
"v_add_u32 v10, 0xc800, v10\n"
"v_add_u32 v11, 0xc800, v11\n"
"v_add_u32 v12, 0xc800, v12\n"
"v_smfmac_f32_32x32x32_bf16 v[48:63], v[80:83], v[16:23], v13\n"
"s_waitcnt vmcnt(7)\n"
"v_cmp_ne_u64 vcc, s[32:33], v[6:7]\n"
"s_nop 0\n"
"s_cmp_eq_u64 vcc, 0\n"
"s_cbranch_scc1 .Lk_polld_40\n"
".Lk_poll_39:\n"
"s_sleep 4\n"
"buffer_load_dwordx2 v[6:7], v15, s[4:7], s21 offen sc1\n"
"s_waitcnt vmcnt(0)\n"
"v_cmp_ne_u64 vcc, s[32:33], v[6:7]\n"
"s_nop 0\n"
"s_cmp_eq_u64 vcc, 0\n"
"s_cbranch_scc1 .Lk_polld_40\n"
"s_sub_u32 s37, s37, 1\n"
"s_cmp_gt_i32 s37, 0\n"
"s_cbranch_scc1 .Lk_poll_39\n"
".Lk_polld_40:\n"
"v_smfmac_f32_32x32x32_bf16 v[48:63], v[84:87], v[24:31], v13\n"
"s_mov_b64 exec, s[18:19]\n"
"ds_read_b128 v[80:83], v12\n"
"ds_read_b128 v[84:87], v12 offset:640\n"
"s_mov_b64 exec, -1\n"
"v_mfma_f32_32x32x8_bf16 v[16:31], v[70:71], v[68:69], 0\n"
"ds_read_b64 v[70:71], v11 offset:512\n"
"buffer_load_dwordx4 v[176:179], v14, s[4:7], s13 offen sc1\n"
"v_mfma_f32_32x32x16_bf16 v[16:31], v[72:75], v[64:67], v[16:31]\n"
"ds_read_b128 v[72:75], v10 offset:1024\n"
"v_smfmac_f32_32x32x32_bf16 v[48:63], v[88:91], v[32:39], v13\n"
"buffer_load_dwordx4 v[180:183], v14, s[4:7], s13 offen offset:1024 sc1\n"
"s_sub_u32 s13, s13, s24\n"
"v_smfmac_f32_32x32x32_bf16 v[48:63], v[92:95], v[40:47], v13\n"
"s_mov_b64 exec, s[18:19]\n"
"ds_read_b128 v[88:91], v12 offset:1280\n"
"ds_read_b128 v[92:95], v12 offset:1920\n"
"s_mov_b64 exec, -1\n"
"v_mfma_f32_32x32x8_bf16 v[32:47], v[8:9], v[68:69], 0\n"
"ds_read_b64 v[8:9], v11 offset:768\n"
"buffer_load_dwordx4 v[168:171], v14, s[4:7], s13 offen sc1\n"
"v_mfma_f32_32x32x16_bf16 v[32:47], v[76:79], v[64:67], v[32:47]\n"
"ds_read_b128 v[76:79], v10 offset:1536\n"
"s_setprio 0\n"
"v_exp_f32 v16, v16\n"
"v_exp_f32 v17, v17\n"
"s_setprio 1\n"
"v_exp_f32 v18, v18\n"
"v_exp_f32 v19, v19\n"
"v_exp_f32 v20, v20\n"
"v_exp_f32 v21, v21\n"
"v_exp_f32 v22, v22\n"
"v_exp_f32 v23, v23\n"
"v_exp_f32 v24, v24\n"
"v_exp_f32 v25, v25\n"
"v_exp_f32 v26, v26\n"
"v_exp_f32 v27, v27\n"
"v_exp_f32 v28, v28\n"
"v_exp_f32 v29, v29\n"
"v_exp_f32 v30, v30\n"
"v_exp_f32 v31, v31\n"
"v_exp_f32 v32, v32\n"
"v_exp_f32 v33, v33\n"
"v_exp_f32 v34, v34\n"
"v_exp_f32 v35, v35\n"
"v_exp_f32 v36, v36\n"
"v_exp_f32 v37, v37\n"
"v_exp_f32 v38, v38\n"
"v_exp_f32 v39, v39\n"
"v_exp_f32 v40, v40\n"
"v_exp_f32 v41, v41\n"
"v_exp_f32 v42, v42\n"
"v_exp_f32 v43, v43\n"
"v_exp_f32 v44, v44\n"
"v_exp_f32 v45, v45\n"
"v_exp_f32 v46, v46\n"
"v_exp_f32 v47, v47\n"
"s_setprio 2\n"
"s_barrier\n"
"s_waitcnt lgkmcnt(0)\n"
"v_smfmac_f32_32x32x32_bf16 v[48:63], v[80:83], v[16:23], v13\n"
"s_mov_b32 m0, s15\n"
"s_nop 0\n"
"buffer_load_dwordx4 v14, s[4:7], s14 offen sc1 lds\n"
"s_add_u32 s14, s14, 0x2000\n"
"s_add_u32 s15, s15, 0x2000\n"
"v_smfmac_f32_32x32x32_bf16 v[48:63], v[84:87], v[24:31], v13\n"
"s_mov_b64 exec, s[18:19]\n"
"ds_read_b128 v[80:83], v12 offset:2560\n"
"ds_read_b128 v[84:87], v12 offset:3200\n"
"s_mov_b64 exec, -1\n"
"v_mfma_f32_32x32x8_bf16 v[16:31], v[70:71], v[68:69], 0\n"
"ds_read_b64 v[70:71], v11 offset:10240\n"
"s_mov_b32 m0, s15\n"
"s_nop 0\n"
"buffer_load_dwordx4 v14, s[4:7], s14 offen sc1 lds\n"
"s_add_u32 s14, s14, 0x2000\n"
"s_add_u32 s15, s15, 0x2000\n"
"v_mfma_f32_32x32x16_bf16 v[16:31], v[72:75], v[64:67], v[16:31]\n"
"ds_read_b128 v[72:75], v10 offset:10240\n"
"v_smfmac_f32_32x32x32_bf16 v[48:63], v[88:91], v[32:39], v13\n"
"buffer_load_dwordx4 v[172:175], v14, s[4:7], s13 offen offset:1024 sc1\n"
"s_sub_u32 s13, s13, s24\n"
"v_smfmac_f32_32x32x32_bf16 v[48:63], v[92:95], v[40:47], v13\n"
"s_mov_b64 exec, s[18:19]\n"
"ds_read_b128 v[88:91], v12 offset:3840\n"
"ds_read_b128 v[92:95], v12 offset:4480\n"
"s_mov_b64 exec, -1\n"
"v_mfma_f32_32x32x8_bf16 v[32:47], v[8:9], v[68:69], 0\n"
"ds_read_b64 v[8:9], v11 offset:10496\n"
"buffer_load_dwordx4 v[160:163], v14, s[4:7], s13 offen sc1\n"
"v_mfma_f32_32x32x16_bf16 v[32:47], v[76:79], v[64:67], v[32:47]\n"
"ds_read_b128 v[76:79], v10 offset:10752\n"
"s_setprio 0\n"
"v_exp_f32 v16, v16\n"
"v_exp_f32 v17, v17\n"
"s_setprio 1\n"
"v_exp_f32 v18, v18\n"
"v_exp_f32 v19, v19\n"
"v_exp_f32 v20, v20\n"
"v_exp_f32 v21, v21\n"
"v_exp_f32 v22, v22\n"
"v_exp_f32 v23, v23\n"
"v_exp_f32 v24, v24\n"
"v_exp_f32 v25, v25\n"
"v_exp_f32 v26, v26\n"
"v_exp_f32 v27, v27\n"
"v_exp_f32 v28, v28\n"
"v_exp_f32 v29, v29\n"
"v_exp_f32 v30, v30\n"
"v_exp_f32 v31, v31\n"
"v_exp_f32 v32, v32\n"
"v_exp_f32 v33, v33\n"
"v_exp_f32 v34, v34\n"
"v_exp_f32 v35, v35\n"
"v_exp_f32 v36, v36\n"
"v_exp_f32 v37, v37\n"
"v_exp_f32 v38, v38\n"
"v_exp_f32 v39, v39\n"
"v_exp_f32 v40, v40\n"
"v_exp_f32 v41, v41\n"
"v_exp_f32 v42, v42\n"
"v_exp_f32 v43, v43\n"
"v_exp_f32 v44, v44\n"
"v_exp_f32 v45, v45\n"
"v_exp_f32 v46, v46\n"
"v_exp_f32 v47, v47\n"
"s_setprio 2\n"
"s_waitcnt lgkmcnt(0)\n"
"v_smfmac_f32_32x32x32_bf16 v[48:63], v[80:83], v[16:23], v13\n"
"s_mov_b32 m0, s15\n"
"s_nop 0\n"
"buffer_load_dwordx4 v14, s[4:7], s14 offen sc1 lds\n"
"s_add_u32 s14, s14, 0x2000\n"
"s_add_u32 s15, s15, 0x2000\n"
"v_smfmac_f32_32x32x32_bf16 v[48:63], v[84:87], v[24:31], v13\n"
"s_lshr_b32 s56, s40, 1\n"
"s_lshl_b32 s57, s56, 12\n"
"s_and_b32 s58, s34, 1\n"
"s_mov_b64 exec, s[18:19]\n"
"ds_read_b128 v[80:83], v12 offset:10240\n"
"ds_read_b128 v[84:87], v12 offset:10880\n"
"s_mov_b64 exec, -1\n"
"v_mfma_f32_32x32x8_bf16 v[16:31], v[70:71], v[68:69], 0\n"
"ds_read_b64 v[70:71], v11 offset:10752\n"
"s_mov_b32 m0, s15\n"
"s_nop 0\n"
"buffer_load_dwordx4 v14, s[4:7], s14 offen sc1 lds\n"
"s_add_u32 s14, s14, 0x2000\n"
"s_add_u32 s15, s15, 0x2000\n"
"v_mfma_f32_32x32x16_bf16 v[16:31], v[72:75], v[64:67], v[16:31]\n"
"ds_read_b128 v[72:75], v10 offset:11264\n"
"v_smfmac_f32_32x32x32_bf16 v[48:63], v[88:91], v[32:39], v13\n"
"buffer_load_dwordx4 v[164:167], v14, s[4:7], s13 offen offset:1024 sc1\n"
"s_sub_u32 s13, s13, s24\n"
"v_smfmac_f32_32x32x32_bf16 v[48:63], v[92:95], v[40:47], v13\n"
"s_mov_b64 exec, s[18:19]\n"
"ds_read_b128 v[88:91], v12 offset:11520\n"
"ds_read_b128 v[92:95], v12 offset:12160\n"
"s_mov_b64 exec, -1\n"
"v_mfma_f32_32x32x8_bf16 v[32:47], v[8:9], v[68:69], 0\n"
"ds_read_b64 v[8:9], v11 offset:11008\n"
"buffer_load_dwordx4 v[152:155], v14, s[4:7], s13 offen sc1\n"
"v_mfma_f32_32x32x16_bf16 v[32:47], v[76:79], v[64:67], v[32:47]\n"
"ds_read_b128 v[76:79], v10 offset:11776\n"
"s_setprio 0\n"
"v_exp_f32 v16, v16\n"
"v_exp_f32 v17, v17\n"
"s_setprio 1\n"
"v_exp_f32 v18, v18\n"
"v_exp_f32 v19, v19\n"
"v_exp_f32 v20, v20\n"
"v_exp_f32 v21, v21\n"
"v_exp_f32 v22, v22\n"
"v_exp_f32 v23, v23\n"
"v_exp_f32 v24, v24\n"
"v_exp_f32 v25, v25\n"
"v_exp_f32 v26, v26\n"
"v_exp_f32 v27, v27\n"
"v_exp_f32 v28, v28\n"
"v_exp_f32 v29, v29\n"
"v_exp_f32 v30, v30\n"
"v_exp_f32 v31, v31\n"
"v_exp_f32 v32, v32\n"
"v_exp_f32 v33, v33\n"
"v_exp_f32 v34, v34\n"
"v_exp_f32 v35, v35\n"
"v_exp_f32 v36, v36\n"
"v_exp_f32 v37, v37\n"
"v_exp_f32 v38, v38\n"
"v_exp_f32 v39, v39\n"
"v_exp_f32 v40, v40\n"
"v_exp_f32 v41, v41\n"
"v_exp_f32 v42, v42\n"
"v_exp_f32 v43, v43\n"
"v_exp_f32 v44, v44\n"
"v_exp_f32 v45, v45\n"
"v_exp_f32 v46, v46\n"
"v_exp_f32 v47, v47\n"
"s_setprio 2\n"
"s_waitcnt lgkmcnt(0)\n"
"v_smfmac_f32_32x32x32_bf16 v[48:63], v[80:83], v[16:23], v13\n"
"s_mov_b32 m0, s15\n"
"s_nop 0\n"
"buffer_load_dwordx4 v14, s[4:7], s14 offen sc1 lds\n"
"s_add_u32 s14, s14, 0x2000\n"
"s_add_u32 s15, s15, 0x2000\n"
"v_smfmac_f32_32x32x32_bf16 v[48:63], v[84:87], v[24:31], v13\n"
"s_lshl_b32 s59, s58, 11\n"
"s_add_u32 s60, s57, s59\n"
"s_and_b32 s61, s40, 1\n"
"s_lshl_b32 s56, s61, 9\n"
"s_mov_b64 exec, s[18:19]\n"
"ds_read_b128 v[80:83], v12 offset:12800\n"
"ds_read_b128 v[84:87], v12 offset:13440\n"
"s_mov_b64 exec, -1\n"
"v_mfma_f32_32x32x8_bf16 v[16:31], v[70:71], v[68:69], 0\n"
"ds_read_b64 v[70:71], v11 offset:20480\n"
"s_mov_b32 m0, s15\n"
"s_nop 0\n"
"buffer_load_dwordx4 v14, s[4:7], s14 offen sc1 lds\n"
"s_add_u32 s14, s14, 0x2000\n"
"s_add_u32 s15, s15, 0x2000\n"
"v_mfma_f32_32x32x16_bf16 v[16:31], v[72:75], v[64:67], v[16:31]\n"
"ds_read_b128 v[72:75], v10 offset:20480\n"
"v_smfmac_f32_32x32x32_bf16 v[48:63], v[88:91], v[32:39], v13\n"
"buffer_load_dwordx4 v[156:159], v14, s[4:7], s13 offen offset:1024 sc1\n"
"s_sub_u32 s13, s13, s24\n"
"v_smfmac_f32_32x32x32_bf16 v[48:63], v[92:95], v[40:47], v13\n"
"s_mov_b64 exec, s[18:19]\n"
"ds_read_b128 v[88:91], v12 offset:14080\n"
"ds_read_b128 v[92:95], v12 offset:14720\n"
"s_mov_b64 exec, -1\n"
"v_mfma_f32_32x32x8_bf16 v[32:47], v[8:9], v[68:69], 0\n"
"ds_read_b64 v[8:9], v11 offset:20736\n"
"buffer_load_dwordx4 v[144:147], v14, s[4:7], s13 offen sc1\n"
"v_mfma_f32_32x32x16_bf16 v[32:47], v[76:79], v[64:67], v[32:47]\n"
"ds_read_b128 v[76:79], v10 offset:20992\n"
"s_setprio 0\n"
"v_exp_f32 v16, v16\n"
"v_exp_f32 v17, v17\n"
"s_setprio 1\n"
"v_exp_f32 v18, v18\n"
"v_exp_f32 v19, v19\n"
"v_exp_f32 v20, v20\n"
"v_exp_f32 v21, v21\n"
"v_exp_f32 v22, v22\n"
"v_exp_f32 v23, v23\n"
"v_exp_f32 v24, v24\n"
"v_exp_f32 v25, v25\n"
"v_exp_f32 v26, v26\n"
"v_exp_f32 v27, v27\n"
"v_exp_f32 v28, v28\n"
"v_exp_f32 v29, v29\n"
"v_exp_f32 v30, v30\n"
"v_exp_f32 v31, v31\n"
"v_exp_f32 v32, v32\n"
"v_exp_f32 v33, v33\n"
"v_exp_f32 v34, v34\n"
"v_exp_f32 v35, v35\n"
"v_exp_f32 v36, v36\n"
"v_exp_f32 v37, v37\n"
"v_exp_f32 v38, v38\n"
"v_exp_f32 v39, v39\n"
"v_exp_f32 v40, v40\n"
"v_exp_f32 v41, v41\n"
"v_exp_f32 v42, v42\n"
"v_exp_f32 v43, v43\n"
"v_exp_f32 v44, v44\n"
"v_exp_f32 v45, v45\n"
"v_exp_f32 v46, v46\n"
"v_exp_f32 v47, v47\n"
"s_setprio 2\n"
"s_waitcnt lgkmcnt(0)\n"
"v_smfmac_f32_32x32x32_bf16 v[48:63], v[80:83], v[16:23], v13\n"
"s_mov_b32 m0, s15\n"
"s_nop 0\n"
"buffer_load_dwordx4 v14, s[4:7], s14 offen sc1 lds\n"
"s_add_u32 s14, s14, 0x2000\n"
"s_add_u32 s15, s15, 0x2000\n"
"v_smfmac_f32_32x32x32_bf16 v[48:63], v[84:87], v[24:31], v13\n"
"s_add_u32 s58, s60, s56\n"
"v_lshrrev_b32 v2, 4, v0\n"
"v_and_b32 v3, 1, v2\n"
"s_mov_b64 exec, s[18:19]\n"
"ds_read_b128 v[80:83], v12 offset:20480\n"
"ds_read_b128 v[84:87], v12 offset:21120\n"
"s_mov_b64 exec, -1\n"
"v_mfma_f32_32x32x8_bf16 v[16:31], v[70:71], v[68:69], 0\n"
"ds_read_b64 v[70:71], v11 offset:20992\n"
"s_mov_b32 m0, s15\n"
"s_nop 0\n"
"buffer_load_dwordx4 v14, s[4:7], s14 offen sc1 lds\n"
"s_add_u32 s14, s14, 0x2000\n"
"s_add_u32 s15, s15, 0x2000\n"
"v_mfma_f32_32x32x16_bf16 v[16:31], v[72:75], v[64:67], v[16:31]\n"
"ds_read_b128 v[72:75], v10 offset:21504\n"
"v_smfmac_f32_32x32x32_bf16 v[48:63], v[88:91], v[32:39], v13\n"
"buffer_load_dwordx4 v[148:151], v14, s[4:7], s13 offen offset:1024 sc1\n"
"s_sub_u32 s13, s13, s24\n"
"v_smfmac_f32_32x32x32_bf16 v[48:63], v[92:95], v[40:47], v13\n"
"s_mov_b64 exec, s[18:19]\n"
"ds_read_b128 v[88:91], v12 offset:21760\n"
"ds_read_b128 v[92:95], v12 offset:22400\n"
"s_mov_b64 exec, -1\n"
"v_mfma_f32_32x32x8_bf16 v[32:47], v[8:9], v[68:69], 0\n"
"ds_read_b64 v[8:9], v11 offset:21248\n"
"buffer_load_dwordx4 v[136:139], v14, s[4:7], s13 offen sc1\n"
"v_mfma_f32_32x32x16_bf16 v[32:47], v[76:79], v[64:67], v[32:47]\n"
"ds_read_b128 v[76:79], v10 offset:22016\n"
"s_setprio 0\n"
"v_exp_f32 v16, v16\n"
"v_exp_f32 v17, v17\n"
"s_setprio 1\n"
"v_exp_f32 v18, v18\n"
"v_exp_f32 v19, v19\n"
"v_exp_f32 v20, v20\n"
"v_exp_f32 v21, v21\n"
"v_exp_f32 v22, v22\n"
"v_exp_f32 v23, v23\n"
"v_exp_f32 v24, v24\n"
"v_exp_f32 v25, v25\n"
"v_exp_f32 v26, v26\n"
"v_exp_f32 v27, v27\n"
"v_exp_f32 v28, v28\n"
"v_exp_f32 v29, v29\n"
"v_exp_f32 v30, v30\n"
"v_exp_f32 v31, v31\n"
"v_exp_f32 v32, v32\n"
"v_exp_f32 v33, v33\n"
"v_exp_f32 v34, v34\n"
"v_exp_f32 v35, v35\n"
"v_exp_f32 v36, v36\n"
"v_exp_f32 v37, v37\n"
"v_exp_f32 v38, v38\n"
"v_exp_f32 v39, v39\n"
"v_exp_f32 v40, v40\n"
"v_exp_f32 v41, v41\n"
"v_exp_f32 v42, v42\n"
"v_exp_f32 v43, v43\n"
"v_exp_f32 v44, v44\n"
"v_exp_f32 v45, v45\n"
"v_exp_f32 v46, v46\n"
"v_exp_f32 v47, v47\n"
"s_setprio 2\n"
"s_waitcnt lgkmcnt(0)\n"
"v_smfmac_f32_32x32x32_bf16 v[48:63], v[80:83], v[16:23], v13\n"
"s_mov_b32 m0, s15\n"
"s_nop 0\n"
"buffer_load_dwordx4 v14, s[4:7], s14 offen sc1 lds\n"
"s_add_u32 s14, s14, 0x2000\n"
"s_add_u32 s15, s15, 0x2000\n"
"v_smfmac_f32_32x32x32_bf16 v[48:63], v[84:87], v[24:31], v13\n"
"v_lshlrev_b32 v4, 10, v3\n"
"v_add_u32 v5, s58, v4\n"
"v_lshrrev_b32 v2, 5, v0\n"
"v_lshlrev_b32 v3, 8, v2\n"
"s_mov_b64 exec, s[18:19]\n"
"ds_read_b128 v[80:83], v12 offset:23040\n"
"ds_read_b128 v[84:87], v12 offset:23680\n"
"s_mov_b64 exec, -1\n"
"v_mfma_f32_32x32x8_bf16 v[16:31], v[70:71], v[68:69], 0\n"
"ds_read_b64 v[70:71], v11 offset:30720\n"
"s_mov_b32 m0, s15\n"
"s_nop 0\n"
"buffer_load_dwordx4 v14, s[4:7], s14 offen sc1 lds\n"
"s_add_u32 s14, s14, 0x2000\n"
"s_add_u32 s15, s15, 0x2000\n"
"v_mfma_f32_32x32x16_bf16 v[16:31], v[72:75], v[64:67], v[16:31]\n"
"ds_read_b128 v[72:75], v10 offset:30720\n"
"v_smfmac_f32_32x32x32_bf16 v[48:63], v[88:91], v[32:39], v13\n"
"buffer_load_dwordx4 v[140:143], v14, s[4:7], s13 offen offset:1024 sc1\n"
"s_sub_u32 s13, s13, s24\n"
"v_smfmac_f32_32x32x32_bf16 v[48:63], v[92:95], v[40:47], v13\n"
"s_mov_b64 exec, s[18:19]\n"
"ds_read_b128 v[88:91], v12 offset:24320\n"
"ds_read_b128 v[92:95], v12 offset:24960\n"
"s_mov_b64 exec, -1\n"
"v_mfma_f32_32x32x8_bf16 v[32:47], v[8:9], v[68:69], 0\n"
"ds_read_b64 v[8:9], v11 offset:30976\n"
"buffer_load_dwordx4 v[128:131], v14, s[4:7], s13 offen sc1\n"
"v_mfma_f32_32x32x16_bf16 v[32:47], v[76:79], v[64:67], v[32:47]\n"
"ds_read_b128 v[76:79], v10 offset:31232\n"
"s_setprio 0\n"
"v_exp_f32 v16, v16\n"
"v_exp_f32 v17, v17\n"
"s_setprio 1\n"
"v_exp_f32 v18, v18\n"
"v_exp_f32 v19, v19\n"
"v_exp_f32 v20, v20\n"
"v_exp_f32 v21, v21\n"
"v_exp_f32 v22, v22\n"
"v_exp_f32 v23, v23\n"
"v_exp_f32 v24, v24\n"
"v_exp_f32 v25, v25\n"
"v_exp_f32 v26, v26\n"
"v_exp_f32 v27, v27\n"
"v_exp_f32 v28, v28\n"
"v_exp_f32 v29, v29\n"
"v_exp_f32 v30, v30\n"
"v_exp_f32 v31, v31\n"
"v_exp_f32 v32, v32\n"
"v_exp_f32 v33, v33\n"
"v_exp_f32 v34, v34\n"
"v_exp_f32 v35, v35\n"
"v_exp_f32 v36, v36\n"
"v_exp_f32 v37, v37\n"
"v_exp_f32 v38, v38\n"
"v_exp_f32 v39, v39\n"
"v_exp_f32 v40, v40\n"
"v_exp_f32 v41, v41\n"
"v_exp_f32 v42, v42\n"
"v_exp_f32 v43, v43\n"
"v_exp_f32 v44, v44\n"
"v_exp_f32 v45, v45\n"
"v_exp_f32 v46, v46\n"
"v_exp_f32 v47, v47\n"
"s_setprio 2\n"
"s_waitcnt lgkmcnt(0)\n"
"v_smfmac_f32_32x32x32_bf16 v[48:63], v[80:83], v[16:23], v13\n"
"s_mov_b32 m0, s15\n"
"s_nop 0\n"
"buffer_load_dwordx4 v14, s[4:7], s14 offen sc1 lds\n"
"s_add_u32 s14, s14, 0x2000\n"
"s_add_u32 s15, s15, 0x2000\n"
"v_smfmac_f32_32x32x32_bf16 v[48:63], v[84:87], v[24:31], v13\n"
"v_add_u32 v4, v5, v3\n"
"v_and_b32 v2, 15, v0\n"
"v_lshlrev_b32 v5, 4, v2\n"
"s_mov_b64 exec, s[18:19]\n"
"ds_read_b128 v[80:83], v12 offset:30720\n"
"ds_read_b128 v[84:87], v12 offset:31360\n"
"s_mov_b64 exec, -1\n"
"v_mfma_f32_32x32x8_bf16 v[16:31], v[70:71], v[68:69], 0\n"
"ds_read_b64 v[70:71], v11 offset:31232\n"
"s_mov_b32 m0, s15\n"
"s_nop 0\n"
"buffer_load_dwordx4 v14, s[4:7], s14 offen sc1 lds\n"
"s_add_u32 s14, s14, 0x2000\n"
"s_add_u32 s15, s15, 0x2000\n"
"v_mfma_f32_32x32x16_bf16 v[16:31], v[72:75], v[64:67], v[16:31]\n"
"ds_read_b128 v[72:75], v10 offset:31744\n"
"v_smfmac_f32_32x32x32_bf16 v[48:63], v[88:91], v[32:39], v13\n"
"buffer_load_dwordx4 v[132:135], v14, s[4:7], s13 offen offset:1024 sc1\n"
"s_sub_u32 s13, s13, s24\n"
"v_smfmac_f32_32x32x32_bf16 v[48:63], v[92:95], v[40:47], v13\n"
"s_mov_b64 exec, s[18:19]\n"
"ds_read_b128 v[88:91], v12 offset:32000\n"
"ds_read_b128 v[92:95], v12 offset:32640\n"
"s_mov_b64 exec, -1\n"
"v_mfma_f32_32x32x8_bf16 v[32:47], v[8:9], v[68:69], 0\n"
"ds_read_b64 v[8:9], v11 offset:31488\n"
"buffer_load_dwordx4 v[120:123], v14, s[4:7], s13 offen sc1\n"
"v_mfma_f32_32x32x16_bf16 v[32:47], v[76:79], v[64:67], v[32:47]\n"
"ds_read_b128 v[76:79], v10 offset:32256\n"
"s_setprio 0\n"
"v_exp_f32 v16, v16\n"
"v_exp_f32 v17, v17\n"
"s_setprio 1\n"
"v_exp_f32 v18, v18\n"
"v_exp_f32 v19, v19\n"
"v_exp_f32 v20, v20\n"
"v_exp_f32 v21, v21\n"
"v_exp_f32 v22, v22\n"
"v_exp_f32 v23, v23\n"
"v_exp_f32 v24, v24\n"
"v_exp_f32 v25, v25\n"
"v_exp_f32 v26, v26\n"
"v_exp_f32 v27, v27\n"
"v_exp_f32 v28, v28\n"
"v_exp_f32 v29, v29\n"
"v_exp_f32 v30, v30\n"
"v_exp_f32 v31, v31\n"
"v_exp_f32 v32, v32\n"
"v_exp_f32 v33, v33\n"
"v_exp_f32 v34, v34\n"
"v_exp_f32 v35, v35\n"
"v_exp_f32 v36, v36\n"
"v_exp_f32 v37, v37\n"
"v_exp_f32 v38, v38\n"
"v_exp_f32 v39, v39\n"
"v_exp_f32 v40, v40\n"
"v_exp_f32 v41, v41\n"
"v_exp_f32 v42, v42\n"
"v_exp_f32 v43, v43\n"
"v_exp_f32 v44, v44\n"
"v_exp_f32 v45, v45\n"
"v_exp_f32 v46, v46\n"
"v_exp_f32 v47, v47\n"
"s_setprio 2\n"
"s_waitcnt lgkmcnt(0)\n"
"v_smfmac_f32_32x32x32_bf16 v[48:63], v[80:83], v[16:23], v13\n"
"s_mov_b32 m0, s15\n"
"s_nop 0\n"
"buffer_load_dwordx4 v14, s[4:7], s14 offen sc1 lds\n"
"s_add_u32 s14, s14, 0x2000\n"
"s_add_u32 s15, s15, 0x2000\n"
"v_smfmac_f32_32x32x32_bf16 v[48:63], v[84:87], v[24:31], v13\n"
"v_add_u32 v6, v4, v5\n"
"s_lshl_b32 s56, s40, 9\n"
"s_add_u32 s57, 0x10000, s56\n"
"s_and_b32 s58, s34, 1\n"
"s_mov_b64 exec, s[18:19]\n"
"ds_read_b128 v[80:83], v12 offset:33280\n"
"ds_read_b128 v[84:87], v12 offset:33920\n"
"s_mov_b64 exec, -1\n"
"v_mfma_f32_32x32x8_bf16 v[16:31], v[70:71], v[68:69], 0\n"
"ds_read_b64 v[70:71], v11 offset:40960\n"
"buffer_load_dwordx4 v[124:127], v14, s[4:7], s13 offen offset:1024 sc1\n"
"s_sub_u32 s13, s13, s24\n"
"v_mfma_f32_32x32x16_bf16 v[16:31], v[72:75], v[64:67], v[16:31]\n"
"ds_read_b128 v[72:75], v10 offset:40960\n"
"v_smfmac_f32_32x32x32_bf16 v[48:63], v[88:91], v[32:39], v13\n"
"buffer_load_dwordx4 v[112:115], v14, s[4:7], s13 offen sc1\n"
"v_smfmac_f32_32x32x32_bf16 v[48:63], v[92:95], v[40:47], v13\n"
"s_mov_b64 exec, s[18:19]\n"
"ds_read_b128 v[88:91], v12 offset:34560\n"
"ds_read_b128 v[92:95], v12 offset:35200\n"
"s_mov_b64 exec, -1\n"
"v_mfma_f32_32x32x8_bf16 v[32:47], v[8:9], v[68:69], 0\n"
"ds_read_b64 v[8:9], v11 offset:41216\n"
"buffer_load_dwordx4 v[116:119], v14, s[4:7], s13 offen offset:1024 sc1\n"
"s_sub_u32 s13, s13, s24\n"
"v_mfma_f32_32x32x16_bf16 v[32:47], v[76:79], v[64:67], v[32:47]\n"
"ds_read_b128 v[76:79], v10 offset:41472\n"
"s_setprio 0\n"
"v_exp_f32 v16, v16\n"
"v_exp_f32 v17, v17\n"
"s_setprio 1\n"
"v_exp_f32 v18, v18\n"
"v_exp_f32 v19, v19\n"
"v_exp_f32 v20, v20\n"
"v_exp_f32 v21, v21\n"
"v_exp_f32 v22, v22\n"
"v_exp_f32 v23, v23\n"
"v_exp_f32 v24, v24\n"
"v_exp_f32 v25, v25\n"
"v_exp_f32 v26, v26\n"
"v_exp_f32 v27, v27\n"
"v_exp_f32 v28, v28\n"
"v_exp_f32 v29, v29\n"
"v_exp_f32 v30, v30\n"
"v_exp_f32 v31, v31\n"
"v_exp_f32 v32, v32\n"
"v_exp_f32 v33, v33\n"
"v_exp_f32 v34, v34\n"
"v_exp_f32 v35, v35\n"
"v_exp_f32 v36, v36\n"
"v_exp_f32 v37, v37\n"
"v_exp_f32 v38, v38\n"
"v_exp_f32 v39, v39\n"
"v_exp_f32 v40, v40\n"
"v_exp_f32 v41, v41\n"
"v_exp_f32 v42, v42\n"
"v_exp_f32 v43, v43\n"
"v_exp_f32 v44, v44\n"
"v_exp_f32 v45, v45\n"
"v_exp_f32 v46, v46\n"
"v_exp_f32 v47, v47\n"
"s_setprio 2\n"
"s_waitcnt lgkmcnt(0)\n"
"v_smfmac_f32_32x32x32_bf16 v[48:63], v[80:83], v[16:23], v13\n"
"buffer_load_dwordx4 v[104:107], v14, s[4:7], s13 offen sc1\n"
"v_smfmac_f32_32x32x32_bf16 v[48:63], v[84:87], v[24:31], v13\n"
"s_lshl_b32 s59, s58, 8\n"
"s_add_u32 s60, s57, s59\n"
"v_lshlrev_b32 v2, 3, v0\n"
"v_add_u32 v7, s60, v2\n"
"s_mov_b64 exec, s[18:19]\n"
"ds_read_b128 v[80:83], v12 offset:40960\n"
"ds_read_b128 v[84:87], v12 offset:41600\n"
"s_mov_b64 exec, -1\n"
"v_mfma_f32_32x32x8_bf16 v[16:31], v[70:71], v[68:69], 0\n"
"ds_read_b64 v[70:71], v11 offset:41472\n"
"buffer_load_dwordx4 v[108:111], v14, s[4:7], s13 offen offset:1024 sc1\n"
"s_sub_u32 s13, s13, s24\n"
"v_mfma_f32_32x32x16_bf16 v[16:31], v[72:75], v[64:67], v[16:31]\n"
"ds_read_b128 v[72:75], v10 offset:41984\n"
"v_smfmac_f32_32x32x32_bf16 v[48:63], v[88:91], v[32:39], v13\n"
"buffer_load_dwordx4 v[96:99], v14, s[4:7], s13 offen sc1\n"
"v_smfmac_f32_32x32x32_bf16 v[48:63], v[92:95], v[40:47], v13\n"
"s_mov_b64 exec, s[18:19]\n"
"ds_read_b128 v[88:91], v12 offset:42240\n"
"ds_read_b128 v[92:95], v12 offset:42880\n"
"s_mov_b64 exec, -1\n"
"v_mfma_f32_32x32x8_bf16 v[32:47], v[8:9], v[68:69], 0\n"
"ds_read_b64 v[8:9], v11 offset:41728\n"
"buffer_load_dwordx4 v[100:103], v14, s[4:7], s13 offen offset:1024 sc1\n"
"v_mfma_f32_32x32x16_bf16 v[32:47], v[76:79], v[64:67], v[32:47]\n"
"ds_read_b128 v[76:79], v10 offset:42496\n"
"s_setprio 0\n"
"v_exp_f32 v16, v16\n"
"v_exp_f32 v17, v17\n"
"s_setprio 1\n"
"v_exp_f32 v18, v18\n"
"v_exp_f32 v19, v19\n"
"v_exp_f32 v20, v20\n"
"v_exp_f32 v21, v21\n"
"v_exp_f32 v22, v22\n"
"v_exp_f32 v23, v23\n"
"v_exp_f32 v24, v24\n"
"v_exp_f32 v25, v25\n"
"v_exp_f32 v26, v26\n"
"v_exp_f32 v27, v27\n"
"v_exp_f32 v28, v28\n"
"v_exp_f32 v29, v29\n"
"v_exp_f32 v30, v30\n"
"v_exp_f32 v31, v31\n"
"v_exp_f32 v32, v32\n"
"v_exp_f32 v33, v33\n"
"v_exp_f32 v34, v34\n"
"v_exp_f32 v35, v35\n"
"v_exp_f32 v36, v36\n"
"v_exp_f32 v37, v37\n"
"v_exp_f32 v38, v38\n"
"v_exp_f32 v39, v39\n"
"v_exp_f32 v40, v40\n"
"v_exp_f32 v41, v41\n"
"v_exp_f32 v42, v42\n"
"v_exp_f32 v43, v43\n"
"v_exp_f32 v44, v44\n"
"v_exp_f32 v45, v45\n"
"v_exp_f32 v46, v46\n"
"v_exp_f32 v47, v47\n"
"s_setprio 2\n"
"s_barrier\n"
"s_waitcnt lgkmcnt(0)\n"
"v_smfmac_f32_32x32x32_bf16 v[48:63], v[80:83], v[16:23], v13\n"
"s_mov_b32 m0, s27\n"
"s_nop 0\n"
"buffer_load_dwordx4 v14, s[4:7], s26 offen sc1 lds\n"
"s_add_u32 s26, s26, s12\n"
"s_add_u32 s27, s27, s20\n"
"v_smfmac_f32_32x32x32_bf16 v[48:63], v[84:87], v[24:31], v13\n"
"s_mov_b64 exec, s[18:19]\n"
"ds_read_b128 v[80:83], v12 offset:43520\n"
"ds_read_b128 v[84:87], v12 offset:44160\n"
"s_mov_b64 exec, -1\n"
"v_mfma_f32_32x32x8_bf16 v[16:31], v[70:71], v[68:69], 0\n"
"ds_read_b64 v[70:71], v11 offset:51200\n"
"s_mov_b32 m0, s27\n"
"s_nop 0\n"
"buffer_load_dwordx4 v14, s[4:7], s26 offen sc1 lds\n"
"s_add_u32 s26, s26, s12\n"
"s_add_u32 s27, s27, s20\n"
"v_mfma_f32_32x32x16_bf16 v[16:31], v[72:75], v[64:67], v[16:31]\n"
"ds_read_b128 v[72:75], v10 offset:51200\n"
"v_smfmac_f32_32x32x32_bf16 v[48:63], v[88:91], v[32:39], v13\n"
"s_mov_b32 m0, s27\n"
"s_nop 0\n"
"buffer_load_dwordx4 v14, s[4:7], s26 offen sc1 lds\n"
"s_add_u32 s26, s26, s12\n"
"s_add_u32 s27, s27, s20\n"
"v_smfmac_f32_32x32x32_bf16 v[48:63], v[92:95], v[40:47], v13\n"
"s_mov_b64 exec, s[18:19]\n"
"ds_read_b128 v[88:91], v12 offset:44800\n"
"ds_read_b128 v[92:95], v12 offset:45440\n"
"s_mov_b64 exec, -1\n"
"v_mfma_f32_32x32x8_bf16 v[32:47], v[8:9], v[68:69], 0\n"
"ds_read_b64 v[8:9], v11 offset:51456\n"
"s_mov_b32 m0, s27\n"
"s_nop 0\n"
"buffer_load_dwordx4 v14, s[4:7], s26 offen sc1 lds\n"
"s_add_u32 s26, s26, s12\n"
"s_add_u32 s27, s27, s20\n"
"v_mfma_f32_32x32x16_bf16 v[32:47], v[76:79], v[64:67], v[32:47]\n"
"ds_read_b128 v[76:79], v10 offset:51712\n"
"s_setprio 0\n"
"v_exp_f32 v16, v16\n"
"v_exp_f32 v17, v17\n"
"s_setprio 1\n"
"v_exp_f32 v18, v18\n"
"v_exp_f32 v19, v19\n"
"v_exp_f32 v20, v20\n"
"v_exp_f32 v21, v21\n"
"v_exp_f32 v22, v22\n"
"v_exp_f32 v23, v23\n"
"v_exp_f32 v24, v24\n"
"v_exp_f32 v25, v25\n"
"v_exp_f32 v26, v26\n"
"v_exp_f32 v27, v27\n"
"v_exp_f32 v28, v28\n"
"v_exp_f32 v29, v29\n"
"v_exp_f32 v30, v30\n"
"v_exp_f32 v31, v31\n"
"v_exp_f32 v32, v32\n"
"v_exp_f32 v33, v33\n"
"v_exp_f32 v34, v34\n"
"v_exp_f32 v35, v35\n"
"v_exp_f32 v36, v36\n"
"v_exp_f32 v37, v37\n"
"v_exp_f32 v38, v38\n"
"v_exp_f32 v39, v39\n"
"v_exp_f32 v40, v40\n"
"v_exp_f32 v41, v41\n"
"v_exp_f32 v42, v42\n"
"v_exp_f32 v43, v43\n"
"v_exp_f32 v44, v44\n"
"v_exp_f32 v45, v45\n"
"v_exp_f32 v46, v46\n"
"v_exp_f32 v47, v47\n"
"s_setprio 2\n"
"s_waitcnt lgkmcnt(0)\n"
"v_add_u32 v10, 0xc800, v10\n"
"v_add_u32 v11, 0xc800, v11\n"
"v_add_u32 v12, 0xc800, v12\n"
"v_smfmac_f32_32x32x32_bf16 v[48:63], v[80:83], v[16:23], v13\n"
"s_mov_b32 m0, s27\n"
"s_nop 0\n"
"buffer_load_dwordx4 v14, s[4:7], s26 offen sc1 lds\n"
"v_smfmac_f32_32x32x32_bf16 v[48:63], v[84:87], v[24:31], v13\n"
"s_mov_b64 exec, s[18:19]\n"
"ds_read_b128 v[80:83], v12\n"
"ds_read_b128 v[84:87], v12 offset:640\n"
"s_mov_b64 exec, -1\n"
"v_mfma_f32_32x32x8_bf16 v[16:31], v[70:71], v[68:69], 0\n"
"s_cbranch_execz .Lk_tramp\n"
"s_nop 0\n"
"ds_read_b64 v[70:71], v11 offset:512\n"
"v_mfma_f32_32x32x16_bf16 v[16:31], v[72:75], v[64:67], v[16:31]\n"
"ds_read_b128 v[72:75], v10 offset:1024\n"
"v_smfmac_f32_32x32x32_bf16 v[48:63], v[88:91], v[32:39], v13\n"
"v_smfmac_f32_32x32x32_bf16 v[48:63], v[92:95], v[40:47], v13\n"
"s_mov_b64 exec, s[18:19]\n"
"ds_read_b128 v[88:91], v12 offset:1280\n"
"ds_read_b128 v[92:95], v12 offset:1920\n"
"s_mov_b64 exec, -1\n"
"v_mfma_f32_32x32x8_bf16 v[32:47], v[8:9], v[68:69], 0\n"
"ds_read_b64 v[8:9], v11 offset:768\n"
"v_mfma_f32_32x32x16_bf16 v[32:47], v[76:79], v[64:67], v[32:47]\n"
"ds_read_b128 v[76:79], v10 offset:1536\n"
"s_setprio 0\n"
"v_exp_f32 v16, v16\n"
"v_exp_f32 v17, v17\n"
"s_setprio 1\n"
"v_exp_f32 v18, v18\n"
"v_exp_f32 v19, v19\n"
"v_exp_f32 v20, v20\n"
"v_exp_f32 v21, v21\n"
"v_exp_f32 v22, v22\n"
"v_exp_f32 v23, v23\n"
"v_exp_f32 v24, v24\n"
"v_exp_f32 v25, v25\n"
"v_exp_f32 v26, v26\n"
"v_exp_f32 v27, v27\n"
"v_exp_f32 v28, v28\n"
"v_exp_f32 v29, v29\n"
"v_exp_f32 v30, v30\n"
"v_exp_f32 v31, v31\n"
"v_exp_f32 v32, v32\n"
"v_exp_f32 v33, v33\n"
"v_exp_f32 v34, v34\n"
"v_exp_f32 v35, v35\n"
"v_exp_f32 v36, v36\n"
"v_exp_f32 v37, v37\n"
"v_exp_f32 v38, v38\n"
"v_exp_f32 v39, v39\n"
"v_exp_f32 v40, v40\n"
"v_exp_f32 v41, v41\n"
"v_exp_f32 v42, v42\n"
"v_exp_f32 v43, v43\n"
"v_exp_f32 v44, v44\n"
"v_exp_f32 v45, v45\n"
"v_exp_f32 v46, v46\n"
"v_exp_f32 v47, v47\n"
"s_setprio 2\n"
"s_waitcnt lgkmcnt(0)\n"
"v_smfmac_f32_32x32x32_bf16 v[48:63], v[80:83], v[16:23], v13\n"
"v_smfmac_f32_32x32x32_bf16 v[48:63], v[84:87], v[24:31], v13\n"
"s_mov_b64 exec, s[18:19]\n"
"ds_read_b128 v[80:83], v12 offset:2560\n"
"ds_read_b128 v[84:87], v12 offset:3200\n"
"s_mov_b64 exec, -1\n"
"v_mfma_f32_32x32x8_bf16 v[16:31], v[70:71], v[68:69], 0\n"
"v_mfma_f32_32x32x16_bf16 v[16:31], v[72:75], v[64:67], v[16:31]\n"
"v_smfmac_f32_32x32x32_bf16 v[48:63], v[88:91], v[32:39], v13\n"
"v_smfmac_f32_32x32x32_bf16 v[48:63], v[92:95], v[40:47], v13\n"
"s_mov_b64 exec, s[18:19]\n"
"ds_read_b128 v[88:91], v12 offset:3840\n"
"ds_read_b128 v[92:95], v12 offset:4480\n"
"s_mov_b64 exec, -1\n"
"v_mfma_f32_32x32x8_bf16 v[32:47], v[8:9], v[68:69], 0\n"
"v_mfma_f32_32x32x16_bf16 v[32:47], v[76:79], v[64:67], v[32:47]\n"
"s_setprio 0\n"
"s_nop 2\n"
"v_exp_f32 v16, v16\n"
"v_exp_f32 v17, v17\n"
"s_setprio 1\n"
"v_exp_f32 v18, v18\n"
"v_exp_f32 v19, v19\n"
"v_exp_f32 v20, v20\n"
"v_exp_f32 v21, v21\n"
"v_exp_f32 v22, v22\n"
"v_exp_f32 v23, v23\n"
"v_exp_f32 v24, v24\n"
"v_exp_f32 v25, v25\n"
"v_exp_f32 v26, v26\n"
"v_exp_f32 v27, v27\n"
"v_exp_f32 v28, v28\n"
"v_exp_f32 v29, v29\n"
"v_exp_f32 v30, v30\n"
"v_exp_f32 v31, v31\n"
"v_exp_f32 v32, v32\n"
"v_exp_f32 v33, v33\n"
"v_exp_f32 v34, v34\n"
"v_exp_f32 v35, v35\n"
"v_exp_f32 v36, v36\n"
"v_exp_f32 v37, v37\n"
"v_exp_f32 v38, v38\n"
"v_exp_f32 v39, v39\n"
"v_exp_f32 v40, v40\n"
"v_exp_f32 v41, v41\n"
"v_exp_f32 v42, v42\n"
"v_exp_f32 v43, v43\n"
"v_exp_f32 v44, v44\n"
"v_exp_f32 v45, v45\n"
"v_exp_f32 v46, v46\n"
"v_exp_f32 v47, v47\n"
"s_setprio 2\n"
"s_waitcnt lgkmcnt(0)\n"
"v_smfmac_f32_32x32x32_bf16 v[48:63], v[80:83], v[16:23], v13\n"
"s_or_b32 s38, s34, s2\n"
"s_cmp_lg_u32 s38, 0\n"
"s_cbranch_scc1 .Lk_noe_41\n"
"s_mov_b32 s38, 0\n"
"v_mov_b32 v2, s32\n"
"v_mov_b32 v3, s33\n"
"s_mov_b64 exec, 0xff\n"
"buffer_store_dwordx4 v[2:5], v14, s[4:7], s38 offen sc1\n"
"s_mov_b64 exec, -1\n"
".Lk_noe_41:\n"
"v_smfmac_f32_32x32x32_bf16 v[48:63], v[84:87], v[24:31], v13\n"
"v_smfmac_f32_32x32x32_bf16 v[48:63], v[88:91], v[32:39], v13\n"
"v_smfmac_f32_32x32x32_bf16 v[48:63], v[92:95], v[40:47], v13\n"
"v_mov_b32 v2, s32\n"
"v_mov_b32 v3, s33\n"
"s_mov_b32 s38, 0x30800000\n"
"s_mov_b32 s39, 0x71800000\n"
"s_nop 7\n"
"v_maximum3_f32 v4, |v48|, |v49|, |v50|\n"
"v_maximum3_f32 v5, |v51|, |v52|, |v53|\n"
"v_maximum3_f32 v8, |v54|, |v55|, |v56|\n"
"v_maximum3_f32 v9, |v57|, |v58|, |v59|\n"
"v_cmp_nlt_f32 vcc, s38, v60\n"
"v_maximum3_f32 v4, v4, v5, |v60|\n"
"v_maximum3_f32 v4, v4, v8, v9\n"
"v_cmp_ngt_f32 s[38:39], s39, v4\n"
"s_nop 0\n"
"s_or_b64 vcc, vcc, s[38:39]\n"
"s_cbranch_scc1 .Lk_slow_42\n"
".Lk_resume_43:\n"
"v_rcp_f32 v8, v60\n"
"s_nop 0\n"
"v_mul_f32 v48, v48, v8\n"
"v_mul_f32 v49, v49, v8\n"
"v_mul_f32 v50, v50, v8\n"
"v_mul_f32 v51, v51, v8\n"
"v_mul_f32 v52, v52, v8\n"
"v_mul_f32 v53, v53, v8\n"
"v_mul_f32 v54, v54, v8\n"
"v_mul_f32 v55, v55, v8\n"
"v_mul_f32 v56, v56, v8\n"
"v_mul_f32 v57, v57, v8\n"
"v_mul_f32 v58, v58, v8\n"
"v_mul_f32 v59, v59, v8\n"
"v_cvt_pk_bf16_f32 v16, v48, v49\n"
"v_cvt_pk_bf16_f32 v17, v50, v51\n"
"v_cvt_pk_bf16_f32 v18, v52, v53\n"
"v_cvt_pk_bf16_f32 v19, v54, v55\n"
"v_cvt_pk_bf16_f32 v20, v56, v57\n"
"v_cvt_pk_bf16_f32 v21, v58, v59\n"
"buffer_store_dwordx4 v[16:19], v6, s[4:7], s22 offen sc1\n"
"s_mov_b32 exec_hi, 0\n"
"buffer_store_dwordx2 v[20:21], v7, s[4:7], s22 offen sc1\n"
"s_mov_b64 exec, -1\n"
"s_setprio 0\n"
"; ==== WO\n"
"s_lshl_b32 s56, s62, 6\n"
"s_lshl_b32 s57, s3, 4\n"
"s_add_u32 s58, s56, s57\n"
"s_mul_i32 s59, s58, 0x500\n"
"s_mul_i32 s60, s63, 0xa0\n"
"s_add_u32 s42, s59, s60\n"
"v_and_b32 v4, 15, v0\n"
"v_mul_u32_u24 v5, 0x500, v4\n"
"v_lshrrev_b32 v1, 4, v0\n"
"v_lshlrev_b32 v95, 4, v1\n"
"v_add_u32 v58, v5, v95\n"
"v_and_b32 v4, 15, v0\n"
"v_mul_u32_u24 v5, 0x500, v4\n"
"v_lshrrev_b32 v1, 4, v0\n"
"v_lshlrev_b32 v95, 3, v1\n"
"v_add_u32 v59, v5, v95\n"
"v_add_u32 v6, 0xa000, v14\n"
"s_mul_i32 s56, s62, 0x14000\n"
"s_add_u32 s57, 0xa91000, s56\n"
"s_lshl_b32 s58, s3, 10\n"
"s_add_u32 s46, s57, s58\n"
"s_mul_i32 s56, s62, 0x14000\n"
"s_add_u32 s57, 0xaa1000, s56\n"
"s_lshl_b32 s58, s3, 7\n"
"s_add_u32 s45, s57, s58\n"
"s_mul_i32 s56, s62, 0x14000\n"
"s_add_u32 s57, 0xd11000, s56\n"
"s_mul_i32 s58, s63, 0x2800\n"
"s_add_u32 s59, s57, s58\n"
"s_lshl_b32 s60, s3, 8\n"
"s_add_u32 s47, s59, s60\n"
"s_lshl_b32 s56, s62, 13\n"
"s_add_u32 s57, 0xf91000, s56\n"
"s_lshl_b32 s58, s63, 10\n"
"s_add_u32 s59, s57, s58\n"
"s_lshl_b32 s60, s3, 7\n"
"s_add_u32 s48, s59, s60\n"
"s_lshl_b32 s56, s62, 3\n"
"s_add_u32 s57, s56, s63\n"
"s_lshl_b32 s58, s57, 10\n"
"s_add_u32 s49, 0xc1000, s58\n"
"s_add_u32 s50, s47, 8192\n"
"v_lshrrev_b32 v4, 4, v0\n"
"v_lshlrev_b32 v5, 10, v4\n"
"v_and_b32 v1, 15, v0\n"
"v_lshlrev_b32 v95, 4, v1\n"
"v_add_u32 v7, v5, v95\n"
"s_mul_i32 s56, s3, 0x1400\n"
"s_add_u32 s57, 0x23000, s56\n"
"v_lshlrev_b32 v4, 4, v0\n"
"v_add_u32 v15, s57, v4\n"
"v_lshrrev_b32 v4, 4, v0\n"
"v_lshlrev_b32 v5, 10, v4\n"
"v_and_b32 v1, 15, v0\n"
"v_lshlrev_b32 v95, 3, v1\n"
"v_add_u32 v56, v5, v95\n"
"v_and_b32 v4, 31, v0\n"
"v_lshlrev_b32 v57, 10, v4\n"
"s_lshr_b32 s56, s62, 2\n"
"s_lshl_b32 s57, s56, 5\n"
"s_lshl_b32 s58, s57, 10\n"
"s_add_u32 s44, 0x81000, s58\n"
"s_waitcnt vmcnt(0)\n"
"s_barrier\n"
"s_cmp_lg_u32 s34, 0\n"
"s_cbranch_scc1 .Lk_np_44\n"
"s_mov_b64 exec, 0xff\n"
"buffer_store_dwordx4 v[2:5], v14, s[4:7], s23 offen sc1\n"
"s_mov_b64 exec, -1\n"
".Lk_np_44:\n"
"buffer_load_dwordx4 v[8:11], v58, s[8:11], s42 offen offset:0\n"
"buffer_load_dwordx2 v[12:13], v59, s[8:11], s42 offen offset:128\n"
"ds_read_b128 v[76:79], v14 offset:0\n"
"ds_read_b128 v[80:83], v14 offset:1024\n"
"ds_read_b128 v[84:87], v14 offset:3072\n"
"ds_read_b128 v[88:91], v14 offset:4096\n"
"ds_read_b128 v[92:95], v14 offset:2048\n"
"s_cmp_lg_u32 s34, 0\n"
"s_cbranch_scc1 .Lk_np_45\n"
"buffer_load_dwordx2 v[16:17], v57, s[4:7], s44 offen sc1\n"
"s_waitcnt vmcnt(0)\n"
"v_cmp_ne_u64 vcc, s[32:33], v[16:17]\n"
"s_nop 0\n"
"s_cmp_eq_u64 vcc, 0\n"
"s_cbranch_scc1 .Lk_polld_47\n"
".Lk_poll_46:\n"
"buffer_load_dwordx2 v[16:17], v57, s[4:7], s44 offen sc1\n"
"s_waitcnt vmcnt(0)\n"
"v_cmp_ne_u64 vcc, s[32:33], v[16:17]\n"
"s_nop 0\n"
"s_cmp_eq_u64 vcc, 0\n"
"s_cbranch_scc1 .Lk_polld_47\n"
"s_sub_u32 s37, s37, 1\n"
"s_cmp_gt_i32 s37, 0\n"
"s_cbranch_scc1 .Lk_poll_46\n"
".Lk_polld_47:\n"
".Lk_np_45:\n"
"s_barrier\n"
"buffer_load_dwordx4 v[16:19], v14, s[4:7], s46 offen sc1\n"
"s_add_u32 s46, s46, 0x1000\n"
"buffer_load_dwordx4 v[20:23], v14, s[4:7], s46 offen sc1\n"
"s_add_u32 s46, s46, 0x1000\n"
"buffer_load_dwordx4 v[24:27], v14, s[4:7], s46 offen sc1\n"
"s_add_u32 s46, s46, 0x1000\n"
"buffer_load_dwordx4 v[28:31], v14, s[4:7], s46 offen sc1\n"
"s_add_u32 s46, s46, 0x1000\n"
"buffer_load_dwordx4 v[32:35], v14, s[4:7], s46 offen sc1\n"
"s_add_u32 s46, s46, 0x1000\n"
"buffer_load_dwordx4 v[36:39], v14, s[4:7], s46 offen sc1\n"
"s_add_u32 s46, s46, 0x1000\n"
"buffer_load_dwordx4 v[40:43], v14, s[4:7], s46 offen sc1\n"
"s_add_u32 s46, s46, 0x1000\n"
"buffer_load_dwordx4 v[44:47], v14, s[4:7], s46 offen sc1\n"
"buffer_load_dwordx2 v[48:49], v56, s[4:7], s45 offen offset:0 sc1\n"
"buffer_load_dwordx2 v[50:51], v56, s[4:7], s45 offen offset:512 sc1\n"
"s_add_u32 s45, s45, 0x1000\n"
"buffer_load_dwordx2 v[52:53], v56, s[4:7], s45 offen offset:0 sc1\n"
"buffer_load_dwordx2 v[54:55], v56, s[4:7], s45 offen offset:512 sc1\n"
"s_waitcnt vmcnt(11) lgkmcnt(4)\n"
"v_mfma_f32_16x16x32_bf16 v[56:59], v[76:79], v[16:19], 0\n"
"ds_read_b128 v[76:79], v14 offset:5120\n"
"s_waitcnt lgkmcnt(4)\n"
"v_mfma_f32_16x16x32_bf16 v[60:63], v[80:83], v[16:19], 0\n"
"ds_read_b128 v[80:83], v14 offset:6144\n"
"s_waitcnt lgkmcnt(4)\n"
"v_mfma_f32_16x16x32_bf16 v[64:67], v[84:87], v[16:19], 0\n"
"ds_read_b128 v[84:87], v14 offset:8192\n"
"s_waitcnt lgkmcnt(4)\n"
"v_mfma_f32_16x16x32_bf16 v[68:71], v[88:91], v[16:19], 0\n"
"ds_read_b128 v[88:91], v14 offset:9216\n"
"s_waitcnt lgkmcnt(4)\n"
"v_mfma_f32_16x16x32_bf16 v[72:75], v[92:95], v[16:19], 0\n"
"ds_read_b128 v[92:95], v14 offset:7168\n"
"s_waitcnt vmcnt(10) lgkmcnt(4)\n"
"v_mfma_f32_16x16x32_bf16 v[56:59], v[76:79], v[20:23], v[56:59]\n"
"ds_read_b128 v[76:79], v14 offset:10240\n"
"s_waitcnt lgkmcnt(4)\n"
"v_mfma_f32_16x16x32_bf16 v[60:63], v[80:83], v[20:23], v[60:63]\n"
"ds_read_b128 v[80:83], v14 offset:11264\n"
"s_waitcnt lgkmcnt(4)\n"
"v_mfma_f32_16x16x32_bf16 v[64:67], v[84:87], v[20:23], v[64:67]\n"
"ds_read_b128 v[84:87], v14 offset:13312\n"
"s_waitcnt lgkmcnt(4)\n"
"v_mfma_f32_16x16x32_bf16 v[68:71], v[88:91], v[20:23], v[68:71]\n"
"ds_read_b128 v[88:91], v14 offset:14336\n"
"s_waitcnt lgkmcnt(4)\n"
"v_mfma_f32_16x16x32_bf16 v[72:75], v[92:95], v[20:23], v[72:75]\n"
"ds_read_b128 v[92:95], v14 offset:12288\n"
"s_waitcnt vmcnt(9) lgkmcnt(4)\n"
"v_mfma_f32_16x16x32_bf16 v[56:59], v[76:79], v[24:27], v[56:59]\n"
"ds_read_b128 v[76:79], v14 offset:15360\n"
"s_waitcnt lgkmcnt(4)\n"
"v_mfma_f32_16x16x32_bf16 v[60:63], v[80:83], v[24:27], v[60:63]\n"
"ds_read_b128 v[80:83], v14 offset:16384\n"
"s_waitcnt lgkmcnt(4)\n"
"v_mfma_f32_16x16x32_bf16 v[64:67], v[84:87], v[24:27], v[64:67]\n"
"ds_read_b128 v[84:87], v14 offset:18432\n"
"s_waitcnt lgkmcnt(4)\n"
"v_mfma_f32_16x16x32_bf16 v[68:71], v[88:91], v[24:27], v[68:71]\n"
"ds_read_b128 v[88:91], v14 offset:19456\n"
"s_waitcnt lgkmcnt(4)\n"
"v_mfma_f32_16x16x32_bf16 v[72:75], v[92:95], v[24:27], v[72:75]\n"
"ds_read_b128 v[92:95], v14 offset:17408\n"
"s_waitcnt vmcnt(8) lgkmcnt(4)\n"
"v_mfma_f32_16x16x32_bf16 v[56:59], v[76:79], v[28:31], v[56:59]\n"
"ds_read_b128 v[76:79], v14 offset:20480\n"
"s_waitcnt lgkmcnt(4)\n"
"v_mfma_f32_16x16x32_bf16 v[60:63], v[80:83], v[28:31], v[60:63]\n"
"ds_read_b128 v[80:83], v14 offset:21504\n"
"s_waitcnt lgkmcnt(4)\n"
"v_mfma_f32_16x16x32_bf16 v[64:67], v[84:87], v[28:31], v[64:67]\n"
"ds_read_b128 v[84:87], v14 offset:23552\n"
"s_waitcnt lgkmcnt(4)\n"
"v_mfma_f32_16x16x32_bf16 v[68:71], v[88:91], v[28:31], v[68:71]\n"
"ds_read_b128 v[88:91], v14 offset:24576\n"
"s_waitcnt lgkmcnt(4)\n"
"v_mfma_f32_16x16x32_bf16 v[72:75], v[92:95], v[28:31], v[72:75]\n"
"ds_read_b128 v[92:95], v14 offset:22528\n"
"s_waitcnt vmcnt(7) lgkmcnt(4)\n"
"v_mfma_f32_16x16x32_bf16 v[56:59], v[76:79], v[32:35], v[56:59]\n"
"ds_read_b128 v[76:79], v14 offset:25600\n"
"s_waitcnt lgkmcnt(4)\n"
"v_mfma_f32_16x16x32_bf16 v[60:63], v[80:83], v[32:35], v[60:63]\n"
"ds_read_b128 v[80:83], v14 offset:26624\n"
"s_waitcnt lgkmcnt(4)\n"
"v_mfma_f32_16x16x32_bf16 v[64:67], v[84:87], v[32:35], v[64:67]\n"
"ds_read_b128 v[84:87], v14 offset:28672\n"
"s_waitcnt lgkmcnt(4)\n"
"v_mfma_f32_16x16x32_bf16 v[68:71], v[88:91], v[32:35], v[68:71]\n"
"ds_read_b128 v[88:91], v14 offset:29696\n"
"s_waitcnt lgkmcnt(4)\n"
"v_mfma_f32_16x16x32_bf16 v[72:75], v[92:95], v[32:35], v[72:75]\n"
"ds_read_b128 v[92:95], v14 offset:27648\n"
"s_waitcnt vmcnt(6) lgkmcnt(4)\n"
"v_mfma_f32_16x16x32_bf16 v[56:59], v[76:79], v[36:39], v[56:59]\n"
"ds_read_b128 v[76:79], v14 offset:30720\n"
"s_waitcnt lgkmcnt(4)\n"
"v_mfma_f32_16x16x32_bf16 v[60:63], v[80:83], v[36:39], v[60:63]\n"
"ds_read_b128 v[80:83], v14 offset:31744\n"
"s_waitcnt lgkmcnt(4)\n"
"v_mfma_f32_16x16x32_bf16 v[64:67], v[84:87], v[36:39], v[64:67]\n"
"ds_read_b128 v[84:87], v14 offset:33792\n"
"s_waitcnt lgkmcnt(4)\n"
"v_mfma_f32_16x16x32_bf16 v[68:71], v[88:91], v[36:39], v[68:71]\n"
"ds_read_b128 v[88:91], v14 offset:34816\n"
"s_waitcnt lgkmcnt(4)\n"
"v_mfma_f32_16x16x32_bf16 v[72:75], v[92:95], v[36:39], v[72:75]\n"
"ds_read_b128 v[92:95], v14 offset:32768\n"
"s_waitcnt vmcnt(5) lgkmcnt(4)\n"
"v_mfma_f32_16x16x32_bf16 v[56:59], v[76:79], v[40:43], v[56:59]\n"
"ds_read_b128 v[76:79], v14 offset:35840\n"
"s_waitcnt lgkmcnt(4)\n"
"v_mfma_f32_16x16x32_bf16 v[60:63], v[80:83], v[40:43], v[60:63]\n"
"ds_read_b128 v[80:83], v14 offset:36864\n"
"s_waitcnt lgkmcnt(4)\n"
"v_mfma_f32_16x16x32_bf16 v[64:67], v[84:87], v[40:43], v[64:67]\n"
"ds_read_b128 v[84:87], v14 offset:38912\n"
"s_waitcnt lgkmcnt(4)\n"
"v_mfma_f32_16x16x32_bf16 v[68:71], v[88:91], v[40:43], v[68:71]\n"
"ds_read_b128 v[88:91], v14 offset:39936\n"
"s_waitcnt lgkmcnt(4)\n"
"v_mfma_f32_16x16x32_bf16 v[72:75], v[92:95], v[40:43], v[72:75]\n"
"ds_read_b128 v[92:95], v14 offset:37888\n"
"s_waitcnt vmcnt(4) lgkmcnt(4)\n"
"v_mfma_f32_16x16x32_bf16 v[56:59], v[76:79], v[44:47], v[56:59]\n"
"ds_read_b128 v[76:79], v6 offset:40960\n"
"s_waitcnt lgkmcnt(4)\n"
"v_mfma_f32_16x16x32_bf16 v[60:63], v[80:83], v[44:47], v[60:63]\n"
"ds_read_b128 v[80:83], v6 offset:41984\n"
"s_waitcnt lgkmcnt(4)\n"
"v_mfma_f32_16x16x32_bf16 v[64:67], v[84:87], v[44:47], v[64:67]\n"
"ds_read_b128 v[84:87], v6 offset:44032\n"
"s_waitcnt lgkmcnt(4)\n"
"v_mfma_f32_16x16x32_bf16 v[68:71], v[88:91], v[44:47], v[68:71]\n"
"ds_read_b128 v[88:91], v6 offset:45056\n"
"s_waitcnt lgkmcnt(4)\n"
"v_mfma_f32_16x16x32_bf16 v[72:75], v[92:95], v[44:47], v[72:75]\n"
"ds_read_b128 v[92:95], v6 offset:43008\n"
"s_waitcnt vmcnt(2) lgkmcnt(4)\n"
"v_mfma_f32_16x16x32_bf16 v[56:59], v[76:79], v[48:51], v[56:59]\n"
"ds_read_b128 v[76:79], v6 offset:46080\n"
"s_waitcnt lgkmcnt(4)\n"
"v_mfma_f32_16x16x32_bf16 v[60:63], v[80:83], v[48:51], v[60:63]\n"
"ds_read_b128 v[80:83], v6 offset:47104\n"
"s_waitcnt lgkmcnt(4)\n"
"v_mfma_f32_16x16x32_bf16 v[64:67], v[84:87], v[48:51], v[64:67]\n"
"ds_read_b128 v[84:87], v6 offset:49152\n"
"s_waitcnt lgkmcnt(4)\n"
"v_mfma_f32_16x16x32_bf16 v[68:71], v[88:91], v[48:51], v[68:71]\n"
"ds_read_b128 v[88:91], v6 offset:50176\n"
"s_waitcnt lgkmcnt(4)\n"
"v_mfma_f32_16x16x32_bf16 v[72:75], v[92:95], v[48:51], v[72:75]\n"
"ds_read_b128 v[92:95], v6 offset:48128\n"
"s_waitcnt vmcnt(0) lgkmcnt(4)\n"
"v_mfma_f32_16x16x32_bf16 v[56:59], v[76:79], v[52:55], v[56:59]\n"
"s_waitcnt lgkmcnt(3)\n"
"v_mfma_f32_16x16x32_bf16 v[60:63], v[80:83], v[52:55], v[60:63]\n"
"s_waitcnt lgkmcnt(2)\n"
"v_mfma_f32_16x16x32_bf16 v[64:67], v[84:87], v[52:55], v[64:67]\n"
"s_waitcnt lgkmcnt(1)\n"
"v_mfma_f32_16x16x32_bf16 v[68:71], v[88:91], v[52:55], v[68:71]\n"
"s_waitcnt lgkmcnt(0)\n"
"v_mfma_f32_16x16x32_bf16 v[72:75], v[92:95], v[52:55], v[72:75]\n"
"v_lshlrev_b32 v16, 16, v8\n"
"v_and_b32 v17, 0xffff0000, v8\n"
"v_lshlrev_b32 v18, 16, v9\n"
"v_and_b32 v19, 0xffff0000, v9\n"
"v_lshlrev_b32 v20, 16, v10\n"
"v_and_b32 v21, 0xffff0000, v10\n"
"v_lshlrev_b32 v22, 16, v11\n"
"v_and_b32 v23, 0xffff0000, v11\n"
"v_lshlrev_b32 v24, 16, v12\n"
"v_and_b32 v25, 0xffff0000, v12\n"
"v_lshlrev_b32 v26, 16, v13\n"
"v_and_b32 v27, 0xffff0000, v13\n"
"ds_write_b128 v15, v[64:67] offset:0\n"
"ds_write_b128 v15, v[68:71] offset:1024\n"
"v_add_f32 v16, v16, v56\n"
"v_add_f32 v17, v17, v57\n"
"v_add_f32 v18, v18, v58\n"
"v_add_f32 v19, v19, v59\n"
"v_add_f32 v20, v20, v60\n"
"v_add_f32 v21, v21, v61\n"
"v_add_f32 v22, v22, v62\n"
"v_add_f32 v23, v23, v63\n"
"v_add_f32 v24, v24, v72\n"
"v_add_f32 v25, v25, v73\n"
"v_add_f32 v26, v26, v74\n"
"v_add_f32 v27, v27, v75\n"
"v_lshrrev_b32 v2, 5, v0\n"
"v_lshlrev_b32 v3, 10, v2\n"
"v_and_b32 v4, 15, v0\n"
"v_lshlrev_b32 v5, 4, v4\n"
"v_add_u32 v2, v3, v5\n"
"v_lshrrev_b32 v4, 4, v0\n"
"v_and_b32 v3, 1, v4\n"
"v_lshlrev_b32 v5, 3, v3\n"
"v_add_u32 v45, v2, v5\n"
"v_lshlrev_b32 v46, 3, v0\n"
"v_mov_b32 v2, s32\n"
"v_mov_b32 v3, s33\n"
"s_waitcnt lgkmcnt(0)\n"
"s_barrier\n"
"ds_read_b128 v[32:35], v15 offset:2048\n"
"ds_read_b128 v[36:39], v15 offset:3072\n"
"ds_read_b128 v[40:43], v15 offset:4096\n"
"s_waitcnt lgkmcnt(2)\n"
"v_pk_add_f32 v[16:17], v[16:17], v[32:33]\n"
"v_pk_add_f32 v[18:19], v[18:19], v[34:35]\n"
"s_waitcnt lgkmcnt(1)\n"
"v_pk_add_f32 v[20:21], v[20:21], v[36:37]\n"
"v_pk_add_f32 v[22:23], v[22:23], v[38:39]\n"
"s_waitcnt lgkmcnt(0)\n"
"v_pk_add_f32 v[24:25], v[24:25], v[40:41]\n"
"v_pk_add_f32 v[26:27], v[26:27], v[42:43]\n"
"v_cvt_pk_bf16_f32 v8, v16, v17\n"
"v_cvt_pk_bf16_f32 v9, v18, v19\n"
"v_cvt_pk_bf16_f32 v10, v20, v21\n"
"v_cvt_pk_bf16_f32 v11, v22, v23\n"
"v_cvt_pk_bf16_f32 v12, v24, v25\n"
"v_cvt_pk_bf16_f32 v13, v26, v27\n"
"buffer_store_dwordx4 v[8:11], v7, s[4:7], s47 offen sc1\n"
"buffer_store_dwordx2 v[12:13], v45, s[4:7], s50 offen sc1\n"
"v_pk_mul_f32 v[34:35], v[16:17], v[16:17]\n"
"v_pk_add_f32 v[32:33], v[16:17], v[18:19]\n"
"v_pk_fma_f32 v[34:35], v[18:19], v[18:19], v[34:35]\n"
"v_pk_fma_f32 v[34:35], v[20:21], v[20:21], v[34:35]\n"
"v_pk_add_f32 v[32:33], v[32:33], v[20:21]\n"
"v_pk_fma_f32 v[34:35], v[22:23], v[22:23], v[34:35]\n"
"v_pk_add_f32 v[32:33], v[32:33], v[22:23]\n"
"v_pk_fma_f32 v[34:35], v[24:25], v[24:25], v[34:35]\n"
"v_pk_add_f32 v[32:33], v[32:33], v[24:25]\n"
"v_pk_fma_f32 v[34:35], v[26:27], v[26:27], v[34:35]\n"
"v_pk_add_f32 v[32:33], v[32:33], v[26:27]\n"
"v_add_f32 v32, v32, v33\n"
"v_add_f32 v33, v34, v35\n"
"v_mov_b32 v34, v32\n"
"v_mov_b32 v35, v33\n"
"s_nop 0\n"
"v_permlane16_swap_b32 v32, v34\n"
"v_permlane16_swap_b32 v33, v35\n"
"v_pk_add_f32 v[32:33], v[32:33], v[34:35]\n"
"v_mov_b32 v34, v32\n"
"v_mov_b32 v35, v33\n"
"s_nop 0\n"
"v_permlane32_swap_b32 v32, v34\n"
"v_permlane32_swap_b32 v33, v35\n"
"v_pk_add_f32 v[32:33], v[32:33], v[34:35]\n"
"s_mov_b64 exec, 0xffff\n"
"buffer_store_dwordx2 v[32:33], v46, s[4:7], s48 offen sc1\n"
"s_mov_b64 exec, -1\n"
"v_mov_b32 v6, v7\n"
"v_lshrrev_b32 v4, 1, v0\n"
"v_and_b32 v5, 3, v4\n"
"v_lshlrev_b32 v94, 10, v5\n"
"v_and_b32 v95, 1, v0\n"
"v_lshlrev_b32 v4, 7, v95\n"
"v_add_u32 v5, v94, v4\n"
"v_lshrrev_b32 v95, 3, v0\n"
"v_lshlrev_b32 v94, 13, v95\n"
"v_add_u32 v40, v5, v94\n"
"s_mov_b32 s42, s47\n"
"s_mov_b32 s39, s48\n"
"v_mov_b32 v16, 0x3f803f80\n"
"v_mov_b32 v17, 0x3f803f80\n"
"v_mov_b32 v18, 0x3f803f80\n"
"v_mov_b32 v19, 0x3f803f80\n"
"s_lshl_b32 s47, s34, 10\n"
"s_mul_i32 s56, s62, 0x14000\n"
"s_add_u32 s57, 0xd11000, s56\n"
"s_add_u32 s46, s57, s47\n"
"v_add_u32 v7, 0x10000, v6\n"
"s_mul_i32 s56, s62, 0x36000\n"
"s_add_u32 s50, 0xfd1000, s56\n"
"s_lshl_b32 s56, s34, 3\n"
"s_add_u32 s57, s56, s63\n"
"s_lshl_b32 s58, s57, 12\n"
"s_add_u32 s48, s50, s58\n"
"s_lshl_b32 s56, s3, 12\n"
"s_add_u32 s50, s50, s56\n"
"s_mul_i32 s51, s3, 0xa000\n"
"v_and_b32 v4, 15, v0\n"
"v_lshlrev_b32 v5, 2, v4\n"
"v_add_u32 v92, 0x14000, v5\n"
"s_lshl_b32 s56, s3, 6\n"
"v_add_u32 v93, s56, v92\n"
"v_lshlrev_b32 v88, 3, v0\n"
"s_lshl_b32 s56, s62, 3\n"
"s_lshl_b32 s57, s56, 10\n"
"s_add_u32 s44, 0xc1000, s57\n"
"s_lshl_b32 s56, s62, 6\n"
"s_lshl_b32 s57, s34, 3\n"
"s_add_u32 s58, s56, s57\n"
"s_add_u32 s59, s58, s63\n"
"s_lshl_b32 s60, s59, 10\n"
"s_add_u32 s45, 0x101000, s60\n"
"s_lshl_b32 s56, s62, 6\n"
"s_add_u32 s57, s56, s3\n"
"s_lshl_b32 s58, s57, 10\n"
"s_add_u32 s43, 0x101000, s58\n"
"s_waitcnt vmcnt(0)\n"
"s_barrier\n"
"s_cmp_eq_u32 s3, 3\n"
"s_cbranch_scc1 .Lk_sb3_48\n"
"s_mul_i32 s56, s3, 0x1800\n"
"s_add_u32 s12, 0x14800, s56\n"
"s_add_u32 m0, s12, 0\n"
"s_add_u32 s38, s25, 125952\n"
"buffer_load_dwordx4 v14, s[4:7], s38 offen sc1 lds\n"
"s_add_u32 m0, s12, 2048\n"
"s_add_u32 s38, s25, 146432\n"
"buffer_load_dwordx4 v14, s[4:7], s38 offen sc1 lds\n"
"s_add_u32 m0, s12, 4096\n"
"s_add_u32 s38, s25, 166912\n"
"buffer_load_dwordx4 v14, s[4:7], s38 offen sc1 lds\n"
"s_add_u32 m0, s12, 59392\n"
"s_add_u32 s38, s25, 187392\n"
"buffer_load_dwordx4 v14, s[4:7], s38 offen sc1 lds\n"
"s_add_u32 m0, s12, 61440\n"
"s_add_u32 s38, s25, 207872\n"
"buffer_load_dwordx4 v14, s[4:7], s38 offen sc1 lds\n"
"s_add_u32 m0, s12, 63488\n"
"s_add_u32 s38, s25, 228352\n"
"buffer_load_dwordx4 v14, s[4:7], s38 offen sc1 lds\n"
"s_branch .Lk_sbe_49\n"
".Lk_sb3_48:\n"
".Lk_sbe_49:\n"
"v_mfma_f32_16x16x32_bf16 v[80:83], v[96:99], v[16:19], 0\n"
"v_mfma_f32_16x16x32_bf16 v[84:87], v[100:103], v[16:19], 0\n"
"v_mfma_f32_16x16x32_bf16 v[80:83], v[104:107], v[16:19], v[80:83]\n"
"v_mfma_f32_16x16x32_bf16 v[84:87], v[108:111], v[16:19], v[84:87]\n"
"v_mfma_f32_16x16x32_bf16 v[80:83], v[112:115], v[16:19], v[80:83]\n"
"v_mfma_f32_16x16x32_bf16 v[84:87], v[116:119], v[16:19], v[84:87]\n"
"v_mfma_f32_16x16x32_bf16 v[80:83], v[120:123], v[16:19], v[80:83]\n"
"v_mfma_f32_16x16x32_bf16 v[84:87], v[124:127], v[16:19], v[84:87]\n"
"v_mfma_f32_16x16x32_bf16 v[80:83], v[128:131], v[16:19], v[80:83]\n"
"v_mfma_f32_16x16x32_bf16 v[84:87], v[132:135], v[16:19], v[84:87]\n"
"v_mfma_f32_16x16x32_bf16 v[80:83], v[136:139], v[16:19], v[80:83]\n"
"v_mfma_f32_16x16x32_bf16 v[84:87], v[140:143], v[16:19], v[84:87]\n"
"v_mfma_f32_16x16x32_bf16 v[80:83], v[144:147], v[16:19], v[80:83]\n"
"v_mfma_f32_16x16x32_bf16 v[84:87], v[148:151], v[16:19], v[84:87]\n"
"v_mfma_f32_16x16x32_bf16 v[80:83], v[152:155], v[16:19], v[80:83]\n"
"v_mfma_f32_16x16x32_bf16 v[84:87], v[156:159], v[16:19], v[84:87]\n"
"v_mfma_f32_16x16x32_bf16 v[80:83], v[160:163], v[16:19], v[80:83]\n"
"v_mfma_f32_16x16x32_bf16 v[84:87], v[164:167], v[16:19], v[84:87]\n"
"v_mfma_f32_16x16x32_bf16 v[80:83], v[168:171], v[16:19], v[80:83]\n"
"v_mfma_f32_16x16x32_bf16 v[84:87], v[172:175], v[16:19], v[84:87]\n"
"v_mfma_f32_16x16x32_bf16 v[80:83], v[176:179], v[16:19], v[80:83]\n"
"v_mfma_f32_16x16x32_bf16 v[84:87], v[180:183], v[16:19], v[84:87]\n"
"v_mfma_f32_16x16x32_bf16 v[80:83], v[184:187], v[16:19], v[80:83]\n"
"v_mfma_f32_16x16x32_bf16 v[84:87], v[188:191], v[16:19], v[84:87]\n"
"v_mfma_f32_16x16x32_bf16 v[80:83], v[192:195], v[16:19], v[80:83]\n"
"v_mfma_f32_16x16x32_bf16 v[84:87], v[196:199], v[16:19], v[84:87]\n"
"v_mfma_f32_16x16x32_bf16 v[80:83], v[200:203], v[16:19], v[80:83]\n"
"v_mfma_f32_16x16x32_bf16 v[84:87], v[204:207], v[16:19], v[84:87]\n"
"v_mfma_f32_16x16x32_bf16 v[80:83], v[208:211], v[16:19], v[80:83]\n"
"v_mfma_f32_16x16x32_bf16 v[84:87], v[212:215], v[16:19], v[84:87]\n"
"v_mfma_f32_16x16x32_bf16 v[80:83], v[216:219], v[16:19], v[80:83]\n"
"v_mfma_f32_16x16x32_bf16 v[84:87], v[220:223], v[16:19], v[84:87]\n"
"v_mfma_f32_16x16x32_bf16 v[80:83], v[224:227], v[16:19], v[80:83]\n"
"v_mfma_f32_16x16x32_bf16 v[84:87], v[228:231], v[16:19], v[84:87]\n"
"v_mfma_f32_16x16x32_bf16 v[80:83], v[232:235], v[16:19], v[80:83]\n"
"v_mfma_f32_16x16x32_bf16 v[84:87], v[236:239], v[16:19], v[84:87]\n"
"v_mfma_f32_16x16x32_bf16 v[80:83], v[240:243], v[16:19], v[80:83]\n"
"v_mfma_f32_16x16x32_bf16 v[84:87], v[244:247], v[16:19], v[84:87]\n"
"v_mfma_f32_16x16x32_bf16 v[80:83], v[248:251], v[16:19], v[80:83]\n"
"v_mfma_f32_16x16x32_bf16 v[84:87], v[252:255], v[16:19], v[84:87]\n"
"s_barrier\n"
"s_mov_b32 m0, s47\n"
"s_nop 0\n"
"buffer_load_dwordx4 v14, s[4:7], s46 offen sc1 lds\n"
"s_add_u32 s46, s46, 0x2000\n"
"s_add_u32 s47, s47, 0x2000\n"
"s_mov_b32 m0, s47\n"
"s_nop 0\n"
"buffer_load_dwordx4 v14, s[4:7], s46 offen sc1 lds\n"
"s_add_u32 s46, s46, 0x2000\n"
"s_add_u32 s47, s47, 0x2000\n"
"s_mov_b32 m0, s47\n"
"s_nop 0\n"
"buffer_load_dwordx4 v14, s[4:7], s46 offen sc1 lds\n"
"s_add_u32 s46, s46, 0x2000\n"
"s_add_u32 s47, s47, 0x2000\n"
"s_mov_b32 m0, s47\n"
"s_nop 0\n"
"buffer_load_dwordx4 v14, s[4:7], s46 offen sc1 lds\n"
"s_add_u32 s46, s46, 0x2000\n"
"s_add_u32 s47, s47, 0x2000\n"
"s_mov_b32 m0, s47\n"
"s_nop 0\n"
"buffer_load_dwordx4 v14, s[4:7], s46 offen sc1 lds\n"
"s_add_u32 s46, s46, 0x2000\n"
"s_add_u32 s47, s47, 0x2000\n"
"s_setprio 1\n"
"s_waitcnt vmcnt(3)\n"
"s_barrier\n"
"ds_read_b128 v[16:19], v6 offset:0\n"
"ds_read_b128 v[20:23], v6 offset:256\n"
"ds_read_b128 v[24:27], v6 offset:512\n"
"ds_read_b128 v[28:31], v6 offset:768\n"
"s_waitcnt lgkmcnt(3)\n"
"v_mfma_f32_16x16x32_bf16 v[48:51], v[96:99], v[16:19], 0\n"
"ds_read_b128 v[32:35], v6 offset:4096\n"
"v_mfma_f32_16x16x32_bf16 v[52:55], v[100:103], v[16:19], 0\n"
"ds_read_b128 v[36:39], v6 offset:4352\n"
"s_waitcnt lgkmcnt(4)\n"
"v_mfma_f32_16x16x32_bf16 v[60:63], v[100:103], v[20:23], 0\n"
"ds_read_b128 v[40:43], v6 offset:4608\n"
"v_mfma_f32_16x16x32_bf16 v[56:59], v[96:99], v[20:23], 0\n"
"ds_read_b128 v[44:47], v6 offset:4864\n"
"s_waitcnt lgkmcnt(5)\n"
"v_mfma_f32_16x16x32_bf16 v[64:67], v[96:99], v[24:27], 0\n"
"s_mov_b32 m0, s47\n"
"s_nop 0\n"
"buffer_load_dwordx4 v14, s[4:7], s46 offen sc1 lds\n"
"s_add_u32 s46, s46, 0x2000\n"
"s_add_u32 s47, s47, 0x2000\n"
"v_mfma_f32_16x16x32_bf16 v[68:71], v[100:103], v[24:27], 0\n"
"s_waitcnt lgkmcnt(4)\n"
"v_mfma_f32_16x16x32_bf16 v[76:79], v[100:103], v[28:31], 0\n"
"v_mfma_f32_16x16x32_bf16 v[72:75], v[96:99], v[28:31], 0\n"
"s_mov_b32 m0, s47\n"
"s_nop 0\n"
"buffer_load_dwordx4 v14, s[4:7], s46 offen sc1 lds\n"
"s_add_u32 s46, s46, 0x2000\n"
"s_add_u32 s47, s47, 0x2000\n"
"s_waitcnt lgkmcnt(0)\n"
"v_mfma_f32_16x16x32_bf16 v[48:51], v[104:107], v[32:35], v[48:51]\n"
"ds_read_b128 v[16:19], v6 offset:8192\n"
"s_nop 0\n"
"s_cbranch_execz .Lk_tramp\n"
"s_nop 0\n"
"v_mfma_f32_16x16x32_bf16 v[52:55], v[108:111], v[32:35], v[52:55]\n"
"ds_read_b128 v[20:23], v6 offset:8448\n"
"v_mfma_f32_16x16x32_bf16 v[60:63], v[108:111], v[36:39], v[60:63]\n"
"ds_read_b128 v[24:27], v6 offset:8704\n"
"v_mfma_f32_16x16x32_bf16 v[56:59], v[104:107], v[36:39], v[56:59]\n"
"ds_read_b128 v[28:31], v6 offset:8960\n"
"v_mfma_f32_16x16x32_bf16 v[64:67], v[104:107], v[40:43], v[64:67]\n"
"s_mov_b32 m0, s47\n"
"s_nop 0\n"
"buffer_load_dwordx4 v14, s[4:7], s46 offen sc1 lds\n"
"s_add_u32 s46, s46, 0x2000\n"
"s_add_u32 s47, s47, 0x2000\n"
"v_mfma_f32_16x16x32_bf16 v[68:71], v[108:111], v[40:43], v[68:71]\n"
"v_mfma_f32_16x16x32_bf16 v[76:79], v[108:111], v[44:47], v[76:79]\n"
"v_mfma_f32_16x16x32_bf16 v[72:75], v[104:107], v[44:47], v[72:75]\n"
"s_mov_b32 m0, s47\n"
"s_nop 0\n"
"buffer_load_dwordx4 v14, s[4:7], s46 offen sc1 lds\n"
"s_add_u32 s46, s46, 0x2000\n"
"s_add_u32 s47, s47, 0x2000\n"
"s_waitcnt lgkmcnt(0)\n"
"v_mfma_f32_16x16x32_bf16 v[48:51], v[112:115], v[16:19], v[48:51]\n"
"ds_read_b128 v[32:35], v6 offset:12288\n"
"v_mfma_f32_16x16x32_bf16 v[52:55], v[116:119], v[16:19], v[52:55]\n"
"ds_read_b128 v[36:39], v6 offset:12544\n"
"v_mfma_f32_16x16x32_bf16 v[60:63], v[116:119], v[20:23], v[60:63]\n"
"ds_read_b128 v[40:43], v6 offset:12800\n"
"v_mfma_f32_16x16x32_bf16 v[56:59], v[112:115], v[20:23], v[56:59]\n"
"ds_read_b128 v[44:47], v6 offset:13056\n"
"v_mfma_f32_16x16x32_bf16 v[64:67], v[112:115], v[24:27], v[64:67]\n"
"s_mov_b32 m0, s47\n"
"s_nop 0\n"
"buffer_load_dwordx4 v14, s[4:7], s46 offen sc1 lds\n"
"v_mfma_f32_16x16x32_bf16 v[68:71], v[116:119], v[24:27], v[68:71]\n"
"v_mfma_f32_16x16x32_bf16 v[76:79], v[116:119], v[28:31], v[76:79]\n"
"v_mfma_f32_16x16x32_bf16 v[72:75], v[112:115], v[28:31], v[72:75]\n"
"s_waitcnt vmcnt(5)\n"
"s_barrier\n"
"s_waitcnt lgkmcnt(0)\n"
"v_mfma_f32_16x16x32_bf16 v[48:51], v[120:123], v[32:35], v[48:51]\n"
"ds_read_b128 v[16:19], v6 offset:16384\n"
"v_mfma_f32_16x16x32_bf16 v[52:55], v[124:127], v[32:35], v[52:55]\n"
"ds_read_b128 v[20:23], v6 offset:16640\n"
"v_mfma_f32_16x16x32_bf16 v[60:63], v[124:127], v[36:39], v[60:63]\n"
"ds_read_b128 v[24:27], v6 offset:16896\n"
"v_mfma_f32_16x16x32_bf16 v[56:59], v[120:123], v[36:39], v[56:59]\n"
"ds_read_b128 v[28:31], v6 offset:17152\n"
"v_mfma_f32_16x16x32_bf16 v[64:67], v[120:123], v[40:43], v[64:67]\n"
"s_add_u32 s13, s25, 0\n"
"buffer_load_dwordx4 v[96:99], v14, s[4:7], s13 offen sc1\n"
"v_mfma_f32_16x16x32_bf16 v[68:71], v[124:127], v[40:43], v[68:71]\n"
"v_mfma_f32_16x16x32_bf16 v[76:79], v[124:127], v[44:47], v[76:79]\n"
"v_mfma_f32_16x16x32_bf16 v[72:75], v[120:123], v[44:47], v[72:75]\n"
"buffer_load_dwordx4 v[100:103], v14, s[4:7], s13 offen offset:1024 sc1\n"
"s_waitcnt lgkmcnt(0)\n"
"v_mfma_f32_16x16x32_bf16 v[48:51], v[128:131], v[16:19], v[48:51]\n"
"ds_read_b128 v[32:35], v6 offset:20480\n"
"v_mfma_f32_16x16x32_bf16 v[52:55], v[132:135], v[16:19], v[52:55]\n"
"ds_read_b128 v[36:39], v6 offset:20736\n"
"v_mfma_f32_16x16x32_bf16 v[60:63], v[132:135], v[20:23], v[60:63]\n"
"ds_read_b128 v[40:43], v6 offset:20992\n"
"v_mfma_f32_16x16x32_bf16 v[56:59], v[128:131], v[20:23], v[56:59]\n"
"ds_read_b128 v[44:47], v6 offset:21248\n"
"v_mfma_f32_16x16x32_bf16 v[64:67], v[128:131], v[24:27], v[64:67]\n"
"buffer_load_dwordx4 v[104:107], v14, s[4:7], s13 offen offset:2048 sc1\n"
"v_mfma_f32_16x16x32_bf16 v[68:71], v[132:135], v[24:27], v[68:71]\n"
"v_mfma_f32_16x16x32_bf16 v[76:79], v[132:135], v[28:31], v[76:79]\n"
"v_mfma_f32_16x16x32_bf16 v[72:75], v[128:131], v[28:31], v[72:75]\n"
"s_add_u32 s13, s25, 20480\n"
"buffer_load_dwordx4 v[108:111], v14, s[4:7], s13 offen sc1\n"
"s_waitcnt lgkmcnt(0)\n"
"v_mfma_f32_16x16x32_bf16 v[48:51], v[136:139], v[32:35], v[48:51]\n"
"ds_read_b128 v[16:19], v6 offset:24576\n"
"v_mfma_f32_16x16x32_bf16 v[52:55], v[140:143], v[32:35], v[52:55]\n"
"ds_read_b128 v[20:23], v6 offset:24832\n"
"v_mfma_f32_16x16x32_bf16 v[60:63], v[140:143], v[36:39], v[60:63]\n"
"ds_read_b128 v[24:27], v6 offset:25088\n"
"v_mfma_f32_16x16x32_bf16 v[56:59], v[136:139], v[36:39], v[56:59]\n"
"ds_read_b128 v[28:31], v6 offset:25344\n"
"v_mfma_f32_16x16x32_bf16 v[64:67], v[136:139], v[40:43], v[64:67]\n"
"buffer_load_dwordx4 v[112:115], v14, s[4:7], s13 offen offset:1024 sc1\n"
"v_mfma_f32_16x16x32_bf16 v[68:71], v[140:143], v[40:43], v[68:71]\n"
"v_mfma_f32_16x16x32_bf16 v[76:79], v[140:143], v[44:47], v[76:79]\n"
"v_mfma_f32_16x16x32_bf16 v[72:75], v[136:139], v[44:47], v[72:75]\n"
"buffer_load_dwordx4 v[116:119], v14, s[4:7], s13 offen offset:2048 sc1\n"
"s_waitcnt lgkmcnt(0)\n"
"v_mfma_f32_16x16x32_bf16 v[48:51], v[144:147], v[16:19], v[48:51]\n"
"ds_read_b128 v[32:35], v6 offset:28672\n"
"v_mfma_f32_16x16x32_bf16 v[52:55], v[148:151], v[16:19], v[52:55]\n"
"ds_read_b128 v[36:39], v6 offset:28928\n"
"v_mfma_f32_16x16x32_bf16 v[60:63], v[148:151], v[20:23], v[60:63]\n"
"ds_read_b128 v[40:43], v6 offset:29184\n"
"v_mfma_f32_16x16x32_bf16 v[56:59], v[144:147], v[20:23], v[56:59]\n"
"ds_read_b128 v[44:47], v6 offset:29440\n"
"v_mfma_f32_16x16x32_bf16 v[64:67], v[144:147], v[24:27], v[64:67]\n"
"s_add_u32 s13, s25, 40960\n"
"buffer_load_dwordx4 v[120:123], v14, s[4:7], s13 offen sc1\n"
"v_mfma_f32_16x16x32_bf16 v[68:71], v[148:151], v[24:27], v[68:71]\n"
"v_mfma_f32_16x16x32_bf16 v[76:79], v[148:151], v[28:31], v[76:79]\n"
"v_mfma_f32_16x16x32_bf16 v[72:75], v[144:147], v[28:31], v[72:75]\n"
"buffer_load_dwordx4 v[124:127], v14, s[4:7], s13 offen offset:1024 sc1\n"
"s_waitcnt lgkmcnt(0)\n"
"v_mfma_f32_16x16x32_bf16 v[48:51], v[152:155], v[32:35], v[48:51]\n"
"ds_read_b128 v[16:19], v6 offset:32768\n"
"v_mfma_f32_16x16x32_bf16 v[52:55], v[156:159], v[32:35], v[52:55]\n"
"ds_read_b128 v[20:23], v6 offset:33024\n"
"v_mfma_f32_16x16x32_bf16 v[60:63], v[156:159], v[36:39], v[60:63]\n"
"ds_read_b128 v[24:27], v6 offset:33280\n"
"v_mfma_f32_16x16x32_bf16 v[56:59], v[152:155], v[36:39], v[56:59]\n"
"ds_read_b128 v[28:31], v6 offset:33536\n"
"v_mfma_f32_16x16x32_bf16 v[64:67], v[152:155], v[40:43], v[64:67]\n"
"buffer_load_dwordx4 v[128:131], v14, s[4:7], s13 offen offset:2048 sc1\n"
"v_mfma_f32_16x16x32_bf16 v[68:71], v[156:159], v[40:43], v[68:71]\n"
"v_mfma_f32_16x16x32_bf16 v[76:79], v[156:159], v[44:47], v[76:79]\n"
"v_mfma_f32_16x16x32_bf16 v[72:75], v[152:155], v[44:47], v[72:75]\n"
"s_add_u32 s13, s25, 61440\n"
"buffer_load_dwordx4 v[132:135], v14, s[4:7], s13 offen sc1\n"
"s_waitcnt lgkmcnt(0)\n"
"v_mfma_f32_16x16x32_bf16 v[48:51], v[160:163], v[16:19], v[48:51]\n"
"ds_read_b128 v[32:35], v6 offset:36864\n"
"v_mfma_f32_16x16x32_bf16 v[52:55], v[164:167], v[16:19], v[52:55]\n"
"ds_read_b128 v[36:39], v6 offset:37120\n"
"v_mfma_f32_16x16x32_bf16 v[60:63], v[164:167], v[20:23], v[60:63]\n"
"ds_read_b128 v[40:43], v6 offset:37376\n"
"v_mfma_f32_16x16x32_bf16 v[56:59], v[160:163], v[20:23], v[56:59]\n"
"ds_read_b128 v[44:47], v6 offset:37632\n"
"v_mfma_f32_16x16x32_bf16 v[64:67], v[160:163], v[24:27], v[64:67]\n"
"buffer_load_dwordx4 v[136:139], v14, s[4:7], s13 offen offset:1024 sc1\n"
"v_mfma_f32_16x16x32_bf16 v[68:71], v[164:167], v[24:27], v[68:71]\n"
"v_mfma_f32_16x16x32_bf16 v[76:79], v[164:167], v[28:31], v[76:79]\n"
"v_mfma_f32_16x16x32_bf16 v[72:75], v[160:163], v[28:31], v[72:75]\n"
"buffer_load_dwordx4 v[140:143], v14, s[4:7], s13 offen offset:2048 sc1\n"
"s_waitcnt vmcnt(12)\n"
"s_barrier\n"
"ds_read_b32 v88, v92 offset:0\n"
"ds_read_b32 v89, v92 offset:64\n"
"ds_read_b32 v90, v92 offset:128\n"
"ds_read_b32 v91, v92 offset:192\n"
"ds_read_b32 v15, v93 offset:256\n"
"s_waitcnt lgkmcnt(5)\n"
"v_mfma_f32_16x16x32_bf16 v[48:51], v[168:171], v[32:35], v[48:51]\n"
"ds_read_b128 v[16:19], v6 offset:40960\n"
"v_mfma_f32_16x16x32_bf16 v[52:55], v[172:175], v[32:35], v[52:55]\n"
"ds_read_b128 v[20:23], v6 offset:41216\n"
"v_mfma_f32_16x16x32_bf16 v[60:63], v[172:175], v[36:39], v[60:63]\n"
"ds_read_b128 v[24:27], v6 offset:41472\n"
"v_mfma_f32_16x16x32_bf16 v[56:59], v[168:171], v[36:39], v[56:59]\n"
"ds_read_b128 v[28:31], v6 offset:41728\n"
"v_mfma_f32_16x16x32_bf16 v[64:67], v[168:171], v[40:43], v[64:67]\n"
"s_add_u32 s13, s25, 81920\n"
"buffer_load_dwordx4 v[144:147], v14, s[4:7], s13 offen sc1\n"
"v_mfma_f32_16x16x32_bf16 v[68:71], v[172:175], v[40:43], v[68:71]\n"
"v_mfma_f32_16x16x32_bf16 v[76:79], v[172:175], v[44:47], v[76:79]\n"
"v_mfma_f32_16x16x32_bf16 v[72:75], v[168:171], v[44:47], v[72:75]\n"
"buffer_load_dwordx4 v[148:151], v14, s[4:7], s13 offen offset:1024 sc1\n"
"s_waitcnt lgkmcnt(0)\n"
"v_mfma_f32_16x16x32_bf16 v[48:51], v[176:179], v[16:19], v[48:51]\n"
"ds_read_b128 v[32:35], v6 offset:45056\n"
"v_mfma_f32_16x16x32_bf16 v[52:55], v[180:183], v[16:19], v[52:55]\n"
"ds_read_b128 v[36:39], v6 offset:45312\n"
"v_mfma_f32_16x16x32_bf16 v[60:63], v[180:183], v[20:23], v[60:63]\n"
"ds_read_b128 v[40:43], v6 offset:45568\n"
"v_mfma_f32_16x16x32_bf16 v[56:59], v[176:179], v[20:23], v[56:59]\n"
"ds_read_b128 v[44:47], v6 offset:45824\n"
"v_mfma_f32_16x16x32_bf16 v[64:67], v[176:179], v[24:27], v[64:67]\n"
"buffer_load_dwordx4 v[152:155], v14, s[4:7], s13 offen offset:2048 sc1\n"
"v_mfma_f32_16x16x32_bf16 v[68:71], v[180:183], v[24:27], v[68:71]\n"
"v_mfma_f32_16x16x32_bf16 v[76:79], v[180:183], v[28:31], v[76:79]\n"
"v_mfma_f32_16x16x32_bf16 v[72:75], v[176:179], v[28:31], v[72:75]\n"
"s_add_u32 s13, s25, 102400\n"
"buffer_load_dwordx4 v[156:159], v14, s[4:7], s13 offen sc1\n"
"s_waitcnt lgkmcnt(0)\n"
"v_mfma_f32_16x16x32_bf16 v[48:51], v[184:187], v[32:35], v[48:51]\n"
"ds_read_b128 v[16:19], v6 offset:49152\n"
"v_mfma_f32_16x16x32_bf16 v[52:55], v[188:191], v[32:35], v[52:55]\n"
"ds_read_b128 v[20:23], v6 offset:49408\n"
"v_mfma_f32_16x16x32_bf16 v[60:63], v[188:191], v[36:39], v[60:63]\n"
"ds_read_b128 v[24:27], v6 offset:49664\n"
"v_mfma_f32_16x16x32_bf16 v[56:59], v[184:187], v[36:39], v[56:59]\n"
"ds_read_b128 v[28:31], v6 offset:49920\n"
"v_mfma_f32_16x16x32_bf16 v[64:67], v[184:187], v[40:43], v[64:67]\n"
"buffer_load_dwordx4 v[160:163], v14, s[4:7], s13 offen offset:1024 sc1\n"
"v_mfma_f32_16x16x32_bf16 v[68:71], v[188:191], v[40:43], v[68:71]\n"
"v_mfma_f32_16x16x32_bf16 v[76:79], v[188:191], v[44:47], v[76:79]\n"
"v_mfma_f32_16x16x32_bf16 v[72:75], v[184:187], v[44:47], v[72:75]\n"
"buffer_load_dwordx4 v[164:167], v14, s[4:7], s13 offen offset:2048 sc1\n"
"s_waitcnt lgkmcnt(0)\n"
"v_mfma_f32_16x16x32_bf16 v[48:51], v[192:195], v[16:19], v[48:51]\n"
"ds_read_b128 v[32:35], v6 offset:53248\n"
"v_mfma_f32_16x16x32_bf16 v[52:55], v[196:199], v[16:19], v[52:55]\n"
"ds_read_b128 v[36:39], v6 offset:53504\n"
"v_mfma_f32_16x16x32_bf16 v[60:63], v[196:199], v[20:23], v[60:63]\n"
"ds_read_b128 v[40:43], v6 offset:53760\n"
"v_mfma_f32_16x16x32_bf16 v[56:59], v[192:195], v[20:23], v[56:59]\n"
"ds_read_b128 v[44:47], v6 offset:54016\n"
"v_mfma_f32_16x16x32_bf16 v[64:67], v[192:195], v[24:27], v[64:67]\n"
"s_add_u32 s13, s25, 122880\n"
"buffer_load_dwordx4 v[168:171], v14, s[4:7], s13 offen sc1\n"
"v_mfma_f32_16x16x32_bf16 v[68:71], v[196:199], v[24:27], v[68:71]\n"
"v_mfma_f32_16x16x32_bf16 v[76:79], v[196:199], v[28:31], v[76:79]\n"
"v_mfma_f32_16x16x32_bf16 v[72:75], v[192:195], v[28:31], v[72:75]\n"
"buffer_load_dwordx4 v[172:175], v14, s[4:7], s13 offen offset:1024 sc1\n"
"s_waitcnt lgkmcnt(0)\n"
"v_mfma_f32_16x16x32_bf16 v[48:51], v[200:203], v[32:35], v[48:51]\n"
"ds_read_b128 v[16:19], v6 offset:57344\n"
"v_mfma_f32_16x16x32_bf16 v[52:55], v[204:207], v[32:35], v[52:55]\n"
"ds_read_b128 v[20:23], v6 offset:57600\n"
"v_mfma_f32_16x16x32_bf16 v[60:63], v[204:207], v[36:39], v[60:63]\n"
"ds_read_b128 v[24:27], v6 offset:57856\n"
"v_mfma_f32_16x16x32_bf16 v[56:59], v[200:203], v[36:39], v[56:59]\n"
"ds_read_b128 v[28:31], v6 offset:58112\n"
"v_mfma_f32_16x16x32_bf16 v[64:67], v[200:203], v[40:43], v[64:67]\n"
"buffer_load_dwordx4 v[176:179], v14, s[4:7], s13 offen offset:2048 sc1\n"
"v_mfma_f32_16x16x32_bf16 v[68:71], v[204:207], v[40:43], v[68:71]\n"
"v_mfma_f32_16x16x32_bf16 v[76:79], v[204:207], v[44:47], v[76:79]\n"
"v_mfma_f32_16x16x32_bf16 v[72:75], v[200:203], v[44:47], v[72:75]\n"
"s_add_u32 s13, s25, 143360\n"
"buffer_load_dwordx4 v[180:183], v14, s[4:7], s13 offen sc1\n"
"s_waitcnt lgkmcnt(0)\n"
"v_mfma_f32_16x16x32_bf16 v[48:51], v[208:211], v[16:19], v[48:51]\n"
"ds_read_b128 v[32:35], v6 offset:61440\n"
"v_mfma_f32_16x16x32_bf16 v[52:55], v[212:215], v[16:19], v[52:55]\n"
"ds_read_b128 v[36:39], v6 offset:61696\n"
"v_mfma_f32_16x16x32_bf16 v[60:63], v[212:215], v[20:23], v[60:63]\n"
"ds_read_b128 v[40:43], v6 offset:61952\n"
"v_mfma_f32_16x16x32_bf16 v[56:59], v[208:211], v[20:23], v[56:59]\n"
"ds_read_b128 v[44:47], v6 offset:62208\n"
"v_mfma_f32_16x16x32_bf16 v[64:67], v[208:211], v[24:27], v[64:67]\n"
"buffer_load_dwordx4 v[184:187], v14, s[4:7], s13 offen offset:1024 sc1\n"
"v_mfma_f32_16x16x32_bf16 v[68:71], v[212:215], v[24:27], v[68:71]\n"
"v_mfma_f32_16x16x32_bf16 v[76:79], v[212:215], v[28:31], v[76:79]\n"
"v_mfma_f32_16x16x32_bf16 v[72:75], v[208:211], v[28:31], v[72:75]\n"
"buffer_load_dwordx4 v[188:191], v14, s[4:7], s13 offen offset:2048 sc1\n"
"s_waitcnt lgkmcnt(0)\n"
"v_mfma_f32_16x16x32_bf16 v[48:51], v[216:219], v[32:35], v[48:51]\n"
"ds_read_b128 v[16:19], v7 offset:0\n"
"v_mfma_f32_16x16x32_bf16 v[52:55], v[220:223], v[32:35], v[52:55]\n"
"ds_read_b128 v[20:23], v7 offset:256\n"
"v_mfma_f32_16x16x32_bf16 v[60:63], v[220:223], v[36:39], v[60:63]\n"
"ds_read_b128 v[24:27], v7 offset:512\n"
"v_mfma_f32_16x16x32_bf16 v[56:59], v[216:219], v[36:39], v[56:59]\n"
"ds_read_b128 v[28:31], v7 offset:768\n"
"v_mfma_f32_16x16x32_bf16 v[64:67], v[216:219], v[40:43], v[64:67]\n"
"s_add_u32 s13, s25, 163840\n"
"buffer_load_dwordx4 v[192:195], v14, s[4:7], s13 offen sc1\n"
"v_mfma_f32_16x16x32_bf16 v[68:71], v[220:223], v[40:43], v[68:71]\n"
"v_mfma_f32_16x16x32_bf16 v[76:79], v[220:223], v[44:47], v[76:79]\n"
"v_mfma_f32_16x16x32_bf16 v[72:75], v[216:219], v[44:47], v[72:75]\n"
"buffer_load_dwordx4 v[196:199], v14, s[4:7], s13 offen offset:1024 sc1\n"
"s_waitcnt lgkmcnt(0)\n"
"v_mfma_f32_16x16x32_bf16 v[48:51], v[224:227], v[16:19], v[48:51]\n"
"ds_read_b128 v[32:35], v7 offset:4096\n"
"v_mfma_f32_16x16x32_bf16 v[52:55], v[228:231], v[16:19], v[52:55]\n"
"ds_read_b128 v[36:39], v7 offset:4352\n"
"v_mfma_f32_16x16x32_bf16 v[60:63], v[228:231], v[20:23], v[60:63]\n"
"ds_read_b128 v[40:43], v7 offset:4608\n"
"v_mfma_f32_16x16x32_bf16 v[56:59], v[224:227], v[20:23], v[56:59]\n"
"ds_read_b128 v[44:47], v7 offset:4864\n"
"v_mfma_f32_16x16x32_bf16 v[64:67], v[224:227], v[24:27], v[64:67]\n"
"buffer_load_dwordx4 v[200:203], v14, s[4:7], s13 offen offset:2048 sc1\n"
"v_mfma_f32_16x16x32_bf16 v[68:71], v[228:231], v[24:27], v[68:71]\n"
"v_mfma_f32_16x16x32_bf16 v[76:79], v[228:231], v[28:31], v[76:79]\n"
"v_mfma_f32_16x16x32_bf16 v[72:75], v[224:227], v[28:31], v[72:75]\n"
"s_add_u32 s13, s25, 184320\n"
"buffer_load_dwordx4 v[204:207], v14, s[4:7], s13 offen sc1\n"
"s_waitcnt lgkmcnt(0)\n"
"v_mfma_f32_16x16x32_bf16 v[48:51], v[232:235], v[32:35], v[48:51]\n"
"ds_read_b128 v[16:19], v7 offset:8192\n"
"v_mfma_f32_16x16x32_bf16 v[52:55], v[236:239], v[32:35], v[52:55]\n"
"ds_read_b128 v[20:23], v7 offset:8448\n"
"v_mfma_f32_16x16x32_bf16 v[60:63], v[236:239], v[36:39], v[60:63]\n"
"ds_read_b128 v[24:27], v7 offset:8704\n"
"v_mfma_f32_16x16x32_bf16 v[56:59], v[232:235], v[36:39], v[56:59]\n"
"ds_read_b128 v[28:31], v7 offset:8960\n"
"v_mfma_f32_16x16x32_bf16 v[64:67], v[232:235], v[40:43], v[64:67]\n"
"buffer_load_dwordx4 v[208:211], v14, s[4:7], s13 offen offset:1024 sc1\n"
"v_mfma_f32_16x16x32_bf16 v[68:71], v[236:239], v[40:43], v[68:71]\n"
"v_mfma_f32_16x16x32_bf16 v[76:79], v[236:239], v[44:47], v[76:79]\n"
"v_mfma_f32_16x16x32_bf16 v[72:75], v[232:235], v[44:47], v[72:75]\n"
"buffer_load_dwordx4 v[212:215], v14, s[4:7], s13 offen offset:2048 sc1\n"
"s_waitcnt lgkmcnt(0)\n"
"v_mfma_f32_16x16x32_bf16 v[48:51], v[240:243], v[16:19], v[48:51]\n"
"ds_read_b128 v[32:35], v7 offset:12288\n"
"v_mfma_f32_16x16x32_bf16 v[52:55], v[244:247], v[16:19], v[52:55]\n"
"ds_read_b128 v[36:39], v7 offset:12544\n"
"v_mfma_f32_16x16x32_bf16 v[60:63], v[244:247], v[20:23], v[60:63]\n"
"ds_read_b128 v[40:43], v7 offset:12800\n"
"v_mfma_f32_16x16x32_bf16 v[56:59], v[240:243], v[20:23], v[56:59]\n"
"ds_read_b128 v[44:47], v7 offset:13056\n"
"v_mfma_f32_16x16x32_bf16 v[64:67], v[240:243], v[24:27], v[64:67]\n"
"s_add_u32 s13, s25, 204800\n"
"buffer_load_dwordx4 v[216:219], v14, s[4:7], s13 offen sc1\n"
"v_mfma_f32_16x16x32_bf16 v[68:71], v[244:247], v[24:27], v[68:71]\n"
"v_mfma_f32_16x16x32_bf16 v[76:79], v[244:247], v[28:31], v[76:79]\n"
"v_mfma_f32_16x16x32_bf16 v[72:75], v[240:243], v[28:31], v[72:75]\n"
"buffer_load_dwordx4 v[220:223], v14, s[4:7], s13 offen offset:1024 sc1\n"
"s_waitcnt lgkmcnt(0)\n"
"v_mfma_f32_16x16x32_bf16 v[48:51], v[248:251], v[32:35], v[48:51]\n"
"buffer_load_dwordx4 v[224:227], v14, s[4:7], s13 offen offset:2048 sc1\n"
"v_mfma_f32_16x16x32_bf16 v[52:55], v[252:255], v[32:35], v[52:55]\n"
"v_mfma_f32_16x16x32_bf16 v[60:63], v[252:255], v[36:39], v[60:63]\n"
"v_mfma_f32_16x16x32_bf16 v[56:59], v[248:251], v[36:39], v[56:59]\n"
"s_add_u32 s13, s25, 225280\n"
"buffer_load_dwordx4 v[228:231], v14, s[4:7], s13 offen sc1\n"
"v_mfma_f32_16x16x32_bf16 v[64:67], v[248:251], v[40:43], v[64:67]\n"
"v_mfma_f32_16x16x32_bf16 v[68:71], v[252:255], v[40:43], v[68:71]\n"
"v_mfma_f32_16x16x32_bf16 v[76:79], v[252:255], v[44:47], v[76:79]\n"
"v_mfma_f32_16x16x32_bf16 v[72:75], v[248:251], v[44:47], v[72:75]\n"
"v_mov_b32 v2, s32\n"
"v_mov_b32 v3, s33\n"
"v_fma_f32 v48, -v88, v80, v48\n"
"v_fma_f32 v49, -v88, v81, v49\n"
"v_fma_f32 v50, -v88, v82, v50\n"
"v_fma_f32 v51, -v88, v83, v51\n"
"v_fma_f32 v52, -v88, v84, v52\n"
"v_fma_f32 v53, -v88, v85, v53\n"
"v_fma_f32 v54, -v88, v86, v54\n"
"v_fma_f32 v55, -v88, v87, v55\n"
"v_maximum3_f32 v48, v48, 0, 0\n"
"v_maximum3_f32 v49, v49, 0, 0\n"
"v_maximum3_f32 v50, v50, 0, 0\n"
"v_maximum3_f32 v51, v51, 0, 0\n"
"v_maximum3_f32 v52, v52, 0, 0\n"
"v_maximum3_f32 v53, v53, 0, 0\n"
"v_maximum3_f32 v54, v54, 0, 0\n"
"v_maximum3_f32 v55, v55, 0, 0\n"
"v_cvt_pk_bf16_f32 v32, v48, v49\n"
"v_cvt_pk_bf16_f32 v33, v50, v51\n"
"v_cvt_pk_bf16_f32 v34, v52, v53\n"
"v_cvt_pk_bf16_f32 v35, v54, v55\n"
"buffer_store_dwordx4 v[32:35], v6, s[4:7], s48 offen offset:0 sc1\n"
"v_fma_f32 v56, -v89, v80, v56\n"
"v_fma_f32 v57, -v89, v81, v57\n"
"v_fma_f32 v58, -v89, v82, v58\n"
"v_fma_f32 v59, -v89, v83, v59\n"
"v_fma_f32 v60, -v89, v84, v60\n"
"v_fma_f32 v61, -v89, v85, v61\n"
"v_fma_f32 v62, -v89, v86, v62\n"
"v_fma_f32 v63, -v89, v87, v63\n"
"v_maximum3_f32 v56, v56, 0, 0\n"
"v_maximum3_f32 v57, v57, 0, 0\n"
"v_maximum3_f32 v58, v58, 0, 0\n"
"v_maximum3_f32 v59, v59, 0, 0\n"
"v_maximum3_f32 v60, v60, 0, 0\n"
"v_maximum3_f32 v61, v61, 0, 0\n"
"v_maximum3_f32 v62, v62, 0, 0\n"
"v_maximum3_f32 v63, v63, 0, 0\n"
"v_cvt_pk_bf16_f32 v36, v56, v57\n"
"v_cvt_pk_bf16_f32 v37, v58, v59\n"
"v_cvt_pk_bf16_f32 v38, v60, v61\n"
"v_cvt_pk_bf16_f32 v39, v62, v63\n"
"buffer_store_dwordx4 v[36:39], v6, s[4:7], s48 offen offset:256 sc1\n"
"v_fma_f32 v64, -v90, v80, v64\n"
"v_fma_f32 v65, -v90, v81, v65\n"
"v_fma_f32 v66, -v90, v82, v66\n"
"v_fma_f32 v67, -v90, v83, v67\n"
"v_fma_f32 v68, -v90, v84, v68\n"
"v_fma_f32 v69, -v90, v85, v69\n"
"v_fma_f32 v70, -v90, v86, v70\n"
"v_fma_f32 v71, -v90, v87, v71\n"
"v_maximum3_f32 v64, v64, 0, 0\n"
"v_maximum3_f32 v65, v65, 0, 0\n"
"v_maximum3_f32 v66, v66, 0, 0\n"
"v_maximum3_f32 v67, v67, 0, 0\n"
"v_maximum3_f32 v68, v68, 0, 0\n"
"v_maximum3_f32 v69, v69, 0, 0\n"
"v_maximum3_f32 v70, v70, 0, 0\n"
"v_maximum3_f32 v71, v71, 0, 0\n"
"v_cvt_pk_bf16_f32 v40, v64, v65\n"
"v_cvt_pk_bf16_f32 v41, v66, v67\n"
"v_cvt_pk_bf16_f32 v42, v68, v69\n"
"v_cvt_pk_bf16_f32 v43, v70, v71\n"
"buffer_store_dwordx4 v[40:43], v6, s[4:7], s48 offen offset:512 sc1\n"
"v_fma_f32 v72, -v91, v80, v72\n"
"v_fma_f32 v73, -v91, v81, v73\n"
"v_fma_f32 v74, -v91, v82, v74\n"
"v_fma_f32 v75, -v91, v83, v75\n"
"v_fma_f32 v76, -v91, v84, v76\n"
"v_fma_f32 v77, -v91, v85, v77\n"
"v_fma_f32 v78, -v91, v86, v78\n"
"v_fma_f32 v79, -v91, v87, v79\n"
"v_maximum3_f32 v72, v72, 0, 0\n"
"v_maximum3_f32 v73, v73, 0, 0\n"
"v_maximum3_f32 v74, v74, 0, 0\n"
"v_maximum3_f32 v75, v75, 0, 0\n"
"v_maximum3_f32 v76, v76, 0, 0\n"
"v_maximum3_f32 v77, v77, 0, 0\n"
"v_maximum3_f32 v78, v78, 0, 0\n"
"v_maximum3_f32 v79, v79, 0, 0\n"
"v_cvt_pk_bf16_f32 v44, v72, v73\n"
"v_cvt_pk_bf16_f32 v45, v74, v75\n"
"v_cvt_pk_bf16_f32 v46, v76, v77\n"
"v_cvt_pk_bf16_f32 v47, v78, v79\n"
"buffer_store_dwordx4 v[44:47], v6, s[4:7], s48 offen offset:768 sc1\n"
"v_lshlrev_b32 v5, 7, v0\n"
"s_waitcnt vmcnt(0)\n"
"s_mov_b64 exec, 0xff\n"
"buffer_store_dwordx4 v[2:5], v14, s[4:7], s45 offen sc1\n"
"s_mov_b64 exec, -1\n"
"buffer_load_dwordx4 v[232:235], v14, s[4:7], s13 offen offset:1024 sc1\n"
"buffer_load_dwordx4 v[236:239], v14, s[4:7], s13 offen offset:2048 sc1\n"
"s_add_u32 s13, s25, 245760\n"
"buffer_load_dwordx4 v[240:243], v14, s[4:7], s13 offen sc1\n"
"buffer_load_dwordx4 v[244:247], v14, s[4:7], s13 offen offset:1024 sc1\n"
"buffer_load_dwordx4 v[248:251], v14, s[4:7], s13 offen offset:2048 sc1\n"
"v_and_b32 v1, 7, v0\n"
"v_lshlrev_b32 v254, 12, v1\n"
"v_add_u32 v1, 0x8000, v254\n"
"s_sub_u32 s56, 3, s3\n"
"s_lshr_b32 s57, s56, 1\n"
"s_lshl_b32 s58, s57, 12\n"
"s_add_u32 s59, 0xc000, s58\n"
"v_min_u32 v255, s59, v1\n"
"s_lshl_b32 s56, s3, 8\n"
"v_add_u32 v1, s56, v6\n"
"v_add_u32 v7, s51, v1\n"
"v_xor_b32 v2, 0x100, v7\n"
"v_xor_b32 v3, 0x200, v7\n"
"v_xor_b32 v4, 0x300, v7\n"
"s_barrier\n"
"buffer_load_dwordx2 v[252:253], v254, s[4:7], s43 offen sc1\n"
"s_waitcnt vmcnt(0)\n"
"v_cmp_ne_u64 vcc, s[32:33], v[252:253]\n"
"s_nop 0\n"
"s_cmp_eq_u64 vcc, 0\n"
"s_cbranch_scc1 .Lk_polld_53\n"
".Lk_poll_52:\n"
"s_sleep 4\n"
"buffer_load_dwordx2 v[252:253], v254, s[4:7], s43 offen sc1\n"
"s_waitcnt vmcnt(0)\n"
"v_cmp_ne_u64 vcc, s[32:33], v[252:253]\n"
"s_nop 0\n"
"s_cmp_eq_u64 vcc, 0\n"
"s_cbranch_scc1 .Lk_polld_53\n"
"s_sub_u32 s37, s37, 1\n"
"s_cmp_gt_i32 s37, 0\n"
"s_cbranch_scc1 .Lk_poll_52\n"
".Lk_polld_53:\n"
"s_add_u32 m0, s51, 0\n"
"s_add_u32 s42, s50, 0\n"
"buffer_load_dwordx4 v14, s[4:7], s42 offen sc1 lds\n"
"buffer_load_dwordx4 v14, s[4:7], s42 offen offset:1024 sc1 lds\n"
"buffer_load_dwordx4 v14, s[4:7], s42 offen offset:2048 sc1 lds\n"
"buffer_load_dwordx4 v14, s[4:7], s42 offen offset:3072 sc1 lds\n"
"s_add_u32 m0, s51, 4096\n"
"s_add_u32 s42, s50, 16384\n"
"buffer_load_dwordx4 v14, s[4:7], s42 offen sc1 lds\n"
"buffer_load_dwordx4 v14, s[4:7], s42 offen offset:1024 sc1 lds\n"
"buffer_load_dwordx4 v14, s[4:7], s42 offen offset:2048 sc1 lds\n"
"buffer_load_dwordx4 v14, s[4:7], s42 offen offset:3072 sc1 lds\n"
"s_setprio 0\n"
"s_waitcnt vmcnt(4)\n"
"s_barrier\n"
"ds_read_b128 v[16:19], v7 offset:0\n"
"ds_read_b128 v[20:23], v2 offset:0\n"
"ds_read_b128 v[24:27], v3 offset:0\n"
"ds_read_b128 v[28:31], v4 offset:0\n"
"s_waitcnt vmcnt(0)\n"
"s_barrier\n"
"s_waitcnt lgkmcnt(3)\n"
"v_mfma_f32_16x16x32_bf16 v[48:51], v[96:99], v[16:19], 0\n"
"ds_read_b128 v[32:35], v7 offset:4096\n"
"v_mfma_f32_16x16x32_bf16 v[52:55], v[100:103], v[16:19], 0\n"
"ds_read_b128 v[36:39], v2 offset:4096\n"
"v_mfma_f32_16x16x32_bf16 v[56:59], v[104:107], v[16:19], 0\n"
"ds_read_b128 v[40:43], v3 offset:4096\n"
"s_add_u32 m0, s51, 16384\n"
"s_add_u32 s42, s50, 65536\n"
"buffer_load_dwordx4 v14, s[4:7], s42 offen sc1 lds\n"
"s_waitcnt lgkmcnt(5)\n"
"v_mfma_f32_16x16x32_bf16 v[68:71], v[104:107], v[20:23], 0\n"
"ds_read_b128 v[44:47], v4 offset:4096\n"
"v_mfma_f32_16x16x32_bf16 v[64:67], v[100:103], v[20:23], 0\n"
"v_mfma_f32_16x16x32_bf16 v[60:63], v[96:99], v[20:23], 0\n"
"buffer_load_dwordx4 v14, s[4:7], s42 offen offset:1024 sc1 lds\n"
"s_waitcnt lgkmcnt(5)\n"
"v_mfma_f32_16x16x32_bf16 v[72:75], v[96:99], v[24:27], 0\n"
"v_mfma_f32_16x16x32_bf16 v[76:79], v[100:103], v[24:27], 0\n"
"v_mfma_f32_16x16x32_bf16 v[80:83], v[104:107], v[24:27], 0\n"
"s_waitcnt lgkmcnt(4)\n"
"v_mfma_f32_16x16x32_bf16 v[92:95], v[104:107], v[28:31], 0\n"
"v_mfma_f32_16x16x32_bf16 v[88:91], v[100:103], v[28:31], 0\n"
"v_mfma_f32_16x16x32_bf16 v[84:87], v[96:99], v[28:31], 0\n"
"s_barrier\n"
"s_waitcnt lgkmcnt(0)\n"
"v_mfma_f32_16x16x32_bf16 v[48:51], v[108:111], v[32:35], v[48:51]\n"
"ds_read_b128 v[16:19], v7 offset:8192\n"
"v_mfma_f32_16x16x32_bf16 v[52:55], v[112:115], v[32:35], v[52:55]\n"
"ds_read_b128 v[20:23], v2 offset:8192\n"
"v_mfma_f32_16x16x32_bf16 v[56:59], v[116:119], v[32:35], v[56:59]\n"
"ds_read_b128 v[24:27], v3 offset:8192\n"
"buffer_load_dwordx2 v[252:253], v255, s[4:7], s43 offen sc1\n"
"v_mfma_f32_16x16x32_bf16 v[68:71], v[116:119], v[36:39], v[68:71]\n"
"ds_read_b128 v[28:31], v4 offset:8192\n"
"v_mfma_f32_16x16x32_bf16 v[64:67], v[112:115], v[36:39], v[64:67]\n"
"v_mfma_f32_16x16x32_bf16 v[60:63], v[108:111], v[36:39], v[60:63]\n"
"s_add_u32 m0, s51, 20480\n"
"s_add_u32 s42, s50, 81920\n"
"buffer_load_dwordx4 v14, s[4:7], s42 offen sc1 lds\n"
"v_mfma_f32_16x16x32_bf16 v[72:75], v[108:111], v[40:43], v[72:75]\n"
"v_mfma_f32_16x16x32_bf16 v[76:79], v[112:115], v[40:43], v[76:79]\n"
"v_mfma_f32_16x16x32_bf16 v[80:83], v[116:119], v[40:43], v[80:83]\n"
"buffer_load_dwordx4 v14, s[4:7], s42 offen offset:1024 sc1 lds\n"
"v_mfma_f32_16x16x32_bf16 v[92:95], v[116:119], v[44:47], v[92:95]\n"
"v_mfma_f32_16x16x32_bf16 v[88:91], v[112:115], v[44:47], v[88:91]\n"
"v_mfma_f32_16x16x32_bf16 v[84:87], v[108:111], v[44:47], v[84:87]\n"
"s_barrier\n"
"s_waitcnt lgkmcnt(0)\n"
"v_mfma_f32_16x16x32_bf16 v[48:51], v[120:123], v[16:19], v[48:51]\n"
"ds_read_b128 v[32:35], v7 offset:12288\n"
"v_mfma_f32_16x16x32_bf16 v[52:55], v[124:127], v[16:19], v[52:55]\n"
"ds_read_b128 v[36:39], v2 offset:12288\n"
"v_mfma_f32_16x16x32_bf16 v[56:59], v[128:131], v[16:19], v[56:59]\n"
"ds_read_b128 v[40:43], v3 offset:12288\n"
"s_add_u32 m0, s51, 24576\n"
"s_add_u32 s42, s50, 98304\n"
"buffer_load_dwordx4 v14, s[4:7], s42 offen sc1 lds\n"
"v_mfma_f32_16x16x32_bf16 v[68:71], v[128:131], v[20:23], v[68:71]\n"
"ds_read_b128 v[44:47], v4 offset:12288\n"
"v_mfma_f32_16x16x32_bf16 v[64:67], v[124:127], v[20:23], v[64:67]\n"
"v_mfma_f32_16x16x32_bf16 v[60:63], v[120:123], v[20:23], v[60:63]\n"
"buffer_load_dwordx4 v14, s[4:7], s42 offen offset:1024 sc1 lds\n"
"v_mfma_f32_16x16x32_bf16 v[72:75], v[120:123], v[24:27], v[72:75]\n"
"v_mfma_f32_16x16x32_bf16 v[76:79], v[124:127], v[24:27], v[76:79]\n"
"v_mfma_f32_16x16x32_bf16 v[80:83], v[128:131], v[24:27], v[80:83]\n"
"v_mfma_f32_16x16x32_bf16 v[92:95], v[128:131], v[28:31], v[92:95]\n"
"v_mfma_f32_16x16x32_bf16 v[88:91], v[124:127], v[28:31], v[88:91]\n"
"v_mfma_f32_16x16x32_bf16 v[84:87], v[120:123], v[28:31], v[84:87]\n"
"s_waitcnt vmcnt(5)\n"
"s_barrier\n"
"s_waitcnt lgkmcnt(0)\n"
"v_mfma_f32_16x16x32_bf16 v[48:51], v[132:135], v[32:35], v[48:51]\n"
"ds_read_b128 v[16:19], v7 offset:16384\n"
"v_mfma_f32_16x16x32_bf16 v[52:55], v[136:139], v[32:35], v[52:55]\n"
"ds_read_b128 v[20:23], v2 offset:16384\n"
"v_mfma_f32_16x16x32_bf16 v[56:59], v[140:143], v[32:35], v[56:59]\n"
"ds_read_b128 v[24:27], v3 offset:16384\n"
"s_add_u32 m0, s51, 28672\n"
"s_add_u32 s42, s50, 114688\n"
"buffer_load_dwordx4 v14, s[4:7], s42 offen sc1 lds\n"
"v_mfma_f32_16x16x32_bf16 v[68:71], v[140:143], v[36:39], v[68:71]\n"
"ds_read_b128 v[28:31], v4 offset:16384\n"
"v_mfma_f32_16x16x32_bf16 v[64:67], v[136:139], v[36:39], v[64:67]\n"
"v_mfma_f32_16x16x32_bf16 v[60:63], v[132:135], v[36:39], v[60:63]\n"
"buffer_load_dwordx4 v14, s[4:7], s42 offen offset:1024 sc1 lds\n"
"v_mfma_f32_16x16x32_bf16 v[72:75], v[132:135], v[40:43], v[72:75]\n"
"v_mfma_f32_16x16x32_bf16 v[76:79], v[136:139], v[40:43], v[76:79]\n"
"v_mfma_f32_16x16x32_bf16 v[80:83], v[140:143], v[40:43], v[80:83]\n"
"v_mfma_f32_16x16x32_bf16 v[92:95], v[140:143], v[44:47], v[92:95]\n"
"v_mfma_f32_16x16x32_bf16 v[88:91], v[136:139], v[44:47], v[88:91]\n"
"v_mfma_f32_16x16x32_bf16 v[84:87], v[132:135], v[44:47], v[84:87]\n"
"s_cbranch_execz .Lk_tramp\n"
"s_nop 0\n"
"s_waitcnt vmcnt(6)\n"
"v_cmp_ne_u64 vcc, s[32:33], v[252:253]\n"
"s_nop 0\n"
"s_cmp_eq_u64 vcc, 0\n"
"s_cbranch_scc1 .Lk_polld_56\n"
".Lk_poll_55:\n"
"s_sleep 4\n"
"buffer_load_dwordx2 v[252:253], v255, s[4:7], s43 offen sc1\n"
"s_waitcnt vmcnt(0)\n"
"v_cmp_ne_u64 vcc, s[32:33], v[252:253]\n"
"s_nop 0\n"
"s_cmp_eq_u64 vcc, 0\n"
"s_cbranch_scc1 .Lk_polld_56\n"
"s_sub_u32 s37, s37, 1\n"
"s_cmp_gt_i32 s37, 0\n"
"s_cbranch_scc1 .Lk_poll_55\n"
".Lk_polld_56:\n"
"s_waitcnt vmcnt(4)\n"
"s_barrier\n"
"s_waitcnt lgkmcnt(0)\n"
"v_mfma_f32_16x16x32_bf16 v[48:51], v[144:147], v[16:19], v[48:51]\n"
"ds_read_b128 v[32:35], v7 offset:20480\n"
"v_mfma_f32_16x16x32_bf16 v[52:55], v[148:151], v[16:19], v[52:55]\n"
"ds_read_b128 v[36:39], v2 offset:20480\n"
"v_mfma_f32_16x16x32_bf16 v[56:59], v[152:155], v[16:19], v[56:59]\n"
"ds_read_b128 v[40:43], v3 offset:20480\n"
"s_add_u32 m0, s51, 32768\n"
"s_add_u32 s42, s50, 131072\n"
"buffer_load_dwordx4 v14, s[4:7], s42 offen sc1 lds\n"
"v_mfma_f32_16x16x32_bf16 v[68:71], v[152:155], v[20:23], v[68:71]\n"
"ds_read_b128 v[44:47], v4 offset:20480\n"
"v_mfma_f32_16x16x32_bf16 v[64:67], v[148:151], v[20:23], v[64:67]\n"
"v_mfma_f32_16x16x32_bf16 v[60:63], v[144:147], v[20:23], v[60:63]\n"
"buffer_load_dwordx4 v14, s[4:7], s42 offen offset:1024 sc1 lds\n"
"v_mfma_f32_16x16x32_bf16 v[72:75], v[144:147], v[24:27], v[72:75]\n"
"v_mfma_f32_16x16x32_bf16 v[76:79], v[148:151], v[24:27], v[76:79]\n"
"v_mfma_f32_16x16x32_bf16 v[80:83], v[152:155], v[24:27], v[80:83]\n"
"v_mfma_f32_16x16x32_bf16 v[92:95], v[152:155], v[28:31], v[92:95]\n"
"v_mfma_f32_16x16x32_bf16 v[88:91], v[148:151], v[28:31], v[88:91]\n"
"v_mfma_f32_16x16x32_bf16 v[84:87], v[144:147], v[28:31], v[84:87]\n"
"s_waitcnt vmcnt(4)\n"
"s_barrier\n"
"s_waitcnt lgkmcnt(0)\n"
"v_mfma_f32_16x16x32_bf16 v[48:51], v[156:159], v[32:35], v[48:51]\n"
"ds_read_b128 v[16:19], v7 offset:24576\n"
"v_mfma_f32_16x16x32_bf16 v[52:55], v[160:163], v[32:35], v[52:55]\n"
"ds_read_b128 v[20:23], v2 offset:24576\n"
"v_mfma_f32_16x16x32_bf16 v[56:59], v[164:167], v[32:35], v[56:59]\n"
"ds_read_b128 v[24:27], v3 offset:24576\n"
"s_add_u32 m0, s51, 36864\n"
"s_add_u32 s42, s50, 147456\n"
"buffer_load_dwordx4 v14, s[4:7], s42 offen sc1 lds\n"
"v_mfma_f32_16x16x32_bf16 v[68:71], v[164:167], v[36:39], v[68:71]\n"
"ds_read_b128 v[28:31], v4 offset:24576\n"
"v_mfma_f32_16x16x32_bf16 v[64:67], v[160:163], v[36:39], v[64:67]\n"
"v_mfma_f32_16x16x32_bf16 v[60:63], v[156:159], v[36:39], v[60:63]\n"
"buffer_load_dwordx4 v14, s[4:7], s42 offen offset:1024 sc1 lds\n"
"v_mfma_f32_16x16x32_bf16 v[72:75], v[156:159], v[40:43], v[72:75]\n"
"v_mfma_f32_16x16x32_bf16 v[76:79], v[160:163], v[40:43], v[76:79]\n"
"v_mfma_f32_16x16x32_bf16 v[80:83], v[164:167], v[40:43], v[80:83]\n"
"v_mfma_f32_16x16x32_bf16 v[92:95], v[164:167], v[44:47], v[92:95]\n"
"v_mfma_f32_16x16x32_bf16 v[88:91], v[160:163], v[44:47], v[88:91]\n"
"v_mfma_f32_16x16x32_bf16 v[84:87], v[156:159], v[44:47], v[84:87]\n"
"s_waitcnt vmcnt(4)\n"
"s_barrier\n"
"s_waitcnt lgkmcnt(0)\n"
"v_mfma_f32_16x16x32_bf16 v[48:51], v[168:171], v[16:19], v[48:51]\n"
"ds_read_b128 v[32:35], v7 offset:28672\n"
"v_mfma_f32_16x16x32_bf16 v[52:55], v[172:175], v[16:19], v[52:55]\n"
"ds_read_b128 v[36:39], v2 offset:28672\n"
"v_mfma_f32_16x16x32_bf16 v[56:59], v[176:179], v[16:19], v[56:59]\n"
"ds_read_b128 v[40:43], v3 offset:28672\n"
"s_add_u32 m0, s51, 0\n"
"s_add_u32 s42, s50, 163840\n"
"buffer_load_dwordx4 v14, s[4:7], s42 offen sc1 lds\n"
"v_mfma_f32_16x16x32_bf16 v[68:71], v[176:179], v[20:23], v[68:71]\n"
"ds_read_b128 v[44:47], v4 offset:28672\n"
"v_mfma_f32_16x16x32_bf16 v[64:67], v[172:175], v[20:23], v[64:67]\n"
"s_xor_b32 s56, s3, 1\n"
"v_mfma_f32_16x16x32_bf16 v[60:63], v[168:171], v[20:23], v[60:63]\n"
"s_mul_i32 s57, s56, 0xa000\n"
"buffer_load_dwordx4 v14, s[4:7], s42 offen offset:1024 sc1 lds\n"
"v_mfma_f32_16x16x32_bf16 v[72:75], v[168:171], v[24:27], v[72:75]\n"
"s_add_u32 s58, s57, 0x4000\n"
"v_mfma_f32_16x16x32_bf16 v[76:79], v[172:175], v[24:27], v[76:79]\n"
"v_add_u32 v121, s58, v14\n"
"v_mfma_f32_16x16x32_bf16 v[80:83], v[176:179], v[24:27], v[80:83]\n"
"s_xor_b32 s56, s3, 2\n"
"s_cmp_lt_u32 s3, 2\n"
"s_cselect_b32 s25, s25, 0x7ff00000\n"
"v_mfma_f32_16x16x32_bf16 v[92:95], v[176:179], v[28:31], v[92:95]\n"
"s_mul_i32 s57, s56, 0xa000\n"
"v_mfma_f32_16x16x32_bf16 v[88:91], v[172:175], v[28:31], v[88:91]\n"
"s_add_u32 s58, s57, 0x4c00\n"
"v_mfma_f32_16x16x32_bf16 v[84:87], v[168:171], v[28:31], v[84:87]\n"
"v_add_u32 v122, s58, v14\n"
"s_add_u32 s13, s25, 266240\n"
"buffer_load_dwordx4 v[96:99], v14, s[4:7], s13 offen sc1\n"
"buffer_load_dwordx4 v[100:103], v14, s[4:7], s13 offen offset:1024 sc1\n"
"buffer_load_dwordx4 v[104:107], v14, s[4:7], s13 offen offset:2048 sc1\n"
"s_waitcnt vmcnt(7)\n"
"s_barrier\n"
"s_waitcnt lgkmcnt(0)\n"
"v_mfma_f32_16x16x32_bf16 v[48:51], v[180:183], v[32:35], v[48:51]\n"
"ds_read_b128 v[16:19], v7 offset:32768\n"
"v_mfma_f32_16x16x32_bf16 v[52:55], v[184:187], v[32:35], v[52:55]\n"
"ds_read_b128 v[20:23], v2 offset:32768\n"
"v_mfma_f32_16x16x32_bf16 v[56:59], v[188:191], v[32:35], v[56:59]\n"
"ds_read_b128 v[24:27], v3 offset:32768\n"
"s_add_u32 m0, s51, 4096\n"
"s_add_u32 s42, s50, 180224\n"
"buffer_load_dwordx4 v14, s[4:7], s42 offen sc1 lds\n"
"v_mfma_f32_16x16x32_bf16 v[68:71], v[188:191], v[36:39], v[68:71]\n"
"ds_read_b128 v[28:31], v4 offset:32768\n"
"v_mfma_f32_16x16x32_bf16 v[64:67], v[184:187], v[36:39], v[64:67]\n"
"s_xor_b32 s56, s3, 3\n"
"v_mfma_f32_16x16x32_bf16 v[60:63], v[180:183], v[36:39], v[60:63]\n"
"s_mul_i32 s57, s56, 0xa000\n"
"buffer_load_dwordx4 v14, s[4:7], s42 offen offset:1024 sc1 lds\n"
"v_mfma_f32_16x16x32_bf16 v[72:75], v[180:183], v[40:43], v[72:75]\n"
"s_add_u32 s58, s57, 0x5800\n"
"v_mfma_f32_16x16x32_bf16 v[76:79], v[184:187], v[40:43], v[76:79]\n"
"v_add_u32 v123, s58, v14\n"
"v_mfma_f32_16x16x32_bf16 v[80:83], v[188:191], v[40:43], v[80:83]\n"
"s_add_u32 s56, s51, 0x4000\n"
"v_mfma_f32_16x16x32_bf16 v[92:95], v[188:191], v[44:47], v[92:95]\n"
"v_add_u32 v120, s56, v14\n"
"v_mfma_f32_16x16x32_bf16 v[88:91], v[184:187], v[44:47], v[88:91]\n"
"s_lshl_b32 s56, s62, 6\n"
"v_mfma_f32_16x16x32_bf16 v[84:87], v[180:183], v[44:47], v[84:87]\n"
"s_lshl_b32 s57, s3, 4\n"
"s_waitcnt vmcnt(7)\n"
"s_barrier\n"
"s_waitcnt lgkmcnt(0)\n"
"v_mfma_f32_16x16x32_bf16 v[48:51], v[192:195], v[16:19], v[48:51]\n"
"ds_read_b128 v[32:35], v7 offset:36864\n"
"v_mfma_f32_16x16x32_bf16 v[52:55], v[196:199], v[16:19], v[52:55]\n"
"ds_read_b128 v[36:39], v2 offset:36864\n"
"v_mfma_f32_16x16x32_bf16 v[56:59], v[200:203], v[16:19], v[56:59]\n"
"ds_read_b128 v[40:43], v3 offset:36864\n"
"s_add_u32 m0, s51, 8192\n"
"s_add_u32 s42, s50, 196608\n"
"buffer_load_dwordx4 v14, s[4:7], s42 offen sc1 lds\n"
"v_mfma_f32_16x16x32_bf16 v[68:71], v[200:203], v[20:23], v[68:71]\n"
"ds_read_b128 v[44:47], v4 offset:36864\n"
"v_mfma_f32_16x16x32_bf16 v[64:67], v[196:199], v[20:23], v[64:67]\n"
"s_add_u32 s58, s56, s57\n"
"v_mfma_f32_16x16x32_bf16 v[60:63], v[192:195], v[20:23], v[60:63]\n"
"s_mul_i32 s59, s58, 0x500\n"
"buffer_load_dwordx4 v14, s[4:7], s42 offen offset:1024 sc1 lds\n"
"v_mfma_f32_16x16x32_bf16 v[72:75], v[192:195], v[24:27], v[72:75]\n"
"s_mul_i32 s60, s63, 0xa0\n"
"v_mfma_f32_16x16x32_bf16 v[76:79], v[196:199], v[24:27], v[76:79]\n"
"s_add_u32 s46, s59, s60\n"
"v_mfma_f32_16x16x32_bf16 v[80:83], v[200:203], v[24:27], v[80:83]\n"
"v_lshrrev_b32 v1, 2, v0\n"
"v_mfma_f32_16x16x32_bf16 v[92:95], v[200:203], v[28:31], v[92:95]\n"
"v_mul_u32_u24 v126, 0x500, v1\n"
"v_mfma_f32_16x16x32_bf16 v[88:91], v[196:199], v[28:31], v[88:91]\n"
"v_and_b32 v1, 3, v0\n"
"v_mfma_f32_16x16x32_bf16 v[84:87], v[192:195], v[28:31], v[84:87]\n"
"v_lshlrev_b32 v140, 4, v1\n"
"s_waitcnt vmcnt(7)\n"
"s_barrier\n"
"s_waitcnt lgkmcnt(0)\n"
"v_mfma_f32_16x16x32_bf16 v[48:51], v[204:207], v[32:35], v[48:51]\n"
"ds_read_b128 v[16:19], v7 offset:0\n"
"v_mfma_f32_16x16x32_bf16 v[52:55], v[208:211], v[32:35], v[52:55]\n"
"ds_read_b128 v[20:23], v2 offset:0\n"
"v_mfma_f32_16x16x32_bf16 v[56:59], v[212:215], v[32:35], v[56:59]\n"
"ds_read_b128 v[24:27], v3 offset:0\n"
"s_cmp_ge_u32 s3, 2\n"
"s_cbranch_scc1 .Lk_d13_57\n"
"s_add_u32 m0, s51, 12288\n"
"s_add_u32 s42, s50, 212992\n"
"buffer_load_dwordx4 v14, s[4:7], s42 offen sc1 lds\n"
"buffer_load_dwordx4 v14, s[4:7], s42 offen offset:1024 sc1 lds\n"
".Lk_d13_57:\n"
"v_mfma_f32_16x16x32_bf16 v[68:71], v[212:215], v[36:39], v[68:71]\n"
"ds_read_b128 v[28:31], v4 offset:0\n"
"v_mfma_f32_16x16x32_bf16 v[64:67], v[208:211], v[36:39], v[64:67]\n"
"v_add_u32 v124, v126, v140\n"
"v_mfma_f32_16x16x32_bf16 v[60:63], v[204:207], v[36:39], v[60:63]\n"
"v_and_b32 v1, 3, v0\n"
"v_mfma_f32_16x16x32_bf16 v[72:75], v[204:207], v[40:43], v[72:75]\n"
"v_lshlrev_b32 v140, 3, v1\n"
"v_mfma_f32_16x16x32_bf16 v[76:79], v[208:211], v[40:43], v[76:79]\n"
"v_add_u32 v125, v126, v140\n"
"v_mfma_f32_16x16x32_bf16 v[80:83], v[212:215], v[40:43], v[80:83]\n"
"v_and_b32 v1, 3, v0\n"
"v_mfma_f32_16x16x32_bf16 v[92:95], v[212:215], v[44:47], v[92:95]\n"
"v_lshlrev_b32 v140, 6, v1\n"
"v_mfma_f32_16x16x32_bf16 v[88:91], v[208:211], v[44:47], v[88:91]\n"
"v_and_b32 v141, 60, v0\n"
"v_mfma_f32_16x16x32_bf16 v[84:87], v[204:207], v[44:47], v[84:87]\n"
"v_add_u32 v126, v140, v141\n"
"s_waitcnt vmcnt(2)\n"
"s_barrier\n"
"s_waitcnt lgkmcnt(0)\n"
"v_mfma_f32_16x16x32_bf16 v[48:51], v[216:219], v[16:19], v[48:51]\n"
"ds_read_b128 v[32:35], v7 offset:4096\n"
"v_mfma_f32_16x16x32_bf16 v[52:55], v[220:223], v[16:19], v[52:55]\n"
"ds_read_b128 v[36:39], v2 offset:4096\n"
"v_mfma_f32_16x16x32_bf16 v[56:59], v[224:227], v[16:19], v[56:59]\n"
"ds_read_b128 v[40:43], v3 offset:4096\n"
"v_mfma_f32_16x16x32_bf16 v[68:71], v[224:227], v[20:23], v[68:71]\n"
"ds_read_b128 v[44:47], v4 offset:4096\n"
"v_mfma_f32_16x16x32_bf16 v[64:67], v[220:223], v[20:23], v[64:67]\n"
"v_lshlrev_b32 v128, 16, v8\n"
"v_mfma_f32_16x16x32_bf16 v[60:63], v[216:219], v[20:23], v[60:63]\n"
"v_and_b32 v129, 0xffff0000, v8\n"
"v_mfma_f32_16x16x32_bf16 v[72:75], v[216:219], v[24:27], v[72:75]\n"
"v_lshlrev_b32 v130, 16, v9\n"
"v_mfma_f32_16x16x32_bf16 v[76:79], v[220:223], v[24:27], v[76:79]\n"
"v_and_b32 v131, 0xffff0000, v9\n"
"v_mfma_f32_16x16x32_bf16 v[80:83], v[224:227], v[24:27], v[80:83]\n"
"v_lshlrev_b32 v132, 16, v10\n"
"v_mfma_f32_16x16x32_bf16 v[92:95], v[224:227], v[28:31], v[92:95]\n"
"v_and_b32 v133, 0xffff0000, v10\n"
"v_mfma_f32_16x16x32_bf16 v[88:91], v[220:223], v[28:31], v[88:91]\n"
"v_lshlrev_b32 v134, 16, v11\n"
"v_mfma_f32_16x16x32_bf16 v[84:87], v[216:219], v[28:31], v[84:87]\n"
"v_and_b32 v135, 0xffff0000, v11\n"
"v_lshlrev_b32 v136, 16, v12\n"
"v_and_b32 v137, 0xffff0000, v12\n"
"v_lshlrev_b32 v138, 16, v13\n"
"v_and_b32 v139, 0xffff0000, v13\n"
"s_waitcnt vmcnt(0)\n"
"s_barrier\n"
"s_cmp_ge_u32 s3, 2\n"
"s_cbranch_scc1 .Lk_no13_54\n"
"s_waitcnt lgkmcnt(2)\n"
"v_mfma_f32_16x16x32_bf16 v[60:63], v[228:231], v[36:39], v[60:63]\n"
"ds_read_b128 v[16:19], v2 offset:8192\n"
"v_mfma_f32_16x16x32_bf16 v[64:67], v[232:235], v[36:39], v[64:67]\n"
"ds_read_b128 v[20:23], v2 offset:12288\n"
"v_mfma_f32_16x16x32_bf16 v[68:71], v[236:239], v[36:39], v[68:71]\n"
"ds_read_b128 v[24:27], v3 offset:8192\n"
"s_waitcnt lgkmcnt(4)\n"
"v_mfma_f32_16x16x32_bf16 v[72:75], v[228:231], v[40:43], v[72:75]\n"
"ds_read_b128 v[28:31], v3 offset:12288\n"
"v_mfma_f32_16x16x32_bf16 v[76:79], v[232:235], v[40:43], v[76:79]\n"
"ds_read_b128 v[36:39], v4 offset:8192\n"
"v_mfma_f32_16x16x32_bf16 v[80:83], v[236:239], v[40:43], v[80:83]\n"
"s_waitcnt lgkmcnt(4)\n"
"v_mfma_f32_16x16x32_bf16 v[60:63], v[240:243], v[16:19], v[60:63]\n"
"ds_read_b128 v[40:43], v4 offset:12288\n"
"v_mfma_f32_16x16x32_bf16 v[64:67], v[244:247], v[16:19], v[64:67]\n"
"v_mfma_f32_16x16x32_bf16 v[68:71], v[248:251], v[16:19], v[68:71]\n"
"s_waitcnt lgkmcnt(4)\n"
"v_mfma_f32_16x16x32_bf16 v[60:63], v[96:99], v[20:23], v[60:63]\n"
"ds_read_b128 v[16:19], v7 offset:8192\n"
"v_mfma_f32_16x16x32_bf16 v[64:67], v[100:103], v[20:23], v[64:67]\n"
"v_mfma_f32_16x16x32_bf16 v[68:71], v[104:107], v[20:23], v[68:71]\n"
"s_waitcnt lgkmcnt(4)\n"
"v_mfma_f32_16x16x32_bf16 v[72:75], v[240:243], v[24:27], v[72:75]\n"
"ds_read_b128 v[20:23], v7 offset:12288\n"
"v_mfma_f32_16x16x32_bf16 v[76:79], v[244:247], v[24:27], v[76:79]\n"
"v_mfma_f32_16x16x32_bf16 v[80:83], v[248:251], v[24:27], v[80:83]\n"
"ds_write_b128 v121, v[60:63] offset:0\n"
"s_waitcnt lgkmcnt(5)\n"
"v_mfma_f32_16x16x32_bf16 v[72:75], v[96:99], v[28:31], v[72:75]\n"
"ds_write_b128 v121, v[64:67] offset:1024\n"
"v_mfma_f32_16x16x32_bf16 v[76:79], v[100:103], v[28:31], v[76:79]\n"
"ds_write_b128 v121, v[68:71] offset:2048\n"
"v_mfma_f32_16x16x32_bf16 v[80:83], v[104:107], v[28:31], v[80:83]\n"
"v_mfma_f32_16x16x32_bf16 v[84:87], v[228:231], v[44:47], v[84:87]\n"
"v_mfma_f32_16x16x32_bf16 v[88:91], v[232:235], v[44:47], v[88:91]\n"
"v_mfma_f32_16x16x32_bf16 v[92:95], v[236:239], v[44:47], v[92:95]\n"
"s_waitcnt lgkmcnt(6)\n"
"v_mfma_f32_16x16x32_bf16 v[84:87], v[240:243], v[36:39], v[84:87]\n"
"ds_write_b128 v122, v[72:75] offset:0\n"
"v_mfma_f32_16x16x32_bf16 v[88:91], v[244:247], v[36:39], v[88:91]\n"
"ds_write_b128 v122, v[76:79] offset:1024\n"
"v_mfma_f32_16x16x32_bf16 v[92:95], v[248:251], v[36:39], v[92:95]\n"
"ds_write_b128 v122, v[80:83] offset:2048\n"
"s_waitcnt lgkmcnt(8)\n"
"v_mfma_f32_16x16x32_bf16 v[84:87], v[96:99], v[40:43], v[84:87]\n"
"v_mfma_f32_16x16x32_bf16 v[88:91], v[100:103], v[40:43], v[88:91]\n"
"v_mfma_f32_16x16x32_bf16 v[92:95], v[104:107], v[40:43], v[92:95]\n"
"v_mfma_f32_16x16x32_bf16 v[48:51], v[228:231], v[32:35], v[48:51]\n"
"v_mfma_f32_16x16x32_bf16 v[52:55], v[232:235], v[32:35], v[52:55]\n"
"v_mfma_f32_16x16x32_bf16 v[56:59], v[236:239], v[32:35], v[56:59]\n"
"s_waitcnt lgkmcnt(7)\n"
"v_mfma_f32_16x16x32_bf16 v[48:51], v[240:243], v[16:19], v[48:51]\n"
"v_mfma_f32_16x16x32_bf16 v[52:55], v[244:247], v[16:19], v[52:55]\n"
"ds_write_b128 v123, v[84:87] offset:0\n"
"v_mfma_f32_16x16x32_bf16 v[56:59], v[248:251], v[16:19], v[56:59]\n"
"ds_write_b128 v123, v[88:91] offset:1024\n"
"s_waitcnt lgkmcnt(8)\n"
"v_mfma_f32_16x16x32_bf16 v[48:51], v[96:99], v[20:23], v[48:51]\n"
"ds_write_b128 v123, v[92:95] offset:2048\n"
"v_mfma_f32_16x16x32_bf16 v[52:55], v[100:103], v[20:23], v[52:55]\n"
"v_mfma_f32_16x16x32_bf16 v[56:59], v[104:107], v[20:23], v[56:59]\n"
"s_waitcnt lgkmcnt(0)\n"
"s_branch .Lk_e13_58\n"
".Lk_no13_54:\n"
"s_waitcnt lgkmcnt(2)\n"
"v_mfma_f32_16x16x32_bf16 v[60:63], v[228:231], v[36:39], v[60:63]\n"
"ds_read_b128 v[16:19], v2 offset:8192\n"
"v_mfma_f32_16x16x32_bf16 v[64:67], v[232:235], v[36:39], v[64:67]\n"
"ds_read_b128 v[20:23], v3 offset:8192\n"
"v_mfma_f32_16x16x32_bf16 v[68:71], v[236:239], v[36:39], v[68:71]\n"
"ds_read_b128 v[24:27], v4 offset:8192\n"
"s_waitcnt lgkmcnt(4)\n"
"v_mfma_f32_16x16x32_bf16 v[72:75], v[228:231], v[40:43], v[72:75]\n"
"ds_read_b128 v[28:31], v7 offset:8192\n"
"v_mfma_f32_16x16x32_bf16 v[76:79], v[232:235], v[40:43], v[76:79]\n"
"v_mfma_f32_16x16x32_bf16 v[80:83], v[236:239], v[40:43], v[80:83]\n"
"s_waitcnt lgkmcnt(3)\n"
"v_mfma_f32_16x16x32_bf16 v[60:63], v[240:243], v[16:19], v[60:63]\n"
"v_mfma_f32_16x16x32_bf16 v[64:67], v[244:247], v[16:19], v[64:67]\n"
"v_mfma_f32_16x16x32_bf16 v[68:71], v[248:251], v[16:19], v[68:71]\n"
"s_waitcnt lgkmcnt(2)\n"
"v_mfma_f32_16x16x32_bf16 v[72:75], v[240:243], v[20:23], v[72:75]\n"
"v_mfma_f32_16x16x32_bf16 v[76:79], v[244:247], v[20:23], v[76:79]\n"
"v_mfma_f32_16x16x32_bf16 v[80:83], v[248:251], v[20:23], v[80:83]\n"
"v_mfma_f32_16x16x32_bf16 v[84:87], v[228:231], v[44:47], v[84:87]\n"
"v_mfma_f32_16x16x32_bf16 v[88:91], v[232:235], v[44:47], v[88:91]\n"
"ds_write_b128 v121, v[60:63] offset:0\n"
"v_mfma_f32_16x16x32_bf16 v[92:95], v[236:239], v[44:47], v[92:95]\n"
"ds_write_b128 v121, v[64:67] offset:1024\n"
"s_waitcnt lgkmcnt(3)\n"
"v_mfma_f32_16x16x32_bf16 v[84:87], v[240:243], v[24:27], v[84:87]\n"
"ds_write_b128 v121, v[68:71] offset:2048\n"
"v_mfma_f32_16x16x32_bf16 v[88:91], v[244:247], v[24:27], v[88:91]\n"
"ds_write_b128 v122, v[72:75] offset:0\n"
"v_mfma_f32_16x16x32_bf16 v[92:95], v[248:251], v[24:27], v[92:95]\n"
"ds_write_b128 v122, v[76:79] offset:1024\n"
"v_mfma_f32_16x16x32_bf16 v[48:51], v[228:231], v[32:35], v[48:51]\n"
"ds_write_b128 v122, v[80:83] offset:2048\n"
"v_mfma_f32_16x16x32_bf16 v[52:55], v[232:235], v[32:35], v[52:55]\n"
"ds_write_b128 v123, v[84:87] offset:0\n"
"v_mfma_f32_16x16x32_bf16 v[56:59], v[236:239], v[32:35], v[56:59]\n"
"ds_write_b128 v123, v[88:91] offset:1024\n"
"s_waitcnt lgkmcnt(8)\n"
"v_mfma_f32_16x16x32_bf16 v[48:51], v[240:243], v[28:31], v[48:51]\n"
"ds_write_b128 v123, v[92:95] offset:2048\n"
"v_mfma_f32_16x16x32_bf16 v[52:55], v[244:247], v[28:31], v[52:55]\n"
"v_mfma_f32_16x16x32_bf16 v[56:59], v[248:251], v[28:31], v[56:59]\n"
".Lk_e13_58:\n"
"s_waitcnt lgkmcnt(0)\n"
"s_barrier\n"
"ds_read_b128 v[60:63], v120 offset:0\n"
"ds_read_b128 v[64:67], v120 offset:1024\n"
"ds_read_b128 v[68:71], v120 offset:2048\n"
"ds_read_b128 v[72:75], v120 offset:3072\n"
"ds_read_b128 v[76:79], v120 offset:4096\n"
"ds_read_b128 v[80:83], v120 offset:5120\n"
"ds_read_b128 v[84:87], v120 offset:6144\n"
"ds_read_b128 v[88:91], v120 offset:7168\n"
"ds_read_b128 v[92:95], v120 offset:8192\n"
"s_waitcnt lgkmcnt(8)\n"
"v_pk_add_f32 v[48:49], v[48:49], v[60:61]\n"
"v_pk_add_f32 v[50:51], v[50:51], v[62:63]\n"
"s_waitcnt lgkmcnt(7)\n"
"v_pk_add_f32 v[52:53], v[52:53], v[64:65]\n"
"v_pk_add_f32 v[54:55], v[54:55], v[66:67]\n"
"s_waitcnt lgkmcnt(6)\n"
"v_pk_add_f32 v[56:57], v[56:57], v[68:69]\n"
"v_pk_add_f32 v[58:59], v[58:59], v[70:71]\n"
"s_waitcnt lgkmcnt(5)\n"
"v_pk_add_f32 v[48:49], v[48:49], v[72:73]\n"
"v_pk_add_f32 v[50:51], v[50:51], v[74:75]\n"
"s_waitcnt lgkmcnt(4)\n"
"v_pk_add_f32 v[52:53], v[52:53], v[76:77]\n"
"v_pk_add_f32 v[54:55], v[54:55], v[78:79]\n"
"s_waitcnt lgkmcnt(3)\n"
"v_pk_add_f32 v[56:57], v[56:57], v[80:81]\n"
"v_pk_add_f32 v[58:59], v[58:59], v[82:83]\n"
"s_waitcnt lgkmcnt(2)\n"
"v_pk_add_f32 v[48:49], v[48:49], v[84:85]\n"
"v_pk_add_f32 v[50:51], v[50:51], v[86:87]\n"
"s_waitcnt lgkmcnt(1)\n"
"v_pk_add_f32 v[52:53], v[52:53], v[88:89]\n"
"v_pk_add_f32 v[54:55], v[54:55], v[90:91]\n"
"s_waitcnt lgkmcnt(0)\n"
"v_pk_add_f32 v[56:57], v[56:57], v[92:93]\n"
"v_pk_add_f32 v[58:59], v[58:59], v[94:95]\n"
"v_pk_fma_f32 v[128:129], v[48:49], v[14:15], v[128:129] op_sel:[0,1,0] op_sel_hi:[1,1,1]\n"
"v_pk_fma_f32 v[130:131], v[50:51], v[14:15], v[130:131] op_sel:[0,1,0] op_sel_hi:[1,1,1]\n"
"v_pk_fma_f32 v[132:133], v[52:53], v[14:15], v[132:133] op_sel:[0,1,0] op_sel_hi:[1,1,1]\n"
"v_pk_fma_f32 v[134:135], v[54:55], v[14:15], v[134:135] op_sel:[0,1,0] op_sel_hi:[1,1,1]\n"
"v_pk_fma_f32 v[136:137], v[56:57], v[14:15], v[136:137] op_sel:[0,1,0] op_sel_hi:[1,1,1]\n"
"v_pk_fma_f32 v[138:139], v[58:59], v[14:15], v[138:139] op_sel:[0,1,0] op_sel_hi:[1,1,1]\n"
"v_cvt_pk_bf16_f32 v32, v128, v129\n"
"v_cvt_pk_bf16_f32 v33, v130, v131\n"
"v_cvt_pk_bf16_f32 v34, v132, v133\n"
"v_cvt_pk_bf16_f32 v35, v134, v135\n"
"v_cvt_pk_bf16_f32 v36, v136, v137\n"
"v_cvt_pk_bf16_f32 v37, v138, v139\n"
"ds_bpermute_b32 v32, v126, v32\n"
"ds_bpermute_b32 v33, v126, v33\n"
"ds_bpermute_b32 v34, v126, v34\n"
"ds_bpermute_b32 v35, v126, v35\n"
"ds_bpermute_b32 v36, v126, v36\n"
"ds_bpermute_b32 v37, v126, v37\n"
"s_waitcnt lgkmcnt(2)\n"
"buffer_store_dwordx4 v[32:35], v124, s[28:31], s46 offen offset:0\n"
"s_waitcnt lgkmcnt(0)\n"
"buffer_store_dwordx2 v[36:37], v125, s[28:31], s46 offen offset:128\n"
"s_branch .Lk_bhe_35\n"
".Lk_late_34:\n"
"s_cmp_lg_u32 s3, 0\n"
"s_cbranch_scc1 .Lk_nopoll_59\n"
"buffer_load_dwordx2 v[16:17], v20, s[4:7], s21 offen sc1\n"
"buffer_load_dwordx2 v[18:19], v21, s[4:7], s21 offen sc1\n"
"s_waitcnt vmcnt(1)\n"
"v_cmp_ne_u64 vcc, s[32:33], v[16:17]\n"
"s_waitcnt vmcnt(0)\n"
"v_cmp_ne_u64 s[38:39], s[32:33], v[18:19]\n"
"s_nop 0\n"
"s_or_b64 vcc, vcc, s[38:39]\n"
"s_cmp_eq_u64 vcc, 0\n"
"s_cbranch_scc1 .Lk_polld_61\n"
".Lk_poll_60:\n"
"buffer_load_dwordx2 v[16:17], v20, s[4:7], s21 offen sc1\n"
"buffer_load_dwordx2 v[18:19], v21, s[4:7], s21 offen sc1\n"
"s_waitcnt vmcnt(0)\n"
"v_cmp_ne_u64 vcc, s[32:33], v[16:17]\n"
"v_cmp_ne_u64 s[38:39], s[32:33], v[18:19]\n"
"s_nop 0\n"
"s_or_b64 vcc, vcc, s[38:39]\n"
"s_cmp_eq_u64 vcc, 0\n"
"s_cbranch_scc1 .Lk_polld_61\n"
"s_sub_u32 s37, s37, 1\n"
"s_cmp_gt_i32 s37, 0\n"
"s_cbranch_scc1 .Lk_poll_60\n"
".Lk_polld_61:\n"
".Lk_nopoll_59:\n"
"s_barrier\n"
"s_mov_b32 m0, s17\n"
"s_nop 0\n"
"buffer_load_dwordx4 v14, s[4:7], s16 offen sc1 lds\n"
"s_add_u32 s16, s16, 0x2000\n"
"s_add_u32 s17, s17, 0x2000\n"
"buffer_load_dwordx4 v[64:67], v6, s[4:7], s20 offen sc1\n"
"buffer_load_dwordx2 v[68:69], v7, s[4:7], s20 offen sc1\n"
"s_mov_b32 m0, s17\n"
"s_nop 0\n"
"buffer_load_dwordx4 v14, s[4:7], s16 offen sc1 lds\n"
"s_add_u32 s16, s16, 0x2000\n"
"s_add_u32 s17, s17, 0x2000\n"
"v_cmp_gt_u32 vcc, 32, v0\n"
"s_waitcnt vmcnt(1)\n"
"s_nop 0\n"
"v_cndmask_b32 v68, 0, v68, vcc\n"
"v_cndmask_b32 v69, 0, v69, vcc\n"
"s_waitcnt vmcnt(0)\n"
"s_barrier\n"
"ds_read_b64 v[70:71], v11\n"
"ds_read_b128 v[72:75], v10\n"
"ds_read_b64 v[8:9], v11 offset:256\n"
"ds_read_b128 v[76:79], v10 offset:512\n"
"s_nop 0\n"
"s_setprio 2\n"
"s_mov_b64 exec, s[18:19]\n"
"ds_read_b128 v[80:83], v12\n"
"ds_read_b128 v[84:87], v12 offset:640\n"
"s_mov_b64 exec, -1\n"
"s_waitcnt lgkmcnt(5)\n"
"v_mfma_f32_32x32x8_bf16 v[16:31], v[70:71], v[68:69], 0\n"
"ds_read_b64 v[70:71], v11 offset:512\n"
"s_mov_b32 m0, s17\n"
"s_nop 0\n"
"buffer_load_dwordx4 v14, s[4:7], s16 offen sc1 lds\n"
"s_add_u32 s16, s16, 0x2000\n"
"s_add_u32 s17, s17, 0x2000\n"
"s_waitcnt lgkmcnt(5)\n"
"v_mfma_f32_32x32x16_bf16 v[16:31], v[72:75], v[64:67], v[16:31]\n"
"ds_read_b128 v[72:75], v10 offset:1024\n"
"s_mov_b64 exec, s[18:19]\n"
"ds_read_b128 v[88:91], v12 offset:1280\n"
"ds_read_b128 v[92:95], v12 offset:1920\n"
"s_mov_b64 exec, -1\n"
"s_waitcnt lgkmcnt(7)\n"
"v_mfma_f32_32x32x8_bf16 v[32:47], v[8:9], v[68:69], 0\n"
"ds_read_b64 v[8:9], v11 offset:768\n"
"s_mov_b32 m0, s17\n"
"s_nop 0\n"
"buffer_load_dwordx4 v14, s[4:7], s16 offen sc1 lds\n"
"s_add_u32 s16, s16, 0x2000\n"
"s_add_u32 s17, s17, 0x2000\n"
"s_waitcnt lgkmcnt(7)\n"
"v_mfma_f32_32x32x16_bf16 v[32:47], v[76:79], v[64:67], v[32:47]\n"
"ds_read_b128 v[76:79], v10 offset:1536\n"
"s_mov_b32 m0, s17\n"
"s_nop 0\n"
"buffer_load_dwordx4 v14, s[4:7], s16 offen sc1 lds\n"
"s_add_u32 s16, s16, 0x2000\n"
"s_add_u32 s17, s17, 0x2000\n"
"s_setprio 1\n"
"v_exp_f32 v16, v16\n"
"v_exp_f32 v17, v17\n"
"v_exp_f32 v18, v18\n"
"v_exp_f32 v19, v19\n"
"v_exp_f32 v20, v20\n"
"v_exp_f32 v21, v21\n"
"v_exp_f32 v22, v22\n"
"v_exp_f32 v23, v23\n"
"v_exp_f32 v24, v24\n"
"v_exp_f32 v25, v25\n"
"v_exp_f32 v26, v26\n"
"v_exp_f32 v27, v27\n"
"v_exp_f32 v28, v28\n"
"v_exp_f32 v29, v29\n"
"v_exp_f32 v30, v30\n"
"v_exp_f32 v31, v31\n"
"v_exp_f32 v32, v32\n"
"v_exp_f32 v33, v33\n"
"v_exp_f32 v34, v34\n"
"v_exp_f32 v35, v35\n"
"v_exp_f32 v36, v36\n"
"v_exp_f32 v37, v37\n"
"v_exp_f32 v38, v38\n"
"v_exp_f32 v39, v39\n"
"v_exp_f32 v40, v40\n"
"v_exp_f32 v41, v41\n"
"v_exp_f32 v42, v42\n"
"v_exp_f32 v43, v43\n"
"v_exp_f32 v44, v44\n"
"v_exp_f32 v45, v45\n"
"v_exp_f32 v46, v46\n"
"v_exp_f32 v47, v47\n"
"s_setprio 2\n"
"s_waitcnt lgkmcnt(0)\n"
"v_smfmac_f32_32x32x32_bf16 v[48:63], v[80:83], v[16:23], v13\n"
"s_add_u32 m0, s42, 0\n"
"s_nop 0\n"
"buffer_load_dwordx4 v205, s[52:55], s43 offen lds\n"
"v_smfmac_f32_32x32x32_bf16 v[48:63], v[84:87], v[24:31], v13\n"
"s_mov_b64 exec, s[18:19]\n"
"ds_read_b128 v[80:83], v12 offset:2560\n"
"ds_read_b128 v[84:87], v12 offset:3200\n"
"s_mov_b64 exec, -1\n"
"v_mfma_f32_32x32x8_bf16 v[16:31], v[70:71], v[68:69], 0\n"
"ds_read_b64 v[70:71], v11 offset:10240\n"
"s_add_u32 m0, s42, 1024\n"
"s_add_u32 s43, s43, s49\n"
"buffer_load_dwordx4 v205, s[52:55], s43 offen lds\n"
"v_mfma_f32_32x32x16_bf16 v[16:31], v[72:75], v[64:67], v[16:31]\n"
"ds_read_b128 v[72:75], v10 offset:10240\n"
"v_smfmac_f32_32x32x32_bf16 v[48:63], v[88:91], v[32:39], v13\n"
"s_add_u32 m0, s42, 2048\n"
"s_add_u32 s43, s43, s49\n"
"buffer_load_dwordx4 v205, s[52:55], s43 offen lds\n"
"v_smfmac_f32_32x32x32_bf16 v[48:63], v[92:95], v[40:47], v13\n"
"s_mov_b64 exec, s[18:19]\n"
"ds_read_b128 v[88:91], v12 offset:3840\n"
"ds_read_b128 v[92:95], v12 offset:4480\n"
"s_mov_b64 exec, -1\n"
"v_mfma_f32_32x32x8_bf16 v[32:47], v[8:9], v[68:69], 0\n"
"ds_read_b64 v[8:9], v11 offset:10496\n"
"v_mfma_f32_32x32x16_bf16 v[32:47], v[76:79], v[64:67], v[32:47]\n"
"ds_read_b128 v[76:79], v10 offset:10752\n"
"s_waitcnt vmcnt(3)\n"
"s_barrier\n"
"s_setprio 1\n"
"v_exp_f32 v16, v16\n"
"v_exp_f32 v17, v17\n"
"v_exp_f32 v18, v18\n"
"v_exp_f32 v19, v19\n"
"v_exp_f32 v20, v20\n"
"v_exp_f32 v21, v21\n"
"v_exp_f32 v22, v22\n"
"v_exp_f32 v23, v23\n"
"v_exp_f32 v24, v24\n"
"v_exp_f32 v25, v25\n"
"v_exp_f32 v26, v26\n"
"v_exp_f32 v27, v27\n"
"v_exp_f32 v28, v28\n"
"v_exp_f32 v29, v29\n"
"v_exp_f32 v30, v30\n"
"v_exp_f32 v31, v31\n"
"v_exp_f32 v32, v32\n"
"v_exp_f32 v33, v33\n"
"v_exp_f32 v34, v34\n"
"v_exp_f32 v35, v35\n"
"v_exp_f32 v36, v36\n"
"v_exp_f32 v37, v37\n"
"v_exp_f32 v38, v38\n"
"v_exp_f32 v39, v39\n"
"v_exp_f32 v40, v40\n"
"v_exp_f32 v41, v41\n"
"v_exp_f32 v42, v42\n"
"v_exp_f32 v43, v43\n"
"v_exp_f32 v44, v44\n"
"v_exp_f32 v45, v45\n"
"v_exp_f32 v46, v46\n"
"v_exp_f32 v47, v47\n"
"s_setprio 2\n"
"s_waitcnt lgkmcnt(0)\n"
"v_smfmac_f32_32x32x32_bf16 v[48:63], v[80:83], v[16:23], v13\n"
"s_add_u32 m0, s42, 3072\n"
"s_add_u32 s43, s43, s49\n"
"buffer_load_dwordx4 v205, s[52:55], s43 offen lds\n"
"v_smfmac_f32_32x32x32_bf16 v[48:63], v[84:87], v[24:31], v13\n"
"s_mov_b64 exec, s[18:19]\n"
"ds_read_b128 v[80:83], v12 offset:10240\n"
"ds_read_b128 v[84:87], v12 offset:10880\n"
"s_mov_b64 exec, -1\n"
"v_mfma_f32_32x32x8_bf16 v[16:31], v[70:71], v[68:69], 0\n"
"ds_read_b64 v[70:71], v11 offset:10752\n"
"s_add_u32 m0, s42, 4096\n"
"s_add_u32 s43, s43, s49\n"
"buffer_load_dwordx4 v205, s[52:55], s43 offen lds\n"
"v_mfma_f32_32x32x16_bf16 v[16:31], v[72:75], v[64:67], v[16:31]\n"
"ds_read_b128 v[72:75], v10 offset:11264\n"
"v_smfmac_f32_32x32x32_bf16 v[48:63], v[88:91], v[32:39], v13\n"
"v_smfmac_f32_32x32x32_bf16 v[48:63], v[92:95], v[40:47], v13\n"
"s_mov_b64 exec, s[18:19]\n"
"ds_read_b128 v[88:91], v12 offset:11520\n"
"ds_read_b128 v[92:95], v12 offset:12160\n"
"s_mov_b64 exec, -1\n"
"v_mfma_f32_32x32x8_bf16 v[32:47], v[8:9], v[68:69], 0\n"
"ds_read_b64 v[8:9], v11 offset:11008\n"
"v_mfma_f32_32x32x16_bf16 v[32:47], v[76:79], v[64:67], v[32:47]\n"
"ds_read_b128 v[76:79], v10 offset:11776\n"
"s_setprio 1\n"
"v_exp_f32 v16, v16\n"
"v_exp_f32 v17, v17\n"
"v_exp_f32 v18, v18\n"
"v_exp_f32 v19, v19\n"
"v_exp_f32 v20, v20\n"
"v_exp_f32 v21, v21\n"
"v_exp_f32 v22, v22\n"
"v_exp_f32 v23, v23\n"
"v_exp_f32 v24, v24\n"
"v_exp_f32 v25, v25\n"
"v_exp_f32 v26, v26\n"
"v_exp_f32 v27, v27\n"
"v_exp_f32 v28, v28\n"
"v_exp_f32 v29, v29\n"
"v_exp_f32 v30, v30\n"
"v_exp_f32 v31, v31\n"
"v_exp_f32 v32, v32\n"
"v_exp_f32 v33, v33\n"
"v_exp_f32 v34, v34\n"
"v_exp_f32 v35, v35\n"
"v_exp_f32 v36, v36\n"
"v_exp_f32 v37, v37\n"
"v_exp_f32 v38, v38\n"
"v_exp_f32 v39, v39\n"
"v_exp_f32 v40, v40\n"
"v_exp_f32 v41, v41\n"
"v_exp_f32 v42, v42\n"
"v_exp_f32 v43, v43\n"
"v_exp_f32 v44, v44\n"
"v_exp_f32 v45, v45\n"
"v_exp_f32 v46, v46\n"
"v_exp_f32 v47, v47\n"
"s_setprio 2\n"
"s_waitcnt lgkmcnt(0)\n"
"v_smfmac_f32_32x32x32_bf16 v[48:63], v[80:83], v[16:23], v13\n"
"s_lshl_b32 s56, s34, 3\n"
"s_add_u32 s57, s56, s63\n"
"s_cbranch_execz .Lk_tramp\n"
"s_nop 0\n"
"s_lshr_b32 s58, s57, 1\n"
"s_min_u32 s59, s58, 26\n"
"s_lshl_b32 s60, s59, 2\n"
"s_lshl_b32 s61, s60, 10\n"
"s_add_u32 s21, 0x41000, s61\n"
"s_lshr_b32 s56, s63, 2\n"
"s_lshl_b32 s57, s56, 10\n"
"s_add_u32 s38, 0x5d800, s57\n"
"s_cmp_eq_u32 s34, 7\n"
"s_cselect_b32 s21, s38, s21\n"
"s_cselect_b32 s38, 12, 3\n"
"s_cselect_b32 s39, 13, 10\n"
"v_min_u32 v15, s38, v0\n"
"v_lshlrev_b32 v15, s39, v15\n"
"v_smfmac_f32_32x32x32_bf16 v[48:63], v[84:87], v[24:31], v13\n"
"s_mov_b64 exec, s[18:19]\n"
"ds_read_b128 v[80:83], v12 offset:12800\n"
"ds_read_b128 v[84:87], v12 offset:13440\n"
"s_mov_b64 exec, -1\n"
"v_mfma_f32_32x32x8_bf16 v[16:31], v[70:71], v[68:69], 0\n"
"ds_read_b64 v[70:71], v11 offset:20480\n"
"v_mfma_f32_32x32x16_bf16 v[16:31], v[72:75], v[64:67], v[16:31]\n"
"ds_read_b128 v[72:75], v10 offset:20480\n"
"v_smfmac_f32_32x32x32_bf16 v[48:63], v[88:91], v[32:39], v13\n"
"v_smfmac_f32_32x32x32_bf16 v[48:63], v[92:95], v[40:47], v13\n"
"s_mov_b64 exec, s[18:19]\n"
"ds_read_b128 v[88:91], v12 offset:14080\n"
"ds_read_b128 v[92:95], v12 offset:14720\n"
"s_mov_b64 exec, -1\n"
"v_mfma_f32_32x32x8_bf16 v[32:47], v[8:9], v[68:69], 0\n"
"ds_read_b64 v[8:9], v11 offset:20736\n"
"v_mfma_f32_32x32x16_bf16 v[32:47], v[76:79], v[64:67], v[32:47]\n"
"ds_read_b128 v[76:79], v10 offset:20992\n"
"s_setprio 1\n"
"v_exp_f32 v16, v16\n"
"v_exp_f32 v17, v17\n"
"v_exp_f32 v18, v18\n"
"v_exp_f32 v19, v19\n"
"v_exp_f32 v20, v20\n"
"v_exp_f32 v21, v21\n"
"v_exp_f32 v22, v22\n"
"v_exp_f32 v23, v23\n"
"v_exp_f32 v24, v24\n"
"v_exp_f32 v25, v25\n"
"v_exp_f32 v26, v26\n"
"v_exp_f32 v27, v27\n"
"v_exp_f32 v28, v28\n"
"v_exp_f32 v29, v29\n"
"v_exp_f32 v30, v30\n"
"v_exp_f32 v31, v31\n"
"v_exp_f32 v32, v32\n"
"v_exp_f32 v33, v33\n"
"v_exp_f32 v34, v34\n"
"v_exp_f32 v35, v35\n"
"v_exp_f32 v36, v36\n"
"v_exp_f32 v37, v37\n"
"v_exp_f32 v38, v38\n"
"v_exp_f32 v39, v39\n"
"v_exp_f32 v40, v40\n"
"v_exp_f32 v41, v41\n"
"v_exp_f32 v42, v42\n"
"v_exp_f32 v43, v43\n"
"v_exp_f32 v44, v44\n"
"v_exp_f32 v45, v45\n"
"v_exp_f32 v46, v46\n"
"v_exp_f32 v47, v47\n"
"s_setprio 2\n"
"s_waitcnt lgkmcnt(0)\n"
"v_smfmac_f32_32x32x32_bf16 v[48:63], v[80:83], v[16:23], v13\n"
"s_mov_b32 m0, s17\n"
"s_nop 0\n"
"buffer_load_dwordx4 v14, s[4:7], s16 offen sc1 lds\n"
"s_add_u32 s16, s16, 0x2000\n"
"s_add_u32 s17, s17, 0x2000\n"
"v_smfmac_f32_32x32x32_bf16 v[48:63], v[84:87], v[24:31], v13\n"
"s_mov_b64 exec, s[18:19]\n"
"ds_read_b128 v[80:83], v12 offset:20480\n"
"ds_read_b128 v[84:87], v12 offset:21120\n"
"s_mov_b64 exec, -1\n"
"v_mfma_f32_32x32x8_bf16 v[16:31], v[70:71], v[68:69], 0\n"
"ds_read_b64 v[70:71], v11 offset:20992\n"
"s_mov_b32 m0, s17\n"
"s_nop 0\n"
"buffer_load_dwordx4 v14, s[4:7], s16 offen sc1 lds\n"
"s_add_u32 s16, s16, 0x2000\n"
"s_add_u32 s17, s17, 0x2000\n"
"v_mfma_f32_32x32x16_bf16 v[16:31], v[72:75], v[64:67], v[16:31]\n"
"ds_read_b128 v[72:75], v10 offset:21504\n"
"v_smfmac_f32_32x32x32_bf16 v[48:63], v[88:91], v[32:39], v13\n"
"s_mov_b32 m0, s17\n"
"s_nop 0\n"
"buffer_load_dwordx4 v14, s[4:7], s16 offen sc1 lds\n"
"s_add_u32 s16, s16, 0x2000\n"
"s_add_u32 s17, s17, 0x2000\n"
"v_smfmac_f32_32x32x32_bf16 v[48:63], v[92:95], v[40:47], v13\n"
"s_mov_b64 exec, s[18:19]\n"
"ds_read_b128 v[88:91], v12 offset:21760\n"
"ds_read_b128 v[92:95], v12 offset:22400\n"
"s_mov_b64 exec, -1\n"
"v_mfma_f32_32x32x8_bf16 v[32:47], v[8:9], v[68:69], 0\n"
"ds_read_b64 v[8:9], v11 offset:21248\n"
"v_mfma_f32_32x32x16_bf16 v[32:47], v[76:79], v[64:67], v[32:47]\n"
"ds_read_b128 v[76:79], v10 offset:22016\n"
"s_setprio 1\n"
"v_exp_f32 v16, v16\n"
"v_exp_f32 v17, v17\n"
"v_exp_f32 v18, v18\n"
"v_exp_f32 v19, v19\n"
"v_exp_f32 v20, v20\n"
"v_exp_f32 v21, v21\n"
"v_exp_f32 v22, v22\n"
"v_exp_f32 v23, v23\n"
"v_exp_f32 v24, v24\n"
"v_exp_f32 v25, v25\n"
"v_exp_f32 v26, v26\n"
"v_exp_f32 v27, v27\n"
"v_exp_f32 v28, v28\n"
"v_exp_f32 v29, v29\n"
"v_exp_f32 v30, v30\n"
"v_exp_f32 v31, v31\n"
"v_exp_f32 v32, v32\n"
"v_exp_f32 v33, v33\n"
"v_exp_f32 v34, v34\n"
"v_exp_f32 v35, v35\n"
"v_exp_f32 v36, v36\n"
"v_exp_f32 v37, v37\n"
"v_exp_f32 v38, v38\n"
"v_exp_f32 v39, v39\n"
"v_exp_f32 v40, v40\n"
"v_exp_f32 v41, v41\n"
"v_exp_f32 v42, v42\n"
"v_exp_f32 v43, v43\n"
"v_exp_f32 v44, v44\n"
"v_exp_f32 v45, v45\n"
"v_exp_f32 v46, v46\n"
"v_exp_f32 v47, v47\n"
"s_setprio 2\n"
"s_waitcnt lgkmcnt(0)\n"
"v_smfmac_f32_32x32x32_bf16 v[48:63], v[80:83], v[16:23], v13\n"
"s_mov_b32 m0, s17\n"
"s_nop 0\n"
"buffer_load_dwordx4 v14, s[4:7], s16 offen sc1 lds\n"
"s_add_u32 s16, s16, 0x2000\n"
"s_add_u32 s17, s17, 0x2000\n"
"v_smfmac_f32_32x32x32_bf16 v[48:63], v[84:87], v[24:31], v13\n"
"s_mov_b64 exec, s[18:19]\n"
"ds_read_b128 v[80:83], v12 offset:23040\n"
"ds_read_b128 v[84:87], v12 offset:23680\n"
"s_mov_b64 exec, -1\n"
"v_mfma_f32_32x32x8_bf16 v[16:31], v[70:71], v[68:69], 0\n"
"ds_read_b64 v[70:71], v11 offset:30720\n"
"s_mov_b32 m0, s17\n"
"s_nop 0\n"
"buffer_load_dwordx4 v14, s[4:7], s16 offen sc1 lds\n"
"s_add_u32 s16, s16, 0x2000\n"
"s_add_u32 s17, s17, 0x2000\n"
"v_mfma_f32_32x32x16_bf16 v[16:31], v[72:75], v[64:67], v[16:31]\n"
"ds_read_b128 v[72:75], v10 offset:30720\n"
"v_smfmac_f32_32x32x32_bf16 v[48:63], v[88:91], v[32:39], v13\n"
"s_mov_b32 m0, s17\n"
"s_nop 0\n"
"buffer_load_dwordx4 v14, s[4:7], s16 offen sc1 lds\n"
"s_add_u32 s16, s16, 0x2000\n"
"s_add_u32 s17, s17, 0x2000\n"
"v_smfmac_f32_32x32x32_bf16 v[48:63], v[92:95], v[40:47], v13\n"
"s_mov_b64 exec, s[18:19]\n"
"ds_read_b128 v[88:91], v12 offset:24320\n"
"ds_read_b128 v[92:95], v12 offset:24960\n"
"s_mov_b64 exec, -1\n"
"v_mfma_f32_32x32x8_bf16 v[32:47], v[8:9], v[68:69], 0\n"
"ds_read_b64 v[8:9], v11 offset:30976\n"
"v_mfma_f32_32x32x16_bf16 v[32:47], v[76:79], v[64:67], v[32:47]\n"
"ds_read_b128 v[76:79], v10 offset:31232\n"
"s_setprio 1\n"
"v_exp_f32 v16, v16\n"
"v_exp_f32 v17, v17\n"
"v_exp_f32 v18, v18\n"
"v_exp_f32 v19, v19\n"
"v_exp_f32 v20, v20\n"
"v_exp_f32 v21, v21\n"
"v_exp_f32 v22, v22\n"
"v_exp_f32 v23, v23\n"
"v_exp_f32 v24, v24\n"
"v_exp_f32 v25, v25\n"
"v_exp_f32 v26, v26\n"
"v_exp_f32 v27, v27\n"
"v_exp_f32 v28, v28\n"
"v_exp_f32 v29, v29\n"
"v_exp_f32 v30, v30\n"
"v_exp_f32 v31, v31\n"
"v_exp_f32 v32, v32\n"
"v_exp_f32 v33, v33\n"
"v_exp_f32 v34, v34\n"
"v_exp_f32 v35, v35\n"
"v_exp_f32 v36, v36\n"
"v_exp_f32 v37, v37\n"
"v_exp_f32 v38, v38\n"
"v_exp_f32 v39, v39\n"
"v_exp_f32 v40, v40\n"
"v_exp_f32 v41, v41\n"
"v_exp_f32 v42, v42\n"
"v_exp_f32 v43, v43\n"
"v_exp_f32 v44, v44\n"
"v_exp_f32 v45, v45\n"
"v_exp_f32 v46, v46\n"
"v_exp_f32 v47, v47\n"
"s_setprio 2\n"
"s_waitcnt lgkmcnt(0)\n"
"v_smfmac_f32_32x32x32_bf16 v[48:63], v[80:83], v[16:23], v13\n"
"v_smfmac_f32_32x32x32_bf16 v[48:63], v[84:87], v[24:31], v13\n"
"s_mov_b64 exec, s[18:19]\n"
"ds_read_b128 v[80:83], v12 offset:30720\n"
"ds_read_b128 v[84:87], v12 offset:31360\n"
"s_mov_b64 exec, -1\n"
"v_mfma_f32_32x32x8_bf16 v[16:31], v[70:71], v[68:69], 0\n"
"ds_read_b64 v[70:71], v11 offset:31232\n"
"v_mfma_f32_32x32x16_bf16 v[16:31], v[72:75], v[64:67], v[16:31]\n"
"ds_read_b128 v[72:75], v10 offset:31744\n"
"v_smfmac_f32_32x32x32_bf16 v[48:63], v[88:91], v[32:39], v13\n"
"v_smfmac_f32_32x32x32_bf16 v[48:63], v[92:95], v[40:47], v13\n"
"s_mov_b64 exec, s[18:19]\n"
"ds_read_b128 v[88:91], v12 offset:32000\n"
"ds_read_b128 v[92:95], v12 offset:32640\n"
"s_mov_b64 exec, -1\n"
"v_mfma_f32_32x32x8_bf16 v[32:47], v[8:9], v[68:69], 0\n"
"ds_read_b64 v[8:9], v11 offset:31488\n"
"v_mfma_f32_32x32x16_bf16 v[32:47], v[76:79], v[64:67], v[32:47]\n"
"ds_read_b128 v[76:79], v10 offset:32256\n"
"s_waitcnt vmcnt(0)\n"
"s_barrier\n"
"s_setprio 1\n"
"v_exp_f32 v16, v16\n"
"v_exp_f32 v17, v17\n"
"v_exp_f32 v18, v18\n"
"v_exp_f32 v19, v19\n"
"v_exp_f32 v20, v20\n"
"v_exp_f32 v21, v21\n"
"v_exp_f32 v22, v22\n"
"v_exp_f32 v23, v23\n"
"v_exp_f32 v24, v24\n"
"v_exp_f32 v25, v25\n"
"v_exp_f32 v26, v26\n"
"v_exp_f32 v27, v27\n"
"v_exp_f32 v28, v28\n"
"v_exp_f32 v29, v29\n"
"v_exp_f32 v30, v30\n"
"v_exp_f32 v31, v31\n"
"v_exp_f32 v32, v32\n"
"v_exp_f32 v33, v33\n"
"v_exp_f32 v34, v34\n"
"v_exp_f32 v35, v35\n"
"v_exp_f32 v36, v36\n"
"v_exp_f32 v37, v37\n"
"v_exp_f32 v38, v38\n"
"v_exp_f32 v39, v39\n"
"v_exp_f32 v40, v40\n"
"v_exp_f32 v41, v41\n"
"v_exp_f32 v42, v42\n"
"v_exp_f32 v43, v43\n"
"v_exp_f32 v44, v44\n"
"v_exp_f32 v45, v45\n"
"v_exp_f32 v46, v46\n"
"v_exp_f32 v47, v47\n"
"s_setprio 2\n"
"s_waitcnt lgkmcnt(0)\n"
"v_smfmac_f32_32x32x32_bf16 v[48:63], v[80:83], v[16:23], v13\n"
"ds_read_b64_tr_b16 v[212:213], v206\n"
"ds_read_b64_tr_b16 v[214:215], v206 offset:512\n"
"v_smfmac_f32_32x32x32_bf16 v[48:63], v[84:87], v[24:31], v13\n"
"s_mov_b64 exec, s[18:19]\n"
"ds_read_b128 v[80:83], v12 offset:33280\n"
"ds_read_b128 v[84:87], v12 offset:33920\n"
"s_mov_b64 exec, -1\n"
"v_mfma_f32_32x32x8_bf16 v[16:31], v[70:71], v[68:69], 0\n"
"ds_read_b64 v[70:71], v11 offset:40960\n"
"ds_read_b64_tr_b16 v[216:217], v207\n"
"ds_read_b64_tr_b16 v[218:219], v207 offset:512\n"
"v_mfma_f32_32x32x16_bf16 v[16:31], v[72:75], v[64:67], v[16:31]\n"
"ds_read_b128 v[72:75], v10 offset:40960\n"
"v_smfmac_f32_32x32x32_bf16 v[48:63], v[88:91], v[32:39], v13\n"
"ds_read_b64_tr_b16 v[220:221], v208\n"
"ds_read_b64_tr_b16 v[222:223], v208 offset:512\n"
"v_smfmac_f32_32x32x32_bf16 v[48:63], v[92:95], v[40:47], v13\n"
"s_mov_b64 exec, s[18:19]\n"
"ds_read_b128 v[88:91], v12 offset:34560\n"
"ds_read_b128 v[92:95], v12 offset:35200\n"
"s_mov_b64 exec, -1\n"
"v_mfma_f32_32x32x8_bf16 v[32:47], v[8:9], v[68:69], 0\n"
"ds_read_b64 v[8:9], v11 offset:41216\n"
"v_mfma_f32_32x32x16_bf16 v[32:47], v[76:79], v[64:67], v[32:47]\n"
"ds_read_b128 v[76:79], v10 offset:41472\n"
"s_setprio 1\n"
"v_exp_f32 v16, v16\n"
"v_exp_f32 v17, v17\n"
"v_exp_f32 v18, v18\n"
"v_exp_f32 v19, v19\n"
"v_exp_f32 v20, v20\n"
"v_exp_f32 v21, v21\n"
"v_exp_f32 v22, v22\n"
"v_exp_f32 v23, v23\n"
"v_exp_f32 v24, v24\n"
"v_exp_f32 v25, v25\n"
"v_exp_f32 v26, v26\n"
"v_exp_f32 v27, v27\n"
"v_exp_f32 v28, v28\n"
"v_exp_f32 v29, v29\n"
"v_exp_f32 v30, v30\n"
"v_exp_f32 v31, v31\n"
"v_exp_f32 v32, v32\n"
"v_exp_f32 v33, v33\n"
"v_exp_f32 v34, v34\n"
"v_exp_f32 v35, v35\n"
"v_exp_f32 v36, v36\n"
"v_exp_f32 v37, v37\n"
"v_exp_f32 v38, v38\n"
"v_exp_f32 v39, v39\n"
"v_exp_f32 v40, v40\n"
"v_exp_f32 v41, v41\n"
"v_exp_f32 v42, v42\n"
"v_exp_f32 v43, v43\n"
"v_exp_f32 v44, v44\n"
"v_exp_f32 v45, v45\n"
"v_exp_f32 v46, v46\n"
"v_exp_f32 v47, v47\n"
"s_setprio 2\n"
"s_waitcnt lgkmcnt(0)\n"
"v_smfmac_f32_32x32x32_bf16 v[48:63], v[80:83], v[16:23], v13\n"
"ds_read_b64_tr_b16 v[224:225], v209\n"
"ds_read_b64_tr_b16 v[226:227], v209 offset:512\n"
"v_smfmac_f32_32x32x32_bf16 v[48:63], v[84:87], v[24:31], v13\n"
"s_mov_b64 exec, s[18:19]\n"
"ds_read_b128 v[80:83], v12 offset:40960\n"
"ds_read_b128 v[84:87], v12 offset:41600\n"
"s_mov_b64 exec, -1\n"
"v_mfma_f32_32x32x8_bf16 v[16:31], v[70:71], v[68:69], 0\n"
"ds_read_b64 v[70:71], v11 offset:41472\n"
"buffer_store_dwordx4 v[212:215], v14, s[4:7], s44 offen sc1\n"
"v_mfma_f32_32x32x16_bf16 v[16:31], v[72:75], v[64:67], v[16:31]\n"
"ds_read_b128 v[72:75], v10 offset:41984\n"
"v_smfmac_f32_32x32x32_bf16 v[48:63], v[88:91], v[32:39], v13\n"
"ds_read_b64_tr_b16 v[228:229], v210\n"
"ds_read_b64_tr_b16 v[230:231], v210 offset:512\n"
"v_smfmac_f32_32x32x32_bf16 v[48:63], v[92:95], v[40:47], v13\n"
"s_mov_b64 exec, s[18:19]\n"
"ds_read_b128 v[88:91], v12 offset:42240\n"
"ds_read_b128 v[92:95], v12 offset:42880\n"
"s_mov_b64 exec, -1\n"
"v_mfma_f32_32x32x8_bf16 v[32:47], v[8:9], v[68:69], 0\n"
"ds_read_b64 v[8:9], v11 offset:41728\n"
"buffer_store_dwordx4 v[216:219], v14, s[4:7], s45 offen sc1\n"
"v_mfma_f32_32x32x16_bf16 v[32:47], v[76:79], v[64:67], v[32:47]\n"
"ds_read_b128 v[76:79], v10 offset:42496\n"
"s_setprio 1\n"
"v_exp_f32 v16, v16\n"
"v_exp_f32 v17, v17\n"
"v_exp_f32 v18, v18\n"
"v_exp_f32 v19, v19\n"
"v_exp_f32 v20, v20\n"
"v_exp_f32 v21, v21\n"
"v_exp_f32 v22, v22\n"
"v_exp_f32 v23, v23\n"
"v_exp_f32 v24, v24\n"
"v_exp_f32 v25, v25\n"
"v_exp_f32 v26, v26\n"
"v_exp_f32 v27, v27\n"
"v_exp_f32 v28, v28\n"
"v_exp_f32 v29, v29\n"
"v_exp_f32 v30, v30\n"
"v_exp_f32 v31, v31\n"
"v_exp_f32 v32, v32\n"
"v_exp_f32 v33, v33\n"
"v_exp_f32 v34, v34\n"
"v_exp_f32 v35, v35\n"
"v_exp_f32 v36, v36\n"
"v_exp_f32 v37, v37\n"
"v_exp_f32 v38, v38\n"
"v_exp_f32 v39, v39\n"
"v_exp_f32 v40, v40\n"
"v_exp_f32 v41, v41\n"
"v_exp_f32 v42, v42\n"
"v_exp_f32 v43, v43\n"
"v_exp_f32 v44, v44\n"
"v_exp_f32 v45, v45\n"
"v_exp_f32 v46, v46\n"
"v_exp_f32 v47, v47\n"
"s_setprio 2\n"
"s_waitcnt lgkmcnt(0)\n"
"v_smfmac_f32_32x32x32_bf16 v[48:63], v[80:83], v[16:23], v13\n"
"buffer_store_dwordx4 v[220:223], v14, s[4:7], s46 offen sc1\n"
"v_smfmac_f32_32x32x32_bf16 v[48:63], v[84:87], v[24:31], v13\n"
"s_mov_b64 exec, s[18:19]\n"
"ds_read_b128 v[80:83], v12 offset:43520\n"
"ds_read_b128 v[84:87], v12 offset:44160\n"
"s_mov_b64 exec, -1\n"
"v_mfma_f32_32x32x8_bf16 v[16:31], v[70:71], v[68:69], 0\n"
"ds_read_b64 v[70:71], v11 offset:51200\n"
"buffer_store_dwordx4 v[224:227], v14, s[4:7], s47 offen sc1\n"
"v_mfma_f32_32x32x16_bf16 v[16:31], v[72:75], v[64:67], v[16:31]\n"
"ds_read_b128 v[72:75], v10 offset:51200\n"
"v_smfmac_f32_32x32x32_bf16 v[48:63], v[88:91], v[32:39], v13\n"
"buffer_store_dwordx4 v[228:231], v14, s[4:7], s48 offen sc1\n"
"v_smfmac_f32_32x32x32_bf16 v[48:63], v[92:95], v[40:47], v13\n"
"s_mov_b64 exec, s[18:19]\n"
"ds_read_b128 v[88:91], v12 offset:44800\n"
"ds_read_b128 v[92:95], v12 offset:45440\n"
"s_mov_b64 exec, -1\n"
"v_mfma_f32_32x32x8_bf16 v[32:47], v[8:9], v[68:69], 0\n"
"ds_read_b64 v[8:9], v11 offset:51456\n"
"v_mfma_f32_32x32x16_bf16 v[32:47], v[76:79], v[64:67], v[32:47]\n"
"ds_read_b128 v[76:79], v10 offset:51712\n"
"s_setprio 1\n"
"v_exp_f32 v16, v16\n"
"v_exp_f32 v17, v17\n"
"v_exp_f32 v18, v18\n"
"v_exp_f32 v19, v19\n"
"v_exp_f32 v20, v20\n"
"v_exp_f32 v21, v21\n"
"v_exp_f32 v22, v22\n"
"v_exp_f32 v23, v23\n"
"v_exp_f32 v24, v24\n"
"v_exp_f32 v25, v25\n"
"v_exp_f32 v26, v26\n"
"v_exp_f32 v27, v27\n"
"v_exp_f32 v28, v28\n"
"v_exp_f32 v29, v29\n"
"v_exp_f32 v30, v30\n"
"v_exp_f32 v31, v31\n"
"v_exp_f32 v32, v32\n"
"v_exp_f32 v33, v33\n"
"v_exp_f32 v34, v34\n"
"v_exp_f32 v35, v35\n"
"v_exp_f32 v36, v36\n"
"v_exp_f32 v37, v37\n"
"v_exp_f32 v38, v38\n"
"v_exp_f32 v39, v39\n"
"v_exp_f32 v40, v40\n"
"v_exp_f32 v41, v41\n"
"v_exp_f32 v42, v42\n"
"v_exp_f32 v43, v43\n"
"v_exp_f32 v44, v44\n"
"v_exp_f32 v45, v45\n"
"v_exp_f32 v46, v46\n"
"v_exp_f32 v47, v47\n"
"s_setprio 2\n"
"s_waitcnt lgkmcnt(0)\n"
"v_add_u32 v10, 0xc800, v10\n"
"v_add_u32 v11, 0xc800, v11\n"
"v_add_u32 v12, 0xc800, v12\n"
"v_smfmac_f32_32x32x32_bf16 v[48:63], v[80:83], v[16:23], v13\n"
"s_mov_b32 m0, s17\n"
"s_nop 0\n"
"buffer_load_dwordx4 v14, s[4:7], s16 offen sc1 lds\n"
"s_add_u32 s16, s16, 0x2000\n"
"s_add_u32 s17, s17, 0x2000\n"
"v_smfmac_f32_32x32x32_bf16 v[48:63], v[84:87], v[24:31], v13\n"
"s_mov_b64 exec, s[18:19]\n"
"ds_read_b128 v[80:83], v12\n"
"ds_read_b128 v[84:87], v12 offset:640\n"
"s_mov_b64 exec, -1\n"
"v_mfma_f32_32x32x8_bf16 v[16:31], v[70:71], v[68:69], 0\n"
"ds_read_b64 v[70:71], v11 offset:512\n"
"s_mov_b32 m0, s17\n"
"s_nop 0\n"
"buffer_load_dwordx4 v14, s[4:7], s16 offen sc1 lds\n"
"s_add_u32 s16, s16, 0x2000\n"
"s_add_u32 s17, s17, 0x2000\n"
"v_mfma_f32_32x32x16_bf16 v[16:31], v[72:75], v[64:67], v[16:31]\n"
"ds_read_b128 v[72:75], v10 offset:1024\n"
"v_smfmac_f32_32x32x32_bf16 v[48:63], v[88:91], v[32:39], v13\n"
"s_mov_b32 m0, s17\n"
"s_nop 0\n"
"buffer_load_dwordx4 v14, s[4:7], s16 offen sc1 lds\n"
"s_add_u32 s16, s16, 0x2000\n"
"s_add_u32 s17, s17, 0x2000\n"
"v_smfmac_f32_32x32x32_bf16 v[48:63], v[92:95], v[40:47], v13\n"
"s_mov_b64 exec, s[18:19]\n"
"ds_read_b128 v[88:91], v12 offset:1280\n"
"ds_read_b128 v[92:95], v12 offset:1920\n"
"s_mov_b64 exec, -1\n"
"v_mfma_f32_32x32x8_bf16 v[32:47], v[8:9], v[68:69], 0\n"
"ds_read_b64 v[8:9], v11 offset:768\n"
"v_mfma_f32_32x32x16_bf16 v[32:47], v[76:79], v[64:67], v[32:47]\n"
"ds_read_b128 v[76:79], v10 offset:1536\n"
"s_setprio 1\n"
"v_exp_f32 v16, v16\n"
"v_exp_f32 v17, v17\n"
"v_exp_f32 v18, v18\n"
"v_exp_f32 v19, v19\n"
"v_exp_f32 v20, v20\n"
"v_exp_f32 v21, v21\n"
"v_exp_f32 v22, v22\n"
"v_exp_f32 v23, v23\n"
"v_exp_f32 v24, v24\n"
"v_exp_f32 v25, v25\n"
"v_exp_f32 v26, v26\n"
"v_exp_f32 v27, v27\n"
"v_exp_f32 v28, v28\n"
"v_exp_f32 v29, v29\n"
"v_exp_f32 v30, v30\n"
"v_exp_f32 v31, v31\n"
"v_exp_f32 v32, v32\n"
"v_exp_f32 v33, v33\n"
"v_exp_f32 v34, v34\n"
"v_exp_f32 v35, v35\n"
"v_exp_f32 v36, v36\n"
"v_exp_f32 v37, v37\n"
"v_exp_f32 v38, v38\n"
"v_exp_f32 v39, v39\n"
"v_exp_f32 v40, v40\n"
"v_exp_f32 v41, v41\n"
"v_exp_f32 v42, v42\n"
"v_exp_f32 v43, v43\n"
"v_exp_f32 v44, v44\n"
"v_exp_f32 v45, v45\n"
"v_exp_f32 v46, v46\n"
"v_exp_f32 v47, v47\n"
"s_setprio 2\n"
"s_waitcnt lgkmcnt(0)\n"
"v_smfmac_f32_32x32x32_bf16 v[48:63], v[80:83], v[16:23], v13\n"
"s_mov_b32 m0, s17\n"
"s_nop 0\n"
"buffer_load_dwordx4 v14, s[4:7], s16 offen sc1 lds\n"
"s_add_u32 s16, s16, 0x2000\n"
"s_add_u32 s17, s17, 0x2000\n"
"v_smfmac_f32_32x32x32_bf16 v[48:63], v[84:87], v[24:31], v13\n"
"s_mov_b64 exec, s[18:19]\n"
"ds_read_b128 v[80:83], v12 offset:2560\n"
"ds_read_b128 v[84:87], v12 offset:3200\n"
"s_mov_b64 exec, -1\n"
"v_mfma_f32_32x32x8_bf16 v[16:31], v[70:71], v[68:69], 0\n"
"ds_read_b64 v[70:71], v11 offset:10240\n"
"s_mov_b32 m0, s17\n"
"s_nop 0\n"
"buffer_load_dwordx4 v14, s[4:7], s16 offen sc1 lds\n"
"s_add_u32 s16, s16, 0x2000\n"
"s_add_u32 s17, s17, 0x2000\n"
"v_mfma_f32_32x32x16_bf16 v[16:31], v[72:75], v[64:67], v[16:31]\n"
"ds_read_b128 v[72:75], v10 offset:10240\n"
"v_smfmac_f32_32x32x32_bf16 v[48:63], v[88:91], v[32:39], v13\n"
"s_mov_b32 m0, s17\n"
"s_nop 0\n"
"buffer_load_dwordx4 v14, s[4:7], s16 offen sc1 lds\n"
"s_add_u32 s16, s16, 0x2000\n"
"s_add_u32 s17, s17, 0x2000\n"
"v_smfmac_f32_32x32x32_bf16 v[48:63], v[92:95], v[40:47], v13\n"
"s_mov_b64 exec, s[18:19]\n"
"ds_read_b128 v[88:91], v12 offset:3840\n"
"ds_read_b128 v[92:95], v12 offset:4480\n"
"s_mov_b64 exec, -1\n"
"v_mfma_f32_32x32x8_bf16 v[32:47], v[8:9], v[68:69], 0\n"
"ds_read_b64 v[8:9], v11 offset:10496\n"
"v_mfma_f32_32x32x16_bf16 v[32:47], v[76:79], v[64:67], v[32:47]\n"
"ds_read_b128 v[76:79], v10 offset:10752\n"
"s_waitcnt vmcnt(6)\n"
"s_barrier\n"
"s_setprio 1\n"
"v_exp_f32 v16, v16\n"
"v_exp_f32 v17, v17\n"
"v_exp_f32 v18, v18\n"
"v_exp_f32 v19, v19\n"
"v_exp_f32 v20, v20\n"
"v_exp_f32 v21, v21\n"
"v_exp_f32 v22, v22\n"
"v_exp_f32 v23, v23\n"
"v_exp_f32 v24, v24\n"
"v_exp_f32 v25, v25\n"
"v_exp_f32 v26, v26\n"
"v_exp_f32 v27, v27\n"
"v_exp_f32 v28, v28\n"
"v_exp_f32 v29, v29\n"
"v_exp_f32 v30, v30\n"
"v_exp_f32 v31, v31\n"
"v_exp_f32 v32, v32\n"
"v_exp_f32 v33, v33\n"
"v_exp_f32 v34, v34\n"
"v_exp_f32 v35, v35\n"
"v_exp_f32 v36, v36\n"
"v_exp_f32 v37, v37\n"
"v_exp_f32 v38, v38\n"
"v_exp_f32 v39, v39\n"
"v_exp_f32 v40, v40\n"
"v_exp_f32 v41, v41\n"
"v_exp_f32 v42, v42\n"
"v_exp_f32 v43, v43\n"
"v_exp_f32 v44, v44\n"
"v_exp_f32 v45, v45\n"
"v_exp_f32 v46, v46\n"
"v_exp_f32 v47, v47\n"
"s_setprio 2\n"
"s_waitcnt lgkmcnt(0)\n"
"v_smfmac_f32_32x32x32_bf16 v[48:63], v[80:83], v[16:23], v13\n"
"s_mov_b32 m0, s17\n"
"s_nop 0\n"
"buffer_load_dwordx4 v14, s[4:7], s16 offen sc1 lds\n"
"s_add_u32 s16, s16, 0x2000\n"
"s_add_u32 s17, s17, 0x2000\n"
"v_smfmac_f32_32x32x32_bf16 v[48:63], v[84:87], v[24:31], v13\n"
"s_mov_b64 exec, s[18:19]\n"
"ds_read_b128 v[80:83], v12 offset:10240\n"
"ds_read_b128 v[84:87], v12 offset:10880\n"
"s_mov_b64 exec, -1\n"
"v_mfma_f32_32x32x8_bf16 v[16:31], v[70:71], v[68:69], 0\n"
"ds_read_b64 v[70:71], v11 offset:10752\n"
"s_mov_b32 m0, s17\n"
"s_nop 0\n"
"buffer_load_dwordx4 v14, s[4:7], s16 offen sc1 lds\n"
"s_add_u32 s16, s16, 0x2000\n"
"s_add_u32 s17, s17, 0x2000\n"
"v_mfma_f32_32x32x16_bf16 v[16:31], v[72:75], v[64:67], v[16:31]\n"
"ds_read_b128 v[72:75], v10 offset:11264\n"
"v_smfmac_f32_32x32x32_bf16 v[48:63], v[88:91], v[32:39], v13\n"
"s_mov_b32 m0, s17\n"
"s_nop 0\n"
"buffer_load_dwordx4 v14, s[4:7], s16 offen sc1 lds\n"
"v_smfmac_f32_32x32x32_bf16 v[48:63], v[92:95], v[40:47], v13\n"
"s_mov_b64 exec, s[18:19]\n"
"ds_read_b128 v[88:91], v12 offset:11520\n"
"ds_read_b128 v[92:95], v12 offset:12160\n"
"s_mov_b64 exec, -1\n"
"v_mfma_f32_32x32x8_bf16 v[32:47], v[8:9], v[68:69], 0\n"
"ds_read_b64 v[8:9], v11 offset:11008\n"
"v_mfma_f32_32x32x16_bf16 v[32:47], v[76:79], v[64:67], v[32:47]\n"
"ds_read_b128 v[76:79], v10 offset:11776\n"
"s_setprio 1\n"
"v_exp_f32 v16, v16\n"
"v_exp_f32 v17, v17\n"
"v_exp_f32 v18, v18\n"
"v_exp_f32 v19, v19\n"
"v_exp_f32 v20, v20\n"
"v_exp_f32 v21, v21\n"
"v_exp_f32 v22, v22\n"
"v_exp_f32 v23, v23\n"
"v_exp_f32 v24, v24\n"
"v_exp_f32 v25, v25\n"
"v_exp_f32 v26, v26\n"
"v_exp_f32 v27, v27\n"
"v_exp_f32 v28, v28\n"
"v_exp_f32 v29, v29\n"
"v_exp_f32 v30, v30\n"
"v_exp_f32 v31, v31\n"
"v_exp_f32 v32, v32\n"
"v_exp_f32 v33, v33\n"
"v_exp_f32 v34, v34\n"
"v_exp_f32 v35, v35\n"
"v_exp_f32 v36, v36\n"
"v_exp_f32 v37, v37\n"
"v_exp_f32 v38, v38\n"
"v_exp_f32 v39, v39\n"
"v_exp_f32 v40, v40\n"
"v_exp_f32 v41, v41\n"
"v_exp_f32 v42, v42\n"
"v_exp_f32 v43, v43\n"
"v_exp_f32 v44, v44\n"
"v_exp_f32 v45, v45\n"
"v_exp_f32 v46, v46\n"
"v_exp_f32 v47, v47\n"
"s_setprio 2\n"
"s_waitcnt lgkmcnt(0)\n"
"v_smfmac_f32_32x32x32_bf16 v[48:63], v[80:83], v[16:23], v13\n"
"s_and_b32 s56, s63, 7\n"
"s_mul_i32 s57, s56, 0x43800\n"
"s_add_u32 s58, 0x1975000, s57\n"
"s_mul_i32 s59, s3, 0x1400\n"
"s_add_u32 s25, s58, s59\n"
"s_add_u32 s26, s25, 4096\n"
"s_add_u32 s38, s26, 87040\n"
"s_mul_i32 s56, s3, 0x2800\n"
"s_add_u32 s27, 0x19400, s56\n"
"s_cmp_eq_u32 s3, 3\n"
"s_cselect_b32 s26, s38, s26\n"
"s_cselect_b32 s27, 138240, s27\n"
"s_movk_i32 s12, 0x5000\n"
"s_cselect_b32 s12, 0x1400, s12\n"
"s_movk_i32 s20, 0x800\n"
"s_cselect_b32 s20, 0x400, s20\n"
"v_smfmac_f32_32x32x32_bf16 v[48:63], v[84:87], v[24:31], v13\n"
"s_mov_b64 exec, s[18:19]\n"
"ds_read_b128 v[80:83], v12 offset:12800\n"
"ds_read_b128 v[84:87], v12 offset:13440\n"
"s_mov_b64 exec, -1\n"
"v_mfma_f32_32x32x8_bf16 v[16:31], v[70:71], v[68:69], 0\n"
"ds_read_b64 v[70:71], v11 offset:20480\n"
"v_mfma_f32_32x32x16_bf16 v[16:31], v[72:75], v[64:67], v[16:31]\n"
"ds_read_b128 v[72:75], v10 offset:20480\n"
"v_smfmac_f32_32x32x32_bf16 v[48:63], v[88:91], v[32:39], v13\n"
"v_smfmac_f32_32x32x32_bf16 v[48:63], v[92:95], v[40:47], v13\n"
"s_mov_b64 exec, s[18:19]\n"
"ds_read_b128 v[88:91], v12 offset:14080\n"
"ds_read_b128 v[92:95], v12 offset:14720\n"
"s_mov_b64 exec, -1\n"
"v_mfma_f32_32x32x8_bf16 v[32:47], v[8:9], v[68:69], 0\n"
"ds_read_b64 v[8:9], v11 offset:20736\n"
"v_mfma_f32_32x32x16_bf16 v[32:47], v[76:79], v[64:67], v[32:47]\n"
"ds_read_b128 v[76:79], v10 offset:20992\n"
"s_setprio 1\n"
"v_exp_f32 v16, v16\n"
"v_exp_f32 v17, v17\n"
"v_exp_f32 v18, v18\n"
"v_exp_f32 v19, v19\n"
"v_exp_f32 v20, v20\n"
"v_exp_f32 v21, v21\n"
"v_exp_f32 v22, v22\n"
"v_exp_f32 v23, v23\n"
"v_exp_f32 v24, v24\n"
"v_exp_f32 v25, v25\n"
"v_exp_f32 v26, v26\n"
"v_exp_f32 v27, v27\n"
"v_exp_f32 v28, v28\n"
"v_exp_f32 v29, v29\n"
"v_exp_f32 v30, v30\n"
"v_exp_f32 v31, v31\n"
"v_exp_f32 v32, v32\n"
"v_exp_f32 v33, v33\n"
"v_exp_f32 v34, v34\n"
"v_exp_f32 v35, v35\n"
"v_exp_f32 v36, v36\n"
"v_exp_f32 v37, v37\n"
"v_exp_f32 v38, v38\n"
"v_exp_f32 v39, v39\n"
"v_exp_f32 v40, v40\n"
"v_exp_f32 v41, v41\n"
"v_exp_f32 v42, v42\n"
"v_exp_f32 v43, v43\n"
"v_exp_f32 v44, v44\n"
"v_exp_f32 v45, v45\n"
"v_exp_f32 v46, v46\n"
"v_exp_f32 v47, v47\n"
"s_setprio 2\n"
"s_waitcnt lgkmcnt(0)\n"
"v_smfmac_f32_32x32x32_bf16 v[48:63], v[80:83], v[16:23], v13\n"
"buffer_load_dwordx2 v[6:7], v15, s[4:7], s21 offen sc1\n"
"v_smfmac_f32_32x32x32_bf16 v[48:63], v[84:87], v[24:31], v13\n"
"s_mov_b64 exec, s[18:19]\n"
"ds_read_b128 v[80:83], v12 offset:20480\n"
"ds_read_b128 v[84:87], v12 offset:21120\n"
"s_mov_b64 exec, -1\n"
"v_mfma_f32_32x32x8_bf16 v[16:31], v[70:71], v[68:69], 0\n"
"ds_read_b64 v[70:71], v11 offset:20992\n"
"v_not_b32 v2, s32\n"
"s_lshl_b32 s56, s62, 6\n"
"s_lshl_b32 s57, s34, 3\n"
"s_add_u32 s58, s56, s57\n"
"s_add_u32 s59, s58, s63\n"
"s_lshl_b32 s60, s59, 10\n"
"s_add_u32 s38, 0x101000, s60\n"
"v_mfma_f32_32x32x16_bf16 v[16:31], v[72:75], v[64:67], v[16:31]\n"
"ds_read_b128 v[72:75], v10 offset:21504\n"
"v_smfmac_f32_32x32x32_bf16 v[48:63], v[88:91], v[32:39], v13\n"
"s_mov_b64 exec, 0xff\n"
"buffer_store_dwordx4 v[2:5], v14, s[4:7], s38 offen sc1\n"
"s_mov_b64 exec, -1\n"
"v_smfmac_f32_32x32x32_bf16 v[48:63], v[92:95], v[40:47], v13\n"
"s_mov_b64 exec, s[18:19]\n"
"ds_read_b128 v[88:91], v12 offset:21760\n"
"ds_read_b128 v[92:95], v12 offset:22400\n"
"s_mov_b64 exec, -1\n"
"v_mfma_f32_32x32x8_bf16 v[32:47], v[8:9], v[68:69], 0\n"
"s_cbranch_execz .Lk_tramp\n"
"s_nop 0\n"
"ds_read_b64 v[8:9], v11 offset:21248\n"
"v_mfma_f32_32x32x16_bf16 v[32:47], v[76:79], v[64:67], v[32:47]\n"
"ds_read_b128 v[76:79], v10 offset:22016\n"
"s_waitcnt vmcnt(2)\n"
"s_barrier\n"
"s_setprio 1\n"
"v_exp_f32 v16, v16\n"
"v_exp_f32 v17, v17\n"
"v_exp_f32 v18, v18\n"
"v_exp_f32 v19, v19\n"
"v_exp_f32 v20, v20\n"
"v_exp_f32 v21, v21\n"
"v_exp_f32 v22, v22\n"
"v_exp_f32 v23, v23\n"
"v_exp_f32 v24, v24\n"
"v_exp_f32 v25, v25\n"
"v_exp_f32 v26, v26\n"
"v_exp_f32 v27, v27\n"
"v_exp_f32 v28, v28\n"
"v_exp_f32 v29, v29\n"
"v_exp_f32 v30, v30\n"
"v_exp_f32 v31, v31\n"
"v_exp_f32 v32, v32\n"
"v_exp_f32 v33, v33\n"
"v_exp_f32 v34, v34\n"
"v_exp_f32 v35, v35\n"
"v_exp_f32 v36, v36\n"
"v_exp_f32 v37, v37\n"
"v_exp_f32 v38, v38\n"
"v_exp_f32 v39, v39\n"
"v_exp_f32 v40, v40\n"
"v_exp_f32 v41, v41\n"
"v_exp_f32 v42, v42\n"
"v_exp_f32 v43, v43\n"
"v_exp_f32 v44, v44\n"
"v_exp_f32 v45, v45\n"
"v_exp_f32 v46, v46\n"
"v_exp_f32 v47, v47\n"
"s_setprio 2\n"
"s_waitcnt lgkmcnt(0)\n"
"v_smfmac_f32_32x32x32_bf16 v[48:63], v[80:83], v[16:23], v13\n"
"s_waitcnt vmcnt(1)\n"
"v_cmp_ne_u64 vcc, s[32:33], v[6:7]\n"
"s_nop 0\n"
"s_cmp_eq_u64 vcc, 0\n"
"s_cbranch_scc1 .Lk_polld_63\n"
".Lk_poll_62:\n"
"s_sleep 4\n"
"buffer_load_dwordx2 v[6:7], v15, s[4:7], s21 offen sc1\n"
"s_waitcnt vmcnt(0)\n"
"v_cmp_ne_u64 vcc, s[32:33], v[6:7]\n"
"s_nop 0\n"
"s_cmp_eq_u64 vcc, 0\n"
"s_cbranch_scc1 .Lk_polld_63\n"
"s_sub_u32 s37, s37, 1\n"
"s_cmp_gt_i32 s37, 0\n"
"s_cbranch_scc1 .Lk_poll_62\n"
".Lk_polld_63:\n"
"v_smfmac_f32_32x32x32_bf16 v[48:63], v[84:87], v[24:31], v13\n"
"s_mov_b64 exec, s[18:19]\n"
"ds_read_b128 v[80:83], v12 offset:23040\n"
"ds_read_b128 v[84:87], v12 offset:23680\n"
"s_mov_b64 exec, -1\n"
"v_mfma_f32_32x32x8_bf16 v[16:31], v[70:71], v[68:69], 0\n"
"ds_read_b64 v[70:71], v11 offset:30720\n"
"buffer_load_dwordx4 v[248:251], v14, s[4:7], s13 offen sc1\n"
"v_mfma_f32_32x32x16_bf16 v[16:31], v[72:75], v[64:67], v[16:31]\n"
"ds_read_b128 v[72:75], v10 offset:30720\n"
"v_smfmac_f32_32x32x32_bf16 v[48:63], v[88:91], v[32:39], v13\n"
"buffer_load_dwordx4 v[252:255], v14, s[4:7], s13 offen offset:1024 sc1\n"
"s_sub_u32 s13, s13, s24\n"
"v_smfmac_f32_32x32x32_bf16 v[48:63], v[92:95], v[40:47], v13\n"
"s_mov_b64 exec, s[18:19]\n"
"ds_read_b128 v[88:91], v12 offset:24320\n"
"ds_read_b128 v[92:95], v12 offset:24960\n"
"s_mov_b64 exec, -1\n"
"v_mfma_f32_32x32x8_bf16 v[32:47], v[8:9], v[68:69], 0\n"
"ds_read_b64 v[8:9], v11 offset:30976\n"
"buffer_load_dwordx4 v[240:243], v14, s[4:7], s13 offen sc1\n"
"v_mfma_f32_32x32x16_bf16 v[32:47], v[76:79], v[64:67], v[32:47]\n"
"ds_read_b128 v[76:79], v10 offset:31232\n"
"s_setprio 1\n"
"v_exp_f32 v16, v16\n"
"v_exp_f32 v17, v17\n"
"v_exp_f32 v18, v18\n"
"v_exp_f32 v19, v19\n"
"v_exp_f32 v20, v20\n"
"v_exp_f32 v21, v21\n"
"v_exp_f32 v22, v22\n"
"v_exp_f32 v23, v23\n"
"v_exp_f32 v24, v24\n"
"v_exp_f32 v25, v25\n"
"v_exp_f32 v26, v26\n"
"v_exp_f32 v27, v27\n"
"v_exp_f32 v28, v28\n"
"v_exp_f32 v29, v29\n"
"v_exp_f32 v30, v30\n"
"v_exp_f32 v31, v31\n"
"v_exp_f32 v32, v32\n"
"v_exp_f32 v33, v33\n"
"v_exp_f32 v34, v34\n"
"v_exp_f32 v35, v35\n"
"v_exp_f32 v36, v36\n"
"v_exp_f32 v37, v37\n"
"v_exp_f32 v38, v38\n"
"v_exp_f32 v39, v39\n"
"v_exp_f32 v40, v40\n"
"v_exp_f32 v41, v41\n"
"v_exp_f32 v42, v42\n"
"v_exp_f32 v43, v43\n"
"v_exp_f32 v44, v44\n"
"v_exp_f32 v45, v45\n"
"v_exp_f32 v46, v46\n"
"v_exp_f32 v47, v47\n"
"s_setprio 2\n"
"s_waitcnt lgkmcnt(0)\n"
"v_smfmac_f32_32x32x32_bf16 v[48:63], v[80:83], v[16:23], v13\n"
"buffer_load_dwordx4 v[244:247], v14, s[4:7], s13 offen offset:1024 sc1\n"
"s_sub_u32 s13, s13, s24\n"
"v_smfmac_f32_32x32x32_bf16 v[48:63], v[84:87], v[24:31], v13\n"
"s_mov_b64 exec, s[18:19]\n"
"ds_read_b128 v[80:83], v12 offset:30720\n"
"ds_read_b128 v[84:87], v12 offset:31360\n"
"s_mov_b64 exec, -1\n"
"v_mfma_f32_32x32x8_bf16 v[16:31], v[70:71], v[68:69], 0\n"
"ds_read_b64 v[70:71], v11 offset:31232\n"
"buffer_load_dwordx4 v[232:235], v14, s[4:7], s13 offen sc1\n"
"v_mfma_f32_32x32x16_bf16 v[16:31], v[72:75], v[64:67], v[16:31]\n"
"ds_read_b128 v[72:75], v10 offset:31744\n"
"v_smfmac_f32_32x32x32_bf16 v[48:63], v[88:91], v[32:39], v13\n"
"buffer_load_dwordx4 v[236:239], v14, s[4:7], s13 offen offset:1024 sc1\n"
"s_sub_u32 s13, s13, s24\n"
"v_smfmac_f32_32x32x32_bf16 v[48:63], v[92:95], v[40:47], v13\n"
"s_mov_b64 exec, s[18:19]\n"
"ds_read_b128 v[88:91], v12 offset:32000\n"
"ds_read_b128 v[92:95], v12 offset:32640\n"
"s_mov_b64 exec, -1\n"
"v_mfma_f32_32x32x8_bf16 v[32:47], v[8:9], v[68:69], 0\n"
"ds_read_b64 v[8:9], v11 offset:31488\n"
"buffer_load_dwordx4 v[224:227], v14, s[4:7], s13 offen sc1\n"
"v_mfma_f32_32x32x16_bf16 v[32:47], v[76:79], v[64:67], v[32:47]\n"
"ds_read_b128 v[76:79], v10 offset:32256\n"
"s_setprio 1\n"
"v_exp_f32 v16, v16\n"
"v_exp_f32 v17, v17\n"
"v_exp_f32 v18, v18\n"
"v_exp_f32 v19, v19\n"
"v_exp_f32 v20, v20\n"
"v_exp_f32 v21, v21\n"
"v_exp_f32 v22, v22\n"
"v_exp_f32 v23, v23\n"
"v_exp_f32 v24, v24\n"
"v_exp_f32 v25, v25\n"
"v_exp_f32 v26, v26\n"
"v_exp_f32 v27, v27\n"
"v_exp_f32 v28, v28\n"
"v_exp_f32 v29, v29\n"
"v_exp_f32 v30, v30\n"
"v_exp_f32 v31, v31\n"
"v_exp_f32 v32, v32\n"
"v_exp_f32 v33, v33\n"
"v_exp_f32 v34, v34\n"
"v_exp_f32 v35, v35\n"
"v_exp_f32 v36, v36\n"
"v_exp_f32 v37, v37\n"
"v_exp_f32 v38, v38\n"
"v_exp_f32 v39, v39\n"
"v_exp_f32 v40, v40\n"
"v_exp_f32 v41, v41\n"
"v_exp_f32 v42, v42\n"
"v_exp_f32 v43, v43\n"
"v_exp_f32 v44, v44\n"
"v_exp_f32 v45, v45\n"
"v_exp_f32 v46, v46\n"
"v_exp_f32 v47, v47\n"
"s_setprio 2\n"
"s_waitcnt lgkmcnt(0)\n"
"v_smfmac_f32_32x32x32_bf16 v[48:63], v[80:83], v[16:23], v13\n"
"buffer_load_dwordx4 v[228:231], v14, s[4:7], s13 offen offset:1024 sc1\n"
"s_sub_u32 s13, s13, s24\n"
"v_smfmac_f32_32x32x32_bf16 v[48:63], v[84:87], v[24:31], v13\n"
"s_mov_b64 exec, s[18:19]\n"
"ds_read_b128 v[80:83], v12 offset:33280\n"
"ds_read_b128 v[84:87], v12 offset:33920\n"
"s_mov_b64 exec, -1\n"
"v_mfma_f32_32x32x8_bf16 v[16:31], v[70:71], v[68:69], 0\n"
"ds_read_b64 v[70:71], v11 offset:40960\n"
"buffer_load_dwordx4 v[216:219], v14, s[4:7], s13 offen sc1\n"
"v_mfma_f32_32x32x16_bf16 v[16:31], v[72:75], v[64:67], v[16:31]\n"
"ds_read_b128 v[72:75], v10 offset:40960\n"
"v_smfmac_f32_32x32x32_bf16 v[48:63], v[88:91], v[32:39], v13\n"
"buffer_load_dwordx4 v[220:223], v14, s[4:7], s13 offen offset:1024 sc1\n"
"s_sub_u32 s13, s13, s24\n"
"v_smfmac_f32_32x32x32_bf16 v[48:63], v[92:95], v[40:47], v13\n"
"s_mov_b64 exec, s[18:19]\n"
"ds_read_b128 v[88:91], v12 offset:34560\n"
"ds_read_b128 v[92:95], v12 offset:35200\n"
"s_mov_b64 exec, -1\n"
"v_mfma_f32_32x32x8_bf16 v[32:47], v[8:9], v[68:69], 0\n"
"ds_read_b64 v[8:9], v11 offset:41216\n"
"buffer_load_dwordx4 v[208:211], v14, s[4:7], s13 offen sc1\n"
"v_mfma_f32_32x32x16_bf16 v[32:47], v[76:79], v[64:67], v[32:47]\n"
"ds_read_b128 v[76:79], v10 offset:41472\n"
"s_setprio 1\n"
"v_exp_f32 v16, v16\n"
"v_exp_f32 v17, v17\n"
"v_exp_f32 v18, v18\n"
"v_exp_f32 v19, v19\n"
"v_exp_f32 v20, v20\n"
"v_exp_f32 v21, v21\n"
"v_exp_f32 v22, v22\n"
"v_exp_f32 v23, v23\n"
"v_exp_f32 v24, v24\n"
"v_exp_f32 v25, v25\n"
"v_exp_f32 v26, v26\n"
"v_exp_f32 v27, v27\n"
"v_exp_f32 v28, v28\n"
"v_exp_f32 v29, v29\n"
"v_exp_f32 v30, v30\n"
"v_exp_f32 v31, v31\n"
"v_exp_f32 v32, v32\n"
"v_exp_f32 v33, v33\n"
"v_exp_f32 v34, v34\n"
"v_exp_f32 v35, v35\n"
"v_exp_f32 v36, v36\n"
"v_exp_f32 v37, v37\n"
"v_exp_f32 v38, v38\n"
"v_exp_f32 v39, v39\n"
"v_exp_f32 v40, v40\n"
"v_exp_f32 v41, v41\n"
"v_exp_f32 v42, v42\n"
"v_exp_f32 v43, v43\n"
"v_exp_f32 v44, v44\n"
"v_exp_f32 v45, v45\n"
"v_exp_f32 v46, v46\n"
"v_exp_f32 v47, v47\n"
"s_setprio 2\n"
"s_waitcnt lgkmcnt(0)\n"
"v_smfmac_f32_32x32x32_bf16 v[48:63], v[80:83], v[16:23], v13\n"
"v_and_b32 v2, 31, v0\n"
"v_lshlrev_b32 v15, 10, v2\n"
"s_lshl_b32 s56, s34, 5\n"
"s_lshl_b32 s57, s56, 10\n"
"s_add_u32 s21, 0x41000, s57\n"
"buffer_load_dwordx2 v[6:7], v15, s[4:7], s21 offen sc1\n"
"v_smfmac_f32_32x32x32_bf16 v[48:63], v[84:87], v[24:31], v13\n"
"s_mov_b64 exec, s[18:19]\n"
"ds_read_b128 v[80:83], v12 offset:40960\n"
"ds_read_b128 v[84:87], v12 offset:41600\n"
"s_mov_b64 exec, -1\n"
"v_mfma_f32_32x32x8_bf16 v[16:31], v[70:71], v[68:69], 0\n"
"ds_read_b64 v[70:71], v11 offset:41472\n"
"buffer_load_dwordx4 v[212:215], v14, s[4:7], s13 offen offset:1024 sc1\n"
"s_sub_u32 s13, s13, s24\n"
"v_mfma_f32_32x32x16_bf16 v[16:31], v[72:75], v[64:67], v[16:31]\n"
"ds_read_b128 v[72:75], v10 offset:41984\n"
"v_smfmac_f32_32x32x32_bf16 v[48:63], v[88:91], v[32:39], v13\n"
"buffer_load_dwordx4 v[200:203], v14, s[4:7], s13 offen sc1\n"
"v_smfmac_f32_32x32x32_bf16 v[48:63], v[92:95], v[40:47], v13\n"
"s_mov_b64 exec, s[18:19]\n"
"ds_read_b128 v[88:91], v12 offset:42240\n"
"ds_read_b128 v[92:95], v12 offset:42880\n"
"s_mov_b64 exec, -1\n"
"v_mfma_f32_32x32x8_bf16 v[32:47], v[8:9], v[68:69], 0\n"
"ds_read_b64 v[8:9], v11 offset:41728\n"
"buffer_load_dwordx4 v[204:207], v14, s[4:7], s13 offen offset:1024 sc1\n"
"s_sub_u32 s13, s13, s24\n"
"s_add_u32 s38, s25, 248832\n"
"s_cmp_eq_u32 s34, 7\n"
"s_cselect_b32 s13, s38, s13\n"
"v_mfma_f32_32x32x16_bf16 v[32:47], v[76:79], v[64:67], v[32:47]\n"
"ds_read_b128 v[76:79], v10 offset:42496\n"
"s_setprio 1\n"
"v_exp_f32 v16, v16\n"
"v_exp_f32 v17, v17\n"
"v_exp_f32 v18, v18\n"
"v_exp_f32 v19, v19\n"
"v_exp_f32 v20, v20\n"
"v_exp_f32 v21, v21\n"
"v_exp_f32 v22, v22\n"
"v_exp_f32 v23, v23\n"
"v_exp_f32 v24, v24\n"
"v_exp_f32 v25, v25\n"
"v_exp_f32 v26, v26\n"
"v_exp_f32 v27, v27\n"
"v_exp_f32 v28, v28\n"
"v_exp_f32 v29, v29\n"
"v_exp_f32 v30, v30\n"
"v_exp_f32 v31, v31\n"
"v_exp_f32 v32, v32\n"
"v_exp_f32 v33, v33\n"
"v_exp_f32 v34, v34\n"
"v_exp_f32 v35, v35\n"
"v_exp_f32 v36, v36\n"
"v_exp_f32 v37, v37\n"
"v_exp_f32 v38, v38\n"
"v_exp_f32 v39, v39\n"
"v_exp_f32 v40, v40\n"
"v_exp_f32 v41, v41\n"
"v_exp_f32 v42, v42\n"
"v_exp_f32 v43, v43\n"
"v_exp_f32 v44, v44\n"
"v_exp_f32 v45, v45\n"
"v_exp_f32 v46, v46\n"
"v_exp_f32 v47, v47\n"
"s_setprio 2\n"
"s_waitcnt lgkmcnt(0)\n"
"v_smfmac_f32_32x32x32_bf16 v[48:63], v[80:83], v[16:23], v13\n"
"buffer_load_dwordx4 v[192:195], v14, s[4:7], s13 offen sc1\n"
"v_smfmac_f32_32x32x32_bf16 v[48:63], v[84:87], v[24:31], v13\n"
"s_mov_b64 exec, s[18:19]\n"
"ds_read_b128 v[80:83], v12 offset:43520\n"
"ds_read_b128 v[84:87], v12 offset:44160\n"
"s_mov_b64 exec, -1\n"
"v_mfma_f32_32x32x8_bf16 v[16:31], v[70:71], v[68:69], 0\n"
"ds_read_b64 v[70:71], v11 offset:51200\n"
"buffer_load_dwordx4 v[196:199], v14, s[4:7], s13 offen offset:1024 sc1\n"
"s_sub_u32 s13, s13, s24\n"
"v_mfma_f32_32x32x16_bf16 v[16:31], v[72:75], v[64:67], v[16:31]\n"
"ds_read_b128 v[72:75], v10 offset:51200\n"
"v_smfmac_f32_32x32x32_bf16 v[48:63], v[88:91], v[32:39], v13\n"
"buffer_load_dwordx4 v[184:187], v14, s[4:7], s13 offen sc1\n"
"v_smfmac_f32_32x32x32_bf16 v[48:63], v[92:95], v[40:47], v13\n"
"s_mov_b64 exec, s[18:19]\n"
"ds_read_b128 v[88:91], v12 offset:44800\n"
"ds_read_b128 v[92:95], v12 offset:45440\n"
"s_mov_b64 exec, -1\n"
"v_mfma_f32_32x32x8_bf16 v[32:47], v[8:9], v[68:69], 0\n"
"ds_read_b64 v[8:9], v11 offset:51456\n"
"buffer_load_dwordx4 v[188:191], v14, s[4:7], s13 offen offset:1024 sc1\n"
"s_sub_u32 s13, s13, s24\n"
"v_mfma_f32_32x32x16_bf16 v[32:47], v[76:79], v[64:67], v[32:47]\n"
"ds_read_b128 v[76:79], v10 offset:51712\n"
"s_setprio 1\n"
"v_exp_f32 v16, v16\n"
"v_exp_f32 v17, v17\n"
"v_exp_f32 v18, v18\n"
"v_exp_f32 v19, v19\n"
"v_exp_f32 v20, v20\n"
"v_exp_f32 v21, v21\n"
"v_exp_f32 v22, v22\n"
"v_exp_f32 v23, v23\n"
"v_exp_f32 v24, v24\n"
"v_exp_f32 v25, v25\n"
"v_exp_f32 v26, v26\n"
"v_exp_f32 v27, v27\n"
"v_exp_f32 v28, v28\n"
"v_exp_f32 v29, v29\n"
"v_exp_f32 v30, v30\n"
"v_exp_f32 v31, v31\n"
"v_exp_f32 v32, v32\n"
"v_exp_f32 v33, v33\n"
"v_exp_f32 v34, v34\n"
"v_exp_f32 v35, v35\n"
"v_exp_f32 v36, v36\n"
"v_exp_f32 v37, v37\n"
"v_exp_f32 v38, v38\n"
"v_exp_f32 v39, v39\n"
"v_exp_f32 v40, v40\n"
"v_exp_f32 v41, v41\n"
"v_exp_f32 v42, v42\n"
"v_exp_f32 v43, v43\n"
"v_exp_f32 v44, v44\n"
"v_exp_f32 v45, v45\n"
"v_exp_f32 v46, v46\n"
"v_exp_f32 v47, v47\n"
"s_setprio 2\n"
"s_waitcnt lgkmcnt(0)\n"
"v_add_u32 v10, 0xc800, v10\n"
"v_add_u32 v11, 0xc800, v11\n"
"v_add_u32 v12, 0xc800, v12\n"
"v_smfmac_f32_32x32x32_bf16 v[48:63], v[80:83], v[16:23], v13\n"
"s_waitcnt vmcnt(7)\n"
"v_cmp_ne_u64 vcc, s[32:33], v[6:7]\n"
"s_nop 0\n"
"s_cmp_eq_u64 vcc, 0\n"
"s_cbranch_scc1 .Lk_polld_65\n"
".Lk_poll_64:\n"
"s_sleep 4\n"
"buffer_load_dwordx2 v[6:7], v15, s[4:7], s21 offen sc1\n"
"s_waitcnt vmcnt(0)\n"
"v_cmp_ne_u64 vcc, s[32:33], v[6:7]\n"
"s_nop 0\n"
"s_cmp_eq_u64 vcc, 0\n"
"s_cbranch_scc1 .Lk_polld_65\n"
"s_sub_u32 s37, s37, 1\n"
"s_cmp_gt_i32 s37, 0\n"
"s_cbranch_scc1 .Lk_poll_64\n"
".Lk_polld_65:\n"
"v_smfmac_f32_32x32x32_bf16 v[48:63], v[84:87], v[24:31], v13\n"
"s_mov_b64 exec, s[18:19]\n"
"ds_read_b128 v[80:83], v12\n"
"ds_read_b128 v[84:87], v12 offset:640\n"
"s_mov_b64 exec, -1\n"
"v_mfma_f32_32x32x8_bf16 v[16:31], v[70:71], v[68:69], 0\n"
"ds_read_b64 v[70:71], v11 offset:512\n"
"buffer_load_dwordx4 v[176:179], v14, s[4:7], s13 offen sc1\n"
"v_mfma_f32_32x32x16_bf16 v[16:31], v[72:75], v[64:67], v[16:31]\n"
"ds_read_b128 v[72:75], v10 offset:1024\n"
"v_smfmac_f32_32x32x32_bf16 v[48:63], v[88:91], v[32:39], v13\n"
"buffer_load_dwordx4 v[180:183], v14, s[4:7], s13 offen offset:1024 sc1\n"
"s_sub_u32 s13, s13, s24\n"
"v_smfmac_f32_32x32x32_bf16 v[48:63], v[92:95], v[40:47], v13\n"
"s_mov_b64 exec, s[18:19]\n"
"ds_read_b128 v[88:91], v12 offset:1280\n"
"ds_read_b128 v[92:95], v12 offset:1920\n"
"s_mov_b64 exec, -1\n"
"v_mfma_f32_32x32x8_bf16 v[32:47], v[8:9], v[68:69], 0\n"
"ds_read_b64 v[8:9], v11 offset:768\n"
"buffer_load_dwordx4 v[168:171], v14, s[4:7], s13 offen sc1\n"
"v_mfma_f32_32x32x16_bf16 v[32:47], v[76:79], v[64:67], v[32:47]\n"
"ds_read_b128 v[76:79], v10 offset:1536\n"
"s_barrier\n"
"s_setprio 1\n"
"v_exp_f32 v16, v16\n"
"v_exp_f32 v17, v17\n"
"v_exp_f32 v18, v18\n"
"v_exp_f32 v19, v19\n"
"v_exp_f32 v20, v20\n"
"v_exp_f32 v21, v21\n"
"v_exp_f32 v22, v22\n"
"v_exp_f32 v23, v23\n"
"v_exp_f32 v24, v24\n"
"v_exp_f32 v25, v25\n"
"v_exp_f32 v26, v26\n"
"v_exp_f32 v27, v27\n"
"v_exp_f32 v28, v28\n"
"v_exp_f32 v29, v29\n"
"v_exp_f32 v30, v30\n"
"v_exp_f32 v31, v31\n"
"v_exp_f32 v32, v32\n"
"v_exp_f32 v33, v33\n"
"v_exp_f32 v34, v34\n"
"v_exp_f32 v35, v35\n"
"v_exp_f32 v36, v36\n"
"v_exp_f32 v37, v37\n"
"v_exp_f32 v38, v38\n"
"v_exp_f32 v39, v39\n"
"v_exp_f32 v40, v40\n"
"v_exp_f32 v41, v41\n"
"v_exp_f32 v42, v42\n"
"v_exp_f32 v43, v43\n"
"v_exp_f32 v44, v44\n"
"v_exp_f32 v45, v45\n"
"v_exp_f32 v46, v46\n"
"v_exp_f32 v47, v47\n"
"s_setprio 2\n"
"s_waitcnt lgkmcnt(0)\n"
"v_smfmac_f32_32x32x32_bf16 v[48:63], v[80:83], v[16:23], v13\n"
"s_mov_b32 m0, s15\n"
"s_nop 0\n"
"buffer_load_dwordx4 v14, s[4:7], s14 offen sc1 lds\n"
"s_add_u32 s14, s14, 0x2000\n"
"s_add_u32 s15, s15, 0x2000\n"
"v_smfmac_f32_32x32x32_bf16 v[48:63], v[84:87], v[24:31], v13\n"
"s_mov_b64 exec, s[18:19]\n"
"ds_read_b128 v[80:83], v12 offset:2560\n"
"ds_read_b128 v[84:87], v12 offset:3200\n"
"s_mov_b64 exec, -1\n"
"v_mfma_f32_32x32x8_bf16 v[16:31], v[70:71], v[68:69], 0\n"
"ds_read_b64 v[70:71], v11 offset:10240\n"
"s_mov_b32 m0, s15\n"
"s_nop 0\n"
"buffer_load_dwordx4 v14, s[4:7], s14 offen sc1 lds\n"
"s_add_u32 s14, s14, 0x2000\n"
"s_add_u32 s15, s15, 0x2000\n"
"v_mfma_f32_32x32x16_bf16 v[16:31], v[72:75], v[64:67], v[16:31]\n"
"ds_read_b128 v[72:75], v10 offset:10240\n"
"v_smfmac_f32_32x32x32_bf16 v[48:63], v[88:91], v[32:39], v13\n"
"buffer_load_dwordx4 v[172:175], v14, s[4:7], s13 offen offset:1024 sc1\n"
"s_sub_u32 s13, s13, s24\n"
"v_smfmac_f32_32x32x32_bf16 v[48:63], v[92:95], v[40:47], v13\n"
"s_mov_b64 exec, s[18:19]\n"
"ds_read_b128 v[88:91], v12 offset:3840\n"
"ds_read_b128 v[92:95], v12 offset:4480\n"
"s_mov_b64 exec, -1\n"
"v_mfma_f32_32x32x8_bf16 v[32:47], v[8:9], v[68:69], 0\n"
"ds_read_b64 v[8:9], v11 offset:10496\n"
"buffer_load_dwordx4 v[160:163], v14, s[4:7], s13 offen sc1\n"
"v_mfma_f32_32x32x16_bf16 v[32:47], v[76:79], v[64:67], v[32:47]\n"
"ds_read_b128 v[76:79], v10 offset:10752\n"
"s_setprio 1\n"
"v_exp_f32 v16, v16\n"
"v_exp_f32 v17, v17\n"
"v_exp_f32 v18, v18\n"
"v_exp_f32 v19, v19\n"
"v_exp_f32 v20, v20\n"
"v_exp_f32 v21, v21\n"
"v_exp_f32 v22, v22\n"
"v_exp_f32 v23, v23\n"
"v_exp_f32 v24, v24\n"
"v_exp_f32 v25, v25\n"
"v_exp_f32 v26, v26\n"
"v_exp_f32 v27, v27\n"
"v_exp_f32 v28, v28\n"
"v_exp_f32 v29, v29\n"
"v_exp_f32 v30, v30\n"
"v_exp_f32 v31, v31\n"
"v_exp_f32 v32, v32\n"
"v_exp_f32 v33, v33\n"
"v_exp_f32 v34, v34\n"
"v_exp_f32 v35, v35\n"
"v_exp_f32 v36, v36\n"
"v_exp_f32 v37, v37\n"
"v_exp_f32 v38, v38\n"
"v_exp_f32 v39, v39\n"
"v_exp_f32 v40, v40\n"
"v_exp_f32 v41, v41\n"
"v_exp_f32 v42, v42\n"
"v_exp_f32 v43, v43\n"
"v_exp_f32 v44, v44\n"
"v_exp_f32 v45, v45\n"
"v_exp_f32 v46, v46\n"
"v_exp_f32 v47, v47\n"
"s_setprio 2\n"
"s_waitcnt lgkmcnt(0)\n"
"v_smfmac_f32_32x32x32_bf16 v[48:63], v[80:83], v[16:23], v13\n"
"s_mov_b32 m0, s15\n"
"s_nop 0\n"
"buffer_load_dwordx4 v14, s[4:7], s14 offen sc1 lds\n"
"s_add_u32 s14, s14, 0x2000\n"
"s_add_u32 s15, s15, 0x2000\n"
"v_smfmac_f32_32x32x32_bf16 v[48:63], v[84:87], v[24:31], v13\n"
"s_lshr_b32 s56, s40, 1\n"
"s_lshl_b32 s57, s56, 12\n"
"s_and_b32 s58, s34, 1\n"
"s_mov_b64 exec, s[18:19]\n"
"ds_read_b128 v[80:83], v12 offset:10240\n"
"ds_read_b128 v[84:87], v12 offset:10880\n"
"s_mov_b64 exec, -1\n"
"v_mfma_f32_32x32x8_bf16 v[16:31], v[70:71], v[68:69], 0\n"
"ds_read_b64 v[70:71], v11 offset:10752\n"
"s_mov_b32 m0, s15\n"
"s_nop 0\n"
"buffer_load_dwordx4 v14, s[4:7], s14 offen sc1 lds\n"
"s_add_u32 s14, s14, 0x2000\n"
"s_add_u32 s15, s15, 0x2000\n"
"v_mfma_f32_32x32x16_bf16 v[16:31], v[72:75], v[64:67], v[16:31]\n"
"ds_read_b128 v[72:75], v10 offset:11264\n"
"v_smfmac_f32_32x32x32_bf16 v[48:63], v[88:91], v[32:39], v13\n"
"buffer_load_dwordx4 v[164:167], v14, s[4:7], s13 offen offset:1024 sc1\n"
"s_sub_u32 s13, s13, s24\n"
"v_smfmac_f32_32x32x32_bf16 v[48:63], v[92:95], v[40:47], v13\n"
"s_mov_b64 exec, s[18:19]\n"
"ds_read_b128 v[88:91], v12 offset:11520\n"
"ds_read_b128 v[92:95], v12 offset:12160\n"
"s_mov_b64 exec, -1\n"
"v_mfma_f32_32x32x8_bf16 v[32:47], v[8:9], v[68:69], 0\n"
"ds_read_b64 v[8:9], v11 offset:11008\n"
"buffer_load_dwordx4 v[152:155], v14, s[4:7], s13 offen sc1\n"
"v_mfma_f32_32x32x16_bf16 v[32:47], v[76:79], v[64:67], v[32:47]\n"
"ds_read_b128 v[76:79], v10 offset:11776\n"
"s_setprio 1\n"
"v_exp_f32 v16, v16\n"
"v_exp_f32 v17, v17\n"
"v_exp_f32 v18, v18\n"
"v_exp_f32 v19, v19\n"
"v_exp_f32 v20, v20\n"
"v_exp_f32 v21, v21\n"
"v_exp_f32 v22, v22\n"
"v_exp_f32 v23, v23\n"
"v_exp_f32 v24, v24\n"
"v_exp_f32 v25, v25\n"
"v_exp_f32 v26, v26\n"
"v_exp_f32 v27, v27\n"
"v_exp_f32 v28, v28\n"
"v_exp_f32 v29, v29\n"
"v_exp_f32 v30, v30\n"
"v_exp_f32 v31, v31\n"
"v_exp_f32 v32, v32\n"
"v_exp_f32 v33, v33\n"
"v_exp_f32 v34, v34\n"
"v_exp_f32 v35, v35\n"
"v_exp_f32 v36, v36\n"
"v_exp_f32 v37, v37\n"
"v_exp_f32 v38, v38\n"
"v_exp_f32 v39, v39\n"
"v_exp_f32 v40, v40\n"
"v_exp_f32 v41, v41\n"
"v_exp_f32 v42, v42\n"
"v_exp_f32 v43, v43\n"
"v_exp_f32 v44, v44\n"
"v_exp_f32 v45, v45\n"
"v_exp_f32 v46, v46\n"
"v_exp_f32 v47, v47\n"
"s_setprio 2\n"
"s_waitcnt lgkmcnt(0)\n"
"v_smfmac_f32_32x32x32_bf16 v[48:63], v[80:83], v[16:23], v13\n"
"s_mov_b32 m0, s15\n"
"s_nop 0\n"
"buffer_load_dwordx4 v14, s[4:7], s14 offen sc1 lds\n"
"s_add_u32 s14, s14, 0x2000\n"
"s_add_u32 s15, s15, 0x2000\n"
"v_smfmac_f32_32x32x32_bf16 v[48:63], v[84:87], v[24:31], v13\n"
"s_lshl_b32 s59, s58, 11\n"
"s_add_u32 s60, s57, s59\n"
"s_and_b32 s61, s40, 1\n"
"s_lshl_b32 s56, s61, 9\n"
"s_mov_b64 exec, s[18:19]\n"
"ds_read_b128 v[80:83], v12 offset:12800\n"
"ds_read_b128 v[84:87], v12 offset:13440\n"
"s_mov_b64 exec, -1\n"
"v_mfma_f32_32x32x8_bf16 v[16:31], v[70:71], v[68:69], 0\n"
"ds_read_b64 v[70:71], v11 offset:20480\n"
"s_mov_b32 m0, s15\n"
"s_nop 0\n"
"buffer_load_dwordx4 v14, s[4:7], s14 offen sc1 lds\n"
"s_add_u32 s14, s14, 0x2000\n"
"s_add_u32 s15, s15, 0x2000\n"
"v_mfma_f32_32x32x16_bf16 v[16:31], v[72:75], v[64:67], v[16:31]\n"
"ds_read_b128 v[72:75], v10 offset:20480\n"
"v_smfmac_f32_32x32x32_bf16 v[48:63], v[88:91], v[32:39], v13\n"
"buffer_load_dwordx4 v[156:159], v14, s[4:7], s13 offen offset:1024 sc1\n"
"s_sub_u32 s13, s13, s24\n"
"v_smfmac_f32_32x32x32_bf16 v[48:63], v[92:95], v[40:47], v13\n"
"s_mov_b64 exec, s[18:19]\n"
"ds_read_b128 v[88:91], v12 offset:14080\n"
"ds_read_b128 v[92:95], v12 offset:14720\n"
"s_mov_b64 exec, -1\n"
"v_mfma_f32_32x32x8_bf16 v[32:47], v[8:9], v[68:69], 0\n"
"ds_read_b64 v[8:9], v11 offset:20736\n"
"buffer_load_dwordx4 v[144:147], v14, s[4:7], s13 offen sc1\n"
"v_mfma_f32_32x32x16_bf16 v[32:47], v[76:79], v[64:67], v[32:47]\n"
"ds_read_b128 v[76:79], v10 offset:20992\n"
"s_setprio 1\n"
"v_exp_f32 v16, v16\n"
"v_exp_f32 v17, v17\n"
"v_exp_f32 v18, v18\n"
"v_exp_f32 v19, v19\n"
"v_exp_f32 v20, v20\n"
"v_exp_f32 v21, v21\n"
"v_exp_f32 v22, v22\n"
"v_exp_f32 v23, v23\n"
"v_exp_f32 v24, v24\n"
"v_exp_f32 v25, v25\n"
"v_exp_f32 v26, v26\n"
"v_exp_f32 v27, v27\n"
"v_exp_f32 v28, v28\n"
"v_exp_f32 v29, v29\n"
"v_exp_f32 v30, v30\n"
"v_exp_f32 v31, v31\n"
"v_exp_f32 v32, v32\n"
"v_exp_f32 v33, v33\n"
"v_exp_f32 v34, v34\n"
"v_exp_f32 v35, v35\n"
"v_exp_f32 v36, v36\n"
"v_exp_f32 v37, v37\n"
"v_exp_f32 v38, v38\n"
"v_exp_f32 v39, v39\n"
"v_exp_f32 v40, v40\n"
"v_exp_f32 v41, v41\n"
"v_exp_f32 v42, v42\n"
"v_exp_f32 v43, v43\n"
"v_exp_f32 v44, v44\n"
"v_exp_f32 v45, v45\n"
"v_exp_f32 v46, v46\n"
"v_exp_f32 v47, v47\n"
"s_setprio 2\n"
"s_waitcnt lgkmcnt(0)\n"
"v_smfmac_f32_32x32x32_bf16 v[48:63], v[80:83], v[16:23], v13\n"
"s_mov_b32 m0, s15\n"
"s_nop 0\n"
"buffer_load_dwordx4 v14, s[4:7], s14 offen sc1 lds\n"
"s_add_u32 s14, s14, 0x2000\n"
"s_add_u32 s15, s15, 0x2000\n"
"v_smfmac_f32_32x32x32_bf16 v[48:63], v[84:87], v[24:31], v13\n"
"s_add_u32 s58, s60, s56\n"
"v_lshrrev_b32 v2, 4, v0\n"
"v_and_b32 v3, 1, v2\n"
"s_mov_b64 exec, s[18:19]\n"
"ds_read_b128 v[80:83], v12 offset:20480\n"
"ds_read_b128 v[84:87], v12 offset:21120\n"
"s_mov_b64 exec, -1\n"
"v_mfma_f32_32x32x8_bf16 v[16:31], v[70:71], v[68:69], 0\n"
"ds_read_b64 v[70:71], v11 offset:20992\n"
"s_mov_b32 m0, s15\n"
"s_nop 0\n"
"buffer_load_dwordx4 v14, s[4:7], s14 offen sc1 lds\n"
"s_add_u32 s14, s14, 0x2000\n"
"s_add_u32 s15, s15, 0x2000\n"
"v_mfma_f32_32x32x16_bf16 v[16:31], v[72:75], v[64:67], v[16:31]\n"
"ds_read_b128 v[72:75], v10 offset:21504\n"
"v_smfmac_f32_32x32x32_bf16 v[48:63], v[88:91], v[32:39], v13\n"
"buffer_load_dwordx4 v[148:151], v14, s[4:7], s13 offen offset:1024 sc1\n"
"s_sub_u32 s13, s13, s24\n"
"v_smfmac_f32_32x32x32_bf16 v[48:63], v[92:95], v[40:47], v13\n"
"s_mov_b64 exec, s[18:19]\n"
"ds_read_b128 v[88:91], v12 offset:21760\n"
"ds_read_b128 v[92:95], v12 offset:22400\n"
"s_mov_b64 exec, -1\n"
"v_mfma_f32_32x32x8_bf16 v[32:47], v[8:9], v[68:69], 0\n"
"ds_read_b64 v[8:9], v11 offset:21248\n"
"buffer_load_dwordx4 v[136:139], v14, s[4:7], s13 offen sc1\n"
"v_mfma_f32_32x32x16_bf16 v[32:47], v[76:79], v[64:67], v[32:47]\n"
"ds_read_b128 v[76:79], v10 offset:22016\n"
"s_setprio 1\n"
"v_exp_f32 v16, v16\n"
"v_exp_f32 v17, v17\n"
"v_exp_f32 v18, v18\n"
"v_exp_f32 v19, v19\n"
"v_exp_f32 v20, v20\n"
"v_exp_f32 v21, v21\n"
"v_exp_f32 v22, v22\n"
"v_exp_f32 v23, v23\n"
"v_exp_f32 v24, v24\n"
"v_exp_f32 v25, v25\n"
"v_exp_f32 v26, v26\n"
"v_exp_f32 v27, v27\n"
"v_exp_f32 v28, v28\n"
"v_exp_f32 v29, v29\n"
"v_exp_f32 v30, v30\n"
"v_exp_f32 v31, v31\n"
"v_exp_f32 v32, v32\n"
"v_exp_f32 v33, v33\n"
"v_exp_f32 v34, v34\n"
"v_exp_f32 v35, v35\n"
"v_exp_f32 v36, v36\n"
"v_exp_f32 v37, v37\n"
"v_exp_f32 v38, v38\n"
"v_exp_f32 v39, v39\n"
"v_exp_f32 v40, v40\n"
"v_exp_f32 v41, v41\n"
"v_exp_f32 v42, v42\n"
"v_exp_f32 v43, v43\n"
"v_exp_f32 v44, v44\n"
"v_exp_f32 v45, v45\n"
"v_exp_f32 v46, v46\n"
"v_exp_f32 v47, v47\n"
"s_setprio 2\n"
"s_waitcnt lgkmcnt(0)\n"
"v_smfmac_f32_32x32x32_bf16 v[48:63], v[80:83], v[16:23], v13\n"
"s_mov_b32 m0, s15\n"
"s_nop 0\n"
"buffer_load_dwordx4 v14, s[4:7], s14 offen sc1 lds\n"
"s_add_u32 s14, s14, 0x2000\n"
"s_add_u32 s15, s15, 0x2000\n"
"v_smfmac_f32_32x32x32_bf16 v[48:63], v[84:87], v[24:31], v13\n"
"v_lshlrev_b32 v4, 10, v3\n"
"v_add_u32 v5, s58, v4\n"
"v_lshrrev_b32 v2, 5, v0\n"
"v_lshlrev_b32 v3, 8, v2\n"
"s_mov_b64 exec, s[18:19]\n"
"ds_read_b128 v[80:83], v12 offset:23040\n"
"ds_read_b128 v[84:87], v12 offset:23680\n"
"s_mov_b64 exec, -1\n"
"v_mfma_f32_32x32x8_bf16 v[16:31], v[70:71], v[68:69], 0\n"
"ds_read_b64 v[70:71], v11 offset:30720\n"
"s_mov_b32 m0, s15\n"
"s_nop 0\n"
"buffer_load_dwordx4 v14, s[4:7], s14 offen sc1 lds\n"
"s_add_u32 s14, s14, 0x2000\n"
"s_add_u32 s15, s15, 0x2000\n"
"v_mfma_f32_32x32x16_bf16 v[16:31], v[72:75], v[64:67], v[16:31]\n"
"ds_read_b128 v[72:75], v10 offset:30720\n"
"v_smfmac_f32_32x32x32_bf16 v[48:63], v[88:91], v[32:39], v13\n"
"buffer_load_dwordx4 v[140:143], v14, s[4:7], s13 offen offset:1024 sc1\n"
"s_sub_u32 s13, s13, s24\n"
"v_smfmac_f32_32x32x32_bf16 v[48:63], v[92:95], v[40:47], v13\n"
"s_mov_b64 exec, s[18:19]\n"
"ds_read_b128 v[88:91], v12 offset:24320\n"
"ds_read_b128 v[92:95], v12 offset:24960\n"
"s_mov_b64 exec, -1\n"
"v_mfma_f32_32x32x8_bf16 v[32:47], v[8:9], v[68:69], 0\n"
"ds_read_b64 v[8:9], v11 offset:30976\n"
"buffer_load_dwordx4 v[128:131], v14, s[4:7], s13 offen sc1\n"
"v_mfma_f32_32x32x16_bf16 v[32:47], v[76:79], v[64:67], v[32:47]\n"
"ds_read_b128 v[76:79], v10 offset:31232\n"
"s_setprio 1\n"
"v_exp_f32 v16, v16\n"
"v_exp_f32 v17, v17\n"
"v_exp_f32 v18, v18\n"
"v_exp_f32 v19, v19\n"
"v_exp_f32 v20, v20\n"
"v_exp_f32 v21, v21\n"
"v_exp_f32 v22, v22\n"
"v_exp_f32 v23, v23\n"
"v_exp_f32 v24, v24\n"
"v_exp_f32 v25, v25\n"
"s_cbranch_execz .Lk_tramp\n"
"s_nop 0\n"
"v_exp_f32 v26, v26\n"
"v_exp_f32 v27, v27\n"
"v_exp_f32 v28, v28\n"
"v_exp_f32 v29, v29\n"
"v_exp_f32 v30, v30\n"
"v_exp_f32 v31, v31\n"
"v_exp_f32 v32, v32\n"
"v_exp_f32 v33, v33\n"
"v_exp_f32 v34, v34\n"
"v_exp_f32 v35, v35\n"
"v_exp_f32 v36, v36\n"
"v_exp_f32 v37, v37\n"
"v_exp_f32 v38, v38\n"
"v_exp_f32 v39, v39\n"
"v_exp_f32 v40, v40\n"
"v_exp_f32 v41, v41\n"
"v_exp_f32 v42, v42\n"
"v_exp_f32 v43, v43\n"
"v_exp_f32 v44, v44\n"
"v_exp_f32 v45, v45\n"
"v_exp_f32 v46, v46\n"
"v_exp_f32 v47, v47\n"
"s_setprio 2\n"
"s_waitcnt lgkmcnt(0)\n"
"v_smfmac_f32_32x32x32_bf16 v[48:63], v[80:83], v[16:23], v13\n"
"s_mov_b32 m0, s15\n"
"s_nop 0\n"
"buffer_load_dwordx4 v14, s[4:7], s14 offen sc1 lds\n"
"s_add_u32 s14, s14, 0x2000\n"
"s_add_u32 s15, s15, 0x2000\n"
"v_smfmac_f32_32x32x32_bf16 v[48:63], v[84:87], v[24:31], v13\n"
"v_add_u32 v4, v5, v3\n"
"v_and_b32 v2, 15, v0\n"
"v_lshlrev_b32 v5, 4, v2\n"
"s_mov_b64 exec, s[18:19]\n"
"ds_read_b128 v[80:83], v12 offset:30720\n"
"ds_read_b128 v[84:87], v12 offset:31360\n"
"s_mov_b64 exec, -1\n"
"v_mfma_f32_32x32x8_bf16 v[16:31], v[70:71], v[68:69], 0\n"
"ds_read_b64 v[70:71], v11 offset:31232\n"
"s_mov_b32 m0, s15\n"
"s_nop 0\n"
"buffer_load_dwordx4 v14, s[4:7], s14 offen sc1 lds\n"
"s_add_u32 s14, s14, 0x2000\n"
"s_add_u32 s15, s15, 0x2000\n"
"v_mfma_f32_32x32x16_bf16 v[16:31], v[72:75], v[64:67], v[16:31]\n"
"ds_read_b128 v[72:75], v10 offset:31744\n"
"v_smfmac_f32_32x32x32_bf16 v[48:63], v[88:91], v[32:39], v13\n"
"buffer_load_dwordx4 v[132:135], v14, s[4:7], s13 offen offset:1024 sc1\n"
"s_sub_u32 s13, s13, s24\n"
"v_smfmac_f32_32x32x32_bf16 v[48:63], v[92:95], v[40:47], v13\n"
"s_mov_b64 exec, s[18:19]\n"
"ds_read_b128 v[88:91], v12 offset:32000\n"
"ds_read_b128 v[92:95], v12 offset:32640\n"
"s_mov_b64 exec, -1\n"
"v_mfma_f32_32x32x8_bf16 v[32:47], v[8:9], v[68:69], 0\n"
"ds_read_b64 v[8:9], v11 offset:31488\n"
"buffer_load_dwordx4 v[120:123], v14, s[4:7], s13 offen sc1\n"
"v_mfma_f32_32x32x16_bf16 v[32:47], v[76:79], v[64:67], v[32:47]\n"
"ds_read_b128 v[76:79], v10 offset:32256\n"
"s_setprio 1\n"
"v_exp_f32 v16, v16\n"
"v_exp_f32 v17, v17\n"
"v_exp_f32 v18, v18\n"
"v_exp_f32 v19, v19\n"
"v_exp_f32 v20, v20\n"
"v_exp_f32 v21, v21\n"
"v_exp_f32 v22, v22\n"
"v_exp_f32 v23, v23\n"
"v_exp_f32 v24, v24\n"
"v_exp_f32 v25, v25\n"
"v_exp_f32 v26, v26\n"
"v_exp_f32 v27, v27\n"
"v_exp_f32 v28, v28\n"
"v_exp_f32 v29, v29\n"
"v_exp_f32 v30, v30\n"
"v_exp_f32 v31, v31\n"
"v_exp_f32 v32, v32\n"
"v_exp_f32 v33, v33\n"
"v_exp_f32 v34, v34\n"
"v_exp_f32 v35, v35\n"
"v_exp_f32 v36, v36\n"
"v_exp_f32 v37, v37\n"
"v_exp_f32 v38, v38\n"
"v_exp_f32 v39, v39\n"
"v_exp_f32 v40, v40\n"
"v_exp_f32 v41, v41\n"
"v_exp_f32 v42, v42\n"
"v_exp_f32 v43, v43\n"
"v_exp_f32 v44, v44\n"
"v_exp_f32 v45, v45\n"
"v_exp_f32 v46, v46\n"
"v_exp_f32 v47, v47\n"
"s_setprio 2\n"
"s_waitcnt lgkmcnt(0)\n"
"v_smfmac_f32_32x32x32_bf16 v[48:63], v[80:83], v[16:23], v13\n"
"buffer_load_dwordx4 v[124:127], v14, s[4:7], s13 offen offset:1024 sc1\n"
"s_sub_u32 s13, s13, s24\n"
"v_smfmac_f32_32x32x32_bf16 v[48:63], v[84:87], v[24:31], v13\n"
"v_add_u32 v6, v4, v5\n"
"s_lshl_b32 s56, s40, 9\n"
"s_add_u32 s57, 0x10000, s56\n"
"s_and_b32 s58, s34, 1\n"
"s_mov_b64 exec, s[18:19]\n"
"ds_read_b128 v[80:83], v12 offset:33280\n"
"ds_read_b128 v[84:87], v12 offset:33920\n"
"s_mov_b64 exec, -1\n"
"v_mfma_f32_32x32x8_bf16 v[16:31], v[70:71], v[68:69], 0\n"
"ds_read_b64 v[70:71], v11 offset:40960\n"
"buffer_load_dwordx4 v[112:115], v14, s[4:7], s13 offen sc1\n"
"v_mfma_f32_32x32x16_bf16 v[16:31], v[72:75], v[64:67], v[16:31]\n"
"ds_read_b128 v[72:75], v10 offset:40960\n"
"v_smfmac_f32_32x32x32_bf16 v[48:63], v[88:91], v[32:39], v13\n"
"buffer_load_dwordx4 v[116:119], v14, s[4:7], s13 offen offset:1024 sc1\n"
"s_sub_u32 s13, s13, s24\n"
"v_smfmac_f32_32x32x32_bf16 v[48:63], v[92:95], v[40:47], v13\n"
"s_mov_b64 exec, s[18:19]\n"
"ds_read_b128 v[88:91], v12 offset:34560\n"
"ds_read_b128 v[92:95], v12 offset:35200\n"
"s_mov_b64 exec, -1\n"
"v_mfma_f32_32x32x8_bf16 v[32:47], v[8:9], v[68:69], 0\n"
"ds_read_b64 v[8:9], v11 offset:41216\n"
"buffer_load_dwordx4 v[104:107], v14, s[4:7], s13 offen sc1\n"
"v_mfma_f32_32x32x16_bf16 v[32:47], v[76:79], v[64:67], v[32:47]\n"
"ds_read_b128 v[76:79], v10 offset:41472\n"
"s_setprio 1\n"
"v_exp_f32 v16, v16\n"
"v_exp_f32 v17, v17\n"
"v_exp_f32 v18, v18\n"
"v_exp_f32 v19, v19\n"
"v_exp_f32 v20, v20\n"
"v_exp_f32 v21, v21\n"
"v_exp_f32 v22, v22\n"
"v_exp_f32 v23, v23\n"
"v_exp_f32 v24, v24\n"
"v_exp_f32 v25, v25\n"
"v_exp_f32 v26, v26\n"
"v_exp_f32 v27, v27\n"
"v_exp_f32 v28, v28\n"
"v_exp_f32 v29, v29\n"
"v_exp_f32 v30, v30\n"
"v_exp_f32 v31, v31\n"
"v_exp_f32 v32, v32\n"
"v_exp_f32 v33, v33\n"
"v_exp_f32 v34, v34\n"
"v_exp_f32 v35, v35\n"
"v_exp_f32 v36, v36\n"
"v_exp_f32 v37, v37\n"
"v_exp_f32 v38, v38\n"
"v_exp_f32 v39, v39\n"
"v_exp_f32 v40, v40\n"
"v_exp_f32 v41, v41\n"
"v_exp_f32 v42, v42\n"
"v_exp_f32 v43, v43\n"
"v_exp_f32 v44, v44\n"
"v_exp_f32 v45, v45\n"
"v_exp_f32 v46, v46\n"
"v_exp_f32 v47, v47\n"
"s_setprio 2\n"
"s_waitcnt lgkmcnt(0)\n"
"v_smfmac_f32_32x32x32_bf16 v[48:63], v[80:83], v[16:23], v13\n"
"buffer_load_dwordx4 v[108:111], v14, s[4:7], s13 offen offset:1024 sc1\n"
"s_sub_u32 s13, s13, s24\n"
"v_smfmac_f32_32x32x32_bf16 v[48:63], v[84:87], v[24:31], v13\n"
"s_lshl_b32 s59, s58, 8\n"
"s_add_u32 s60, s57, s59\n"
"v_lshlrev_b32 v2, 3, v0\n"
"v_add_u32 v7, s60, v2\n"
"s_mov_b64 exec, s[18:19]\n"
"ds_read_b128 v[80:83], v12 offset:40960\n"
"ds_read_b128 v[84:87], v12 offset:41600\n"
"s_mov_b64 exec, -1\n"
"v_mfma_f32_32x32x8_bf16 v[16:31], v[70:71], v[68:69], 0\n"
"ds_read_b64 v[70:71], v11 offset:41472\n"
"buffer_load_dwordx4 v[96:99], v14, s[4:7], s13 offen sc1\n"
"v_mfma_f32_32x32x16_bf16 v[16:31], v[72:75], v[64:67], v[16:31]\n"
"ds_read_b128 v[72:75], v10 offset:41984\n"
"v_smfmac_f32_32x32x32_bf16 v[48:63], v[88:91], v[32:39], v13\n"
"buffer_load_dwordx4 v[100:103], v14, s[4:7], s13 offen offset:1024 sc1\n"
"v_smfmac_f32_32x32x32_bf16 v[48:63], v[92:95], v[40:47], v13\n"
"s_mov_b64 exec, s[18:19]\n"
"ds_read_b128 v[88:91], v12 offset:42240\n"
"ds_read_b128 v[92:95], v12 offset:42880\n"
"s_mov_b64 exec, -1\n"
"v_mfma_f32_32x32x8_bf16 v[32:47], v[8:9], v[68:69], 0\n"
"ds_read_b64 v[8:9], v11 offset:41728\n"
"v_mfma_f32_32x32x16_bf16 v[32:47], v[76:79], v[64:67], v[32:47]\n"
"ds_read_b128 v[76:79], v10 offset:42496\n"
"s_barrier\n"
"s_setprio 1\n"
"v_exp_f32 v16, v16\n"
"v_exp_f32 v17, v17\n"
"v_exp_f32 v18, v18\n"
"v_exp_f32 v19, v19\n"
"v_exp_f32 v20, v20\n"
"v_exp_f32 v21, v21\n"
"v_exp_f32 v22, v22\n"
"v_exp_f32 v23, v23\n"
"v_exp_f32 v24, v24\n"
"v_exp_f32 v25, v25\n"
"v_exp_f32 v26, v26\n"
"v_exp_f32 v27, v27\n"
"v_exp_f32 v28, v28\n"
"v_exp_f32 v29, v29\n"
"v_exp_f32 v30, v30\n"
"v_exp_f32 v31, v31\n"
"v_exp_f32 v32, v32\n"
"v_exp_f32 v33, v33\n"
"v_exp_f32 v34, v34\n"
"v_exp_f32 v35, v35\n"
"v_exp_f32 v36, v36\n"
"v_exp_f32 v37, v37\n"
"v_exp_f32 v38, v38\n"
"v_exp_f32 v39, v39\n"
"v_exp_f32 v40, v40\n"
"v_exp_f32 v41, v41\n"
"v_exp_f32 v42, v42\n"
"v_exp_f32 v43, v43\n"
"v_exp_f32 v44, v44\n"
"v_exp_f32 v45, v45\n"
"v_exp_f32 v46, v46\n"
"v_exp_f32 v47, v47\n"
"s_setprio 2\n"
"s_waitcnt lgkmcnt(0)\n"
"v_smfmac_f32_32x32x32_bf16 v[48:63], v[80:83], v[16:23], v13\n"
"s_mov_b32 m0, s27\n"
"s_nop 0\n"
"buffer_load_dwordx4 v14, s[4:7], s26 offen sc1 lds\n"
"s_add_u32 s26, s26, s12\n"
"s_add_u32 s27, s27, s20\n"
"v_smfmac_f32_32x32x32_bf16 v[48:63], v[84:87], v[24:31], v13\n"
"s_mov_b64 exec, s[18:19]\n"
"ds_read_b128 v[80:83], v12 offset:43520\n"
"ds_read_b128 v[84:87], v12 offset:44160\n"
"s_mov_b64 exec, -1\n"
"v_mfma_f32_32x32x8_bf16 v[16:31], v[70:71], v[68:69], 0\n"
"ds_read_b64 v[70:71], v11 offset:51200\n"
"s_mov_b32 m0, s27\n"
"s_nop 0\n"
"buffer_load_dwordx4 v14, s[4:7], s26 offen sc1 lds\n"
"s_add_u32 s26, s26, s12\n"
"s_add_u32 s27, s27, s20\n"
"v_mfma_f32_32x32x16_bf16 v[16:31], v[72:75], v[64:67], v[16:31]\n"
"ds_read_b128 v[72:75], v10 offset:51200\n"
"v_smfmac_f32_32x32x32_bf16 v[48:63], v[88:91], v[32:39], v13\n"
"s_mov_b32 m0, s27\n"
"s_nop 0\n"
"buffer_load_dwordx4 v14, s[4:7], s26 offen sc1 lds\n"
"s_add_u32 s26, s26, s12\n"
"s_add_u32 s27, s27, s20\n"
"v_smfmac_f32_32x32x32_bf16 v[48:63], v[92:95], v[40:47], v13\n"
"s_mov_b64 exec, s[18:19]\n"
"ds_read_b128 v[88:91], v12 offset:44800\n"
"ds_read_b128 v[92:95], v12 offset:45440\n"
"s_mov_b64 exec, -1\n"
"v_mfma_f32_32x32x8_bf16 v[32:47], v[8:9], v[68:69], 0\n"
"ds_read_b64 v[8:9], v11 offset:51456\n"
"s_mov_b32 m0, s27\n"
"s_nop 0\n"
"buffer_load_dwordx4 v14, s[4:7], s26 offen sc1 lds\n"
"s_add_u32 s26, s26, s12\n"
"s_add_u32 s27, s27, s20\n"
"v_mfma_f32_32x32x16_bf16 v[32:47], v[76:79], v[64:67], v[32:47]\n"
"ds_read_b128 v[76:79], v10 offset:51712\n"
"s_setprio 1\n"
"v_exp_f32 v16, v16\n"
"v_exp_f32 v17, v17\n"
"v_exp_f32 v18, v18\n"
"v_exp_f32 v19, v19\n"
"v_exp_f32 v20, v20\n"
"v_exp_f32 v21, v21\n"
"v_exp_f32 v22, v22\n"
"v_exp_f32 v23, v23\n"
"v_exp_f32 v24, v24\n"
"v_exp_f32 v25, v25\n"
"v_exp_f32 v26, v26\n"
"v_exp_f32 v27, v27\n"
"v_exp_f32 v28, v28\n"
"v_exp_f32 v29, v29\n"
"v_exp_f32 v30, v30\n"
"v_exp_f32 v31, v31\n"
"v_exp_f32 v32, v32\n"
"v_exp_f32 v33, v33\n"
"v_exp_f32 v34, v34\n"
"v_exp_f32 v35, v35\n"
"v_exp_f32 v36, v36\n"
"v_exp_f32 v37, v37\n"
"v_exp_f32 v38, v38\n"
"v_exp_f32 v39, v39\n"
"v_exp_f32 v40, v40\n"
"v_exp_f32 v41, v41\n"
"v_exp_f32 v42, v42\n"
"v_exp_f32 v43, v43\n"
"v_exp_f32 v44, v44\n"
"v_exp_f32 v45, v45\n"
"v_exp_f32 v46, v46\n"
"v_exp_f32 v47, v47\n"
"s_setprio 2\n"
"s_waitcnt lgkmcnt(0)\n"
"v_add_u32 v10, 0xc800, v10\n"
"v_add_u32 v11, 0xc800, v11\n"
"v_add_u32 v12, 0xc800, v12\n"
"v_smfmac_f32_32x32x32_bf16 v[48:63], v[80:83], v[16:23], v13\n"
"s_mov_b32 m0, s27\n"
"s_nop 0\n"
"buffer_load_dwordx4 v14, s[4:7], s26 offen sc1 lds\n"
"v_smfmac_f32_32x32x32_bf16 v[48:63], v[84:87], v[24:31], v13\n"
"s_mov_b64 exec, s[18:19]\n"
"ds_read_b128 v[80:83], v12\n"
"ds_read_b128 v[84:87], v12 offset:640\n"
"s_mov_b64 exec, -1\n"
"v_mfma_f32_32x32x8_bf16 v[16:31], v[70:71], v[68:69], 0\n"
"ds_read_b64 v[70:71], v11 offset:512\n"
"v_mfma_f32_32x32x16_bf16 v[16:31], v[72:75], v[64:67], v[16:31]\n"
"ds_read_b128 v[72:75], v10 offset:1024\n"
"v_smfmac_f32_32x32x32_bf16 v[48:63], v[88:91], v[32:39], v13\n"
"v_smfmac_f32_32x32x32_bf16 v[48:63], v[92:95], v[40:47], v13\n"
"s_mov_b64 exec, s[18:19]\n"
"ds_read_b128 v[88:91], v12 offset:1280\n"
"ds_read_b128 v[92:95], v12 offset:1920\n"
"s_mov_b64 exec, -1\n"
"v_mfma_f32_32x32x8_bf16 v[32:47], v[8:9], v[68:69], 0\n"
"ds_read_b64 v[8:9], v11 offset:768\n"
"v_mfma_f32_32x32x16_bf16 v[32:47], v[76:79], v[64:67], v[32:47]\n"
"ds_read_b128 v[76:79], v10 offset:1536\n"
"s_setprio 1\n"
"v_exp_f32 v16, v16\n"
"v_exp_f32 v17, v17\n"
"v_exp_f32 v18, v18\n"
"v_exp_f32 v19, v19\n"
"v_exp_f32 v20, v20\n"
"v_exp_f32 v21, v21\n"
"v_exp_f32 v22, v22\n"
"v_exp_f32 v23, v23\n"
"v_exp_f32 v24, v24\n"
"v_exp_f32 v25, v25\n"
"v_exp_f32 v26, v26\n"
"v_exp_f32 v27, v27\n"
"v_exp_f32 v28, v28\n"
"v_exp_f32 v29, v29\n"
"v_exp_f32 v30, v30\n"
"v_exp_f32 v31, v31\n"
"v_exp_f32 v32, v32\n"
"v_exp_f32 v33, v33\n"
"v_exp_f32 v34, v34\n"
"v_exp_f32 v35, v35\n"
"v_exp_f32 v36, v36\n"
"v_exp_f32 v37, v37\n"
"v_exp_f32 v38, v38\n"
"v_exp_f32 v39, v39\n"
"v_exp_f32 v40, v40\n"
"v_exp_f32 v41, v41\n"
"v_exp_f32 v42, v42\n"
"v_exp_f32 v43, v43\n"
"v_exp_f32 v44, v44\n"
"v_exp_f32 v45, v45\n"
"v_exp_f32 v46, v46\n"
"v_exp_f32 v47, v47\n"
"s_setprio 2\n"
"s_waitcnt lgkmcnt(0)\n"
"v_smfmac_f32_32x32x32_bf16 v[48:63], v[80:83], v[16:23], v13\n"
"v_smfmac_f32_32x32x32_bf16 v[48:63], v[84:87], v[24:31], v13\n"
"s_mov_b64 exec, s[18:19]\n"
"ds_read_b128 v[80:83], v12 offset:2560\n"
"ds_read_b128 v[84:87], v12 offset:3200\n"
"s_mov_b64 exec, -1\n"
"v_mfma_f32_32x32x8_bf16 v[16:31], v[70:71], v[68:69], 0\n"
"v_mfma_f32_32x32x16_bf16 v[16:31], v[72:75], v[64:67], v[16:31]\n"
"v_smfmac_f32_32x32x32_bf16 v[48:63], v[88:91], v[32:39], v13\n"
"v_smfmac_f32_32x32x32_bf16 v[48:63], v[92:95], v[40:47], v13\n"
"s_mov_b64 exec, s[18:19]\n"
"ds_read_b128 v[88:91], v12 offset:3840\n"
"ds_read_b128 v[92:95], v12 offset:4480\n"
"s_mov_b64 exec, -1\n"
"v_mfma_f32_32x32x8_bf16 v[32:47], v[8:9], v[68:69], 0\n"
"v_mfma_f32_32x32x16_bf16 v[32:47], v[76:79], v[64:67], v[32:47]\n"
"s_setprio 1\n"
"s_nop 2\n"
"v_exp_f32 v16, v16\n"
"v_exp_f32 v17, v17\n"
"v_exp_f32 v18, v18\n"
"v_exp_f32 v19, v19\n"
"v_exp_f32 v20, v20\n"
"v_exp_f32 v21, v21\n"
"v_exp_f32 v22, v22\n"
"v_exp_f32 v23, v23\n"
"v_exp_f32 v24, v24\n"
"v_exp_f32 v25, v25\n"
"v_exp_f32 v26, v26\n"
"v_exp_f32 v27, v27\n"
"v_exp_f32 v28, v28\n"
"v_exp_f32 v29, v29\n"
"v_exp_f32 v30, v30\n"
"v_exp_f32 v31, v31\n"
"v_exp_f32 v32, v32\n"
"v_exp_f32 v33, v33\n"
"v_exp_f32 v34, v34\n"
"v_exp_f32 v35, v35\n"
"v_exp_f32 v36, v36\n"
"v_exp_f32 v37, v37\n"
"v_exp_f32 v38, v38\n"
"v_exp_f32 v39, v39\n"
"v_exp_f32 v40, v40\n"
"v_exp_f32 v41, v41\n"
"v_exp_f32 v42, v42\n"
"v_exp_f32 v43, v43\n"
"v_exp_f32 v44, v44\n"
"v_exp_f32 v45, v45\n"
"v_exp_f32 v46, v46\n"
"v_exp_f32 v47, v47\n"
"s_setprio 2\n"
"s_waitcnt lgkmcnt(0)\n"
"v_smfmac_f32_32x32x32_bf16 v[48:63], v[80:83], v[16:23], v13\n"
"v_smfmac_f32_32x32x32_bf16 v[48:63], v[84:87], v[24:31], v13\n"
"v_smfmac_f32_32x32x32_bf16 v[48:63], v[88:91], v[32:39], v13\n"
"v_smfmac_f32_32x32x32_bf16 v[48:63], v[92:95], v[40:47], v13\n"
"v_mov_b32 v2, s32\n"
"v_mov_b32 v3, s33\n"
"s_mov_b32 s38, 0x30800000\n"
"s_mov_b32 s39, 0x71800000\n"
"s_nop 7\n"
"v_maximum3_f32 v4, |v48|, |v49|, |v50|\n"
"v_maximum3_f32 v5, |v51|, |v52|, |v53|\n"
"v_maximum3_f32 v8, |v54|, |v55|, |v56|\n"
"v_maximum3_f32 v9, |v57|, |v58|, |v59|\n"
"v_cmp_nlt_f32 vcc, s38, v60\n"
"v_maximum3_f32 v4, v4, v5, |v60|\n"
"v_maximum3_f32 v4, v4, v8, v9\n"
"v_cmp_ngt_f32 s[38:39], s39, v4\n"
"s_nop 0\n"
"s_or_b64 vcc, vcc, s[38:39]\n"
"s_cbranch_scc1 .Lk_slow_66\n"
".Lk_resume_67:\n"
"v_rcp_f32 v8, v60\n"
"s_nop 0\n"
"v_mul_f32 v48, v48, v8\n"
"v_mul_f32 v49, v49, v8\n"
"v_mul_f32 v50, v50, v8\n"
"v_mul_f32 v51, v51, v8\n"
"v_mul_f32 v52, v52, v8\n"
"v_mul_f32 v53, v53, v8\n"
"v_mul_f32 v54, v54, v8\n"
"v_mul_f32 v55, v55, v8\n"
"v_mul_f32 v56, v56, v8\n"
"v_mul_f32 v57, v57, v8\n"
"v_mul_f32 v58, v58, v8\n"
"v_mul_f32 v59, v59, v8\n"
"v_cvt_pk_bf16_f32 v16, v48, v49\n"
"v_cvt_pk_bf16_f32 v17, v50, v51\n"
"v_cvt_pk_bf16_f32 v18, v52, v53\n"
"v_cvt_pk_bf16_f32 v19, v54, v55\n"
"v_cvt_pk_bf16_f32 v20, v56, v57\n"
"v_cvt_pk_bf16_f32 v21, v58, v59\n"
"buffer_store_dwordx4 v[16:19], v6, s[4:7], s22 offen sc1\n"
"s_mov_b32 exec_hi, 0\n"
"buffer_store_dwordx2 v[20:21], v7, s[4:7], s22 offen sc1\n"
"s_mov_b64 exec, -1\n"
"s_setprio 0\n"
"; ==== WO\n"
"s_lshl_b32 s56, s62, 6\n"
"s_lshl_b32 s57, s3, 4\n"
"s_add_u32 s58, s56, s57\n"
"s_mul_i32 s59, s58, 0x500\n"
"s_mul_i32 s60, s63, 0xa0\n"
"s_add_u32 s42, s59, s60\n"
"v_and_b32 v4, 15, v0\n"
"v_mul_u32_u24 v5, 0x500, v4\n"
"v_lshrrev_b32 v1, 4, v0\n"
"v_lshlrev_b32 v95, 4, v1\n"
"v_add_u32 v58, v5, v95\n"
"v_and_b32 v4, 15, v0\n"
"v_mul_u32_u24 v5, 0x500, v4\n"
"v_lshrrev_b32 v1, 4, v0\n"
"v_lshlrev_b32 v95, 3, v1\n"
"v_add_u32 v59, v5, v95\n"
"v_add_u32 v6, 0xa000, v14\n"
"s_mul_i32 s56, s62, 0x14000\n"
"s_add_u32 s57, 0xa99000, s56\n"
"s_lshl_b32 s58, s3, 10\n"
"s_add_u32 s46, s57, s58\n"
"s_mul_i32 s56, s62, 0x14000\n"
"s_add_u32 s57, 0xaa3000, s56\n"
"s_lshl_b32 s58, s3, 7\n"
"s_add_u32 s45, s57, s58\n"
"s_mul_i32 s56, s62, 0x14000\n"
"s_add_u32 s57, 0xd12000, s56\n"
"s_mul_i32 s58, s63, 0x2800\n"
"s_add_u32 s59, s57, s58\n"
"s_lshl_b32 s60, s3, 8\n"
"s_add_u32 s47, s59, s60\n"
"s_lshl_b32 s56, s62, 13\n"
"s_add_u32 s57, 0xf91200, s56\n"
"s_lshl_b32 s58, s63, 10\n"
"s_add_u32 s59, s57, s58\n"
"s_lshl_b32 s60, s3, 7\n"
"s_add_u32 s48, s59, s60\n"
"s_lshl_b32 s56, s62, 3\n"
"s_add_u32 s57, s56, s63\n"
"s_lshl_b32 s58, s57, 10\n"
"s_add_u32 s49, 0xc1000, s58\n"
"s_add_u32 s50, s47, 8192\n"
"v_lshrrev_b32 v4, 4, v0\n"
"v_lshlrev_b32 v5, 10, v4\n"
"v_and_b32 v1, 15, v0\n"
"v_lshlrev_b32 v95, 4, v1\n"
"v_add_u32 v7, v5, v95\n"
"s_mul_i32 s56, s3, 0x1400\n"
"s_add_u32 s57, 0x23000, s56\n"
"v_lshlrev_b32 v4, 4, v0\n"
"v_add_u32 v15, s57, v4\n"
"v_lshrrev_b32 v4, 4, v0\n"
"v_lshlrev_b32 v5, 10, v4\n"
"v_and_b32 v1, 15, v0\n"
"v_lshlrev_b32 v95, 3, v1\n"
"v_add_u32 v56, v5, v95\n"
"s_waitcnt vmcnt(0)\n"
"s_barrier\n"
"buffer_load_dwordx4 v[8:11], v58, s[8:11], s42 offen offset:64\n"
"ds_read_b128 v[76:79], v6 offset:3072\n"
"ds_read_b128 v[80:83], v6 offset:4096\n"
"ds_read_b128 v[84:87], v6 offset:0\n"
"ds_read_b128 v[88:91], v6 offset:1024\n"
"ds_read_b128 v[92:95], v6 offset:2048\n"
"s_barrier\n"
"buffer_load_dwordx4 v[16:19], v14, s[4:7], s46 offen sc1\n"
"s_add_u32 s46, s46, 0x1000\n"
"buffer_load_dwordx4 v[20:23], v14, s[4:7], s46 offen sc1\n"
"s_add_u32 s46, s46, 0x1000\n"
"buffer_load_dwordx4 v[24:27], v14, s[4:7], s46 offen sc1\n"
"s_add_u32 s46, s46, 0x1000\n"
"buffer_load_dwordx4 v[28:31], v14, s[4:7], s46 offen sc1\n"
"s_add_u32 s46, s46, 0x1000\n"
"buffer_load_dwordx4 v[32:35], v14, s[4:7], s46 offen sc1\n"
"s_add_u32 s46, s46, 0x1000\n"
"buffer_load_dwordx4 v[36:39], v14, s[4:7], s46 offen sc1\n"
"s_add_u32 s46, s46, 0x1000\n"
"buffer_load_dwordx4 v[40:43], v14, s[4:7], s46 offen sc1\n"
"s_add_u32 s46, s46, 0x1000\n"
"buffer_load_dwordx4 v[44:47], v14, s[4:7], s46 offen sc1\n"
"buffer_load_dwordx2 v[48:49], v56, s[4:7], s45 offen offset:0 sc1\n"
"buffer_load_dwordx2 v[50:51], v56, s[4:7], s45 offen offset:512 sc1\n"
"s_add_u32 s45, s45, 0x1000\n"
"buffer_load_dwordx2 v[52:53], v56, s[4:7], s45 offen offset:0 sc1\n"
"buffer_load_dwordx2 v[54:55], v56, s[4:7], s45 offen offset:512 sc1\n"
"s_waitcnt vmcnt(11) lgkmcnt(4)\n"
"v_mfma_f32_16x16x32_bf16 v[56:59], v[76:79], v[16:19], 0\n"
"ds_read_b128 v[76:79], v6 offset:8192\n"
"s_waitcnt lgkmcnt(4)\n"
"v_mfma_f32_16x16x32_bf16 v[60:63], v[80:83], v[16:19], 0\n"
"ds_read_b128 v[80:83], v6 offset:9216\n"
"s_waitcnt lgkmcnt(4)\n"
"v_mfma_f32_16x16x32_bf16 v[64:67], v[84:87], v[16:19], 0\n"
"ds_read_b128 v[84:87], v6 offset:5120\n"
"s_waitcnt lgkmcnt(4)\n"
"v_mfma_f32_16x16x32_bf16 v[68:71], v[88:91], v[16:19], 0\n"
"ds_read_b128 v[88:91], v6 offset:6144\n"
"s_waitcnt lgkmcnt(4)\n"
"v_mfma_f32_16x16x32_bf16 v[72:75], v[92:95], v[16:19], 0\n"
"ds_read_b128 v[92:95], v6 offset:7168\n"
"s_waitcnt vmcnt(10) lgkmcnt(4)\n"
"v_mfma_f32_16x16x32_bf16 v[56:59], v[76:79], v[20:23], v[56:59]\n"
"ds_read_b128 v[76:79], v6 offset:13312\n"
"s_waitcnt lgkmcnt(4)\n"
"v_mfma_f32_16x16x32_bf16 v[60:63], v[80:83], v[20:23], v[60:63]\n"
"ds_read_b128 v[80:83], v6 offset:14336\n"
"s_waitcnt lgkmcnt(4)\n"
"v_mfma_f32_16x16x32_bf16 v[64:67], v[84:87], v[20:23], v[64:67]\n"
"ds_read_b128 v[84:87], v6 offset:10240\n"
"s_waitcnt lgkmcnt(4)\n"
"v_mfma_f32_16x16x32_bf16 v[68:71], v[88:91], v[20:23], v[68:71]\n"
"ds_read_b128 v[88:91], v6 offset:11264\n"
"s_waitcnt lgkmcnt(4)\n"
"v_mfma_f32_16x16x32_bf16 v[72:75], v[92:95], v[20:23], v[72:75]\n"
"ds_read_b128 v[92:95], v6 offset:12288\n"
"s_waitcnt vmcnt(9) lgkmcnt(4)\n"
"v_mfma_f32_16x16x32_bf16 v[56:59], v[76:79], v[24:27], v[56:59]\n"
"ds_read_b128 v[76:79], v6 offset:18432\n"
"s_waitcnt lgkmcnt(4)\n"
"v_mfma_f32_16x16x32_bf16 v[60:63], v[80:83], v[24:27], v[60:63]\n"
"ds_read_b128 v[80:83], v6 offset:19456\n"
"s_waitcnt lgkmcnt(4)\n"
"v_mfma_f32_16x16x32_bf16 v[64:67], v[84:87], v[24:27], v[64:67]\n"
"ds_read_b128 v[84:87], v6 offset:15360\n"
"s_waitcnt lgkmcnt(4)\n"
"v_mfma_f32_16x16x32_bf16 v[68:71], v[88:91], v[24:27], v[68:71]\n"
"ds_read_b128 v[88:91], v6 offset:16384\n"
"s_waitcnt lgkmcnt(4)\n"
"v_mfma_f32_16x16x32_bf16 v[72:75], v[92:95], v[24:27], v[72:75]\n"
"ds_read_b128 v[92:95], v6 offset:17408\n"
"s_waitcnt vmcnt(8) lgkmcnt(4)\n"
"v_mfma_f32_16x16x32_bf16 v[56:59], v[76:79], v[28:31], v[56:59]\n"
"ds_read_b128 v[76:79], v6 offset:23552\n"
"s_waitcnt lgkmcnt(4)\n"
"v_mfma_f32_16x16x32_bf16 v[60:63], v[80:83], v[28:31], v[60:63]\n"
"ds_read_b128 v[80:83], v6 offset:24576\n"
"s_waitcnt lgkmcnt(4)\n"
"v_mfma_f32_16x16x32_bf16 v[64:67], v[84:87], v[28:31], v[64:67]\n"
"ds_read_b128 v[84:87], v6 offset:20480\n"
"s_waitcnt lgkmcnt(4)\n"
"v_mfma_f32_16x16x32_bf16 v[68:71], v[88:91], v[28:31], v[68:71]\n"
"ds_read_b128 v[88:91], v6 offset:21504\n"
"s_waitcnt lgkmcnt(4)\n"
"v_mfma_f32_16x16x32_bf16 v[72:75], v[92:95], v[28:31], v[72:75]\n"
"ds_read_b128 v[92:95], v6 offset:22528\n"
"s_waitcnt vmcnt(7) lgkmcnt(4)\n"
"v_mfma_f32_16x16x32_bf16 v[56:59], v[76:79], v[32:35], v[56:59]\n"
"ds_read_b128 v[76:79], v6 offset:28672\n"
"s_waitcnt lgkmcnt(4)\n"
"v_mfma_f32_16x16x32_bf16 v[60:63], v[80:83], v[32:35], v[60:63]\n"
"ds_read_b128 v[80:83], v6 offset:29696\n"
"s_waitcnt lgkmcnt(4)\n"
"v_mfma_f32_16x16x32_bf16 v[64:67], v[84:87], v[32:35], v[64:67]\n"
"ds_read_b128 v[84:87], v6 offset:25600\n"
"s_waitcnt lgkmcnt(4)\n"
"v_mfma_f32_16x16x32_bf16 v[68:71], v[88:91], v[32:35], v[68:71]\n"
"ds_read_b128 v[88:91], v6 offset:26624\n"
"s_waitcnt lgkmcnt(4)\n"
"v_mfma_f32_16x16x32_bf16 v[72:75], v[92:95], v[32:35], v[72:75]\n"
"ds_read_b128 v[92:95], v6 offset:27648\n"
"s_waitcnt vmcnt(6) lgkmcnt(4)\n"
"v_mfma_f32_16x16x32_bf16 v[56:59], v[76:79], v[36:39], v[56:59]\n"
"ds_read_b128 v[76:79], v6 offset:33792\n"
"s_waitcnt lgkmcnt(4)\n"
"v_mfma_f32_16x16x32_bf16 v[60:63], v[80:83], v[36:39], v[60:63]\n"
"ds_read_b128 v[80:83], v6 offset:34816\n"
"s_waitcnt lgkmcnt(4)\n"
"v_mfma_f32_16x16x32_bf16 v[64:67], v[84:87], v[36:39], v[64:67]\n"
"ds_read_b128 v[84:87], v6 offset:30720\n"
"s_waitcnt lgkmcnt(4)\n"
"v_mfma_f32_16x16x32_bf16 v[68:71], v[88:91], v[36:39], v[68:71]\n"
"ds_read_b128 v[88:91], v6 offset:31744\n"
"s_waitcnt lgkmcnt(4)\n"
"v_mfma_f32_16x16x32_bf16 v[72:75], v[92:95], v[36:39], v[72:75]\n"
"ds_read_b128 v[92:95], v6 offset:32768\n"
"s_waitcnt vmcnt(5) lgkmcnt(4)\n"
"v_mfma_f32_16x16x32_bf16 v[56:59], v[76:79], v[40:43], v[56:59]\n"
"ds_read_b128 v[76:79], v6 offset:38912\n"
"s_waitcnt lgkmcnt(4)\n"
"v_mfma_f32_16x16x32_bf16 v[60:63], v[80:83], v[40:43], v[60:63]\n"
"ds_read_b128 v[80:83], v6 offset:39936\n"
"s_waitcnt lgkmcnt(4)\n"
"v_mfma_f32_16x16x32_bf16 v[64:67], v[84:87], v[40:43], v[64:67]\n"
"ds_read_b128 v[84:87], v6 offset:35840\n"
"s_waitcnt lgkmcnt(4)\n"
"v_mfma_f32_16x16x32_bf16 v[68:71], v[88:91], v[40:43], v[68:71]\n"
"ds_read_b128 v[88:91], v6 offset:36864\n"
"s_waitcnt lgkmcnt(4)\n"
"v_mfma_f32_16x16x32_bf16 v[72:75], v[92:95], v[40:43], v[72:75]\n"
"ds_read_b128 v[92:95], v6 offset:37888\n"
"s_waitcnt vmcnt(4) lgkmcnt(4)\n"
"v_mfma_f32_16x16x32_bf16 v[56:59], v[76:79], v[44:47], v[56:59]\n"
"ds_read_b128 v[76:79], v6 offset:54272\n"
"s_waitcnt lgkmcnt(4)\n"
"v_mfma_f32_16x16x32_bf16 v[60:63], v[80:83], v[44:47], v[60:63]\n"
"ds_read_b128 v[80:83], v6 offset:55296\n"
"s_waitcnt lgkmcnt(4)\n"
"v_mfma_f32_16x16x32_bf16 v[64:67], v[84:87], v[44:47], v[64:67]\n"
"ds_read_b128 v[84:87], v6 offset:51200\n"
"s_waitcnt lgkmcnt(4)\n"
"v_mfma_f32_16x16x32_bf16 v[68:71], v[88:91], v[44:47], v[68:71]\n"
"ds_read_b128 v[88:91], v6 offset:52224\n"
"s_waitcnt lgkmcnt(4)\n"
"v_mfma_f32_16x16x32_bf16 v[72:75], v[92:95], v[44:47], v[72:75]\n"
"ds_read_b128 v[92:95], v6 offset:53248\n"
"s_waitcnt vmcnt(2) lgkmcnt(4)\n"
"v_mfma_f32_16x16x32_bf16 v[56:59], v[76:79], v[48:51], v[56:59]\n"
"ds_read_b128 v[76:79], v6 offset:59392\n"
"s_waitcnt lgkmcnt(4)\n"
"v_mfma_f32_16x16x32_bf16 v[60:63], v[80:83], v[48:51], v[60:63]\n"
"ds_read_b128 v[80:83], v6 offset:60416\n"
"s_waitcnt lgkmcnt(4)\n"
"v_mfma_f32_16x16x32_bf16 v[64:67], v[84:87], v[48:51], v[64:67]\n"
"ds_read_b128 v[84:87], v6 offset:56320\n"
"s_waitcnt lgkmcnt(4)\n"
"v_mfma_f32_16x16x32_bf16 v[68:71], v[88:91], v[48:51], v[68:71]\n"
"ds_read_b128 v[88:91], v6 offset:57344\n"
"s_waitcnt lgkmcnt(4)\n"
"v_mfma_f32_16x16x32_bf16 v[72:75], v[92:95], v[48:51], v[72:75]\n"
"ds_read_b128 v[92:95], v6 offset:58368\n"
"s_waitcnt vmcnt(0) lgkmcnt(4)\n"
"v_mfma_f32_16x16x32_bf16 v[56:59], v[76:79], v[52:55], v[56:59]\n"
"s_waitcnt lgkmcnt(3)\n"
"v_mfma_f32_16x16x32_bf16 v[60:63], v[80:83], v[52:55], v[60:63]\n"
"s_waitcnt lgkmcnt(2)\n"
"v_mfma_f32_16x16x32_bf16 v[64:67], v[84:87], v[52:55], v[64:67]\n"
"s_waitcnt lgkmcnt(1)\n"
"v_mfma_f32_16x16x32_bf16 v[68:71], v[88:91], v[52:55], v[68:71]\n"
"s_waitcnt lgkmcnt(0)\n"
"v_mfma_f32_16x16x32_bf16 v[72:75], v[92:95], v[52:55], v[72:75]\n"
"v_lshlrev_b32 v16, 16, v8\n"
"v_and_b32 v17, 0xffff0000, v8\n"
"v_lshlrev_b32 v18, 16, v9\n"
"v_and_b32 v19, 0xffff0000, v9\n"
"v_lshlrev_b32 v20, 16, v10\n"
"v_and_b32 v21, 0xffff0000, v10\n"
"v_lshlrev_b32 v22, 16, v11\n"
"v_and_b32 v23, 0xffff0000, v11\n"
"ds_write_b128 v15, v[64:67] offset:2048\n"
"ds_write_b128 v15, v[68:71] offset:3072\n"
"ds_write_b128 v15, v[72:75] offset:4096\n"
"v_add_f32 v16, v16, v56\n"
"v_add_f32 v17, v17, v57\n"
"v_add_f32 v18, v18, v58\n"
"v_add_f32 v19, v19, v59\n"
"v_add_f32 v20, v20, v60\n"
"v_add_f32 v21, v21, v61\n"
"v_add_f32 v22, v22, v62\n"
"v_add_f32 v23, v23, v63\n"
"v_lshlrev_b32 v46, 3, v0\n"
"v_mov_b32 v2, s32\n"
"v_mov_b32 v3, s33\n"
"s_waitcnt lgkmcnt(0)\n"
"s_barrier\n"
"ds_read_b128 v[32:35], v15 offset:0\n"
"ds_read_b128 v[36:39], v15 offset:1024\n"
"s_waitcnt lgkmcnt(1)\n"
"v_pk_add_f32 v[16:17], v[16:17], v[32:33]\n"
"v_pk_add_f32 v[18:19], v[18:19], v[34:35]\n"
"s_waitcnt lgkmcnt(0)\n"
"v_pk_add_f32 v[20:21], v[20:21], v[36:37]\n"
"v_pk_add_f32 v[22:23], v[22:23], v[38:39]\n"
"v_cvt_pk_bf16_f32 v8, v16, v17\n"
"v_cvt_pk_bf16_f32 v9, v18, v19\n"
"v_cvt_pk_bf16_f32 v10, v20, v21\n"
"v_cvt_pk_bf16_f32 v11, v22, v23\n"
"buffer_store_dwordx4 v[8:11], v7, s[4:7], s47 offen sc1\n"
"v_pk_mul_f32 v[34:35], v[16:17], v[16:17]\n"
"v_pk_add_f32 v[32:33], v[16:17], v[18:19]\n"
"s_nop 0\n"
"s_cbranch_execz .Lk_tramp\n"
"s_nop 0\n"
"v_pk_fma_f32 v[34:35], v[18:19], v[18:19], v[34:35]\n"
"v_pk_fma_f32 v[34:35], v[20:21], v[20:21], v[34:35]\n"
"v_pk_add_f32 v[32:33], v[32:33], v[20:21]\n"
"v_pk_fma_f32 v[34:35], v[22:23], v[22:23], v[34:35]\n"
"v_pk_add_f32 v[32:33], v[32:33], v[22:23]\n"
"v_add_f32 v32, v32, v33\n"
"v_add_f32 v33, v34, v35\n"
"v_mov_b32 v34, v32\n"
"v_mov_b32 v35, v33\n"
"s_nop 0\n"
"v_permlane16_swap_b32 v32, v34\n"
"v_permlane16_swap_b32 v33, v35\n"
"v_pk_add_f32 v[32:33], v[32:33], v[34:35]\n"
"v_mov_b32 v34, v32\n"
"v_mov_b32 v35, v33\n"
"s_nop 0\n"
"v_permlane32_swap_b32 v32, v34\n"
"v_permlane32_swap_b32 v33, v35\n"
"v_pk_add_f32 v[32:33], v[32:33], v[34:35]\n"
"s_mov_b64 exec, 0xffff\n"
"buffer_store_dwordx2 v[32:33], v46, s[4:7], s48 offen sc1\n"
"s_mov_b64 exec, -1\n"
"v_mov_b32 v6, v7\n"
"v_lshrrev_b32 v4, 1, v0\n"
"v_and_b32 v5, 3, v4\n"
"v_lshlrev_b32 v94, 10, v5\n"
"v_and_b32 v95, 1, v0\n"
"v_lshlrev_b32 v4, 7, v95\n"
"v_add_u32 v5, v94, v4\n"
"v_lshrrev_b32 v95, 3, v0\n"
"v_lshlrev_b32 v94, 13, v95\n"
"v_add_u32 v40, v5, v94\n"
"s_mov_b32 s42, s47\n"
"s_mov_b32 s39, s48\n"
"v_mov_b32 v16, 0x3f803f80\n"
"v_mov_b32 v17, 0x3f803f80\n"
"v_mov_b32 v18, 0x3f803f80\n"
"v_mov_b32 v19, 0x3f803f80\n"
"s_lshl_b32 s47, s34, 10\n"
"s_mul_i32 s56, s62, 0x14000\n"
"s_add_u32 s57, 0xd11000, s56\n"
"s_add_u32 s46, s57, s47\n"
"v_add_u32 v7, 0x10000, v6\n"
"s_mul_i32 s56, s62, 0x36000\n"
"s_add_u32 s50, 0xfd1000, s56\n"
"s_lshl_b32 s56, s34, 3\n"
"s_add_u32 s57, s56, s63\n"
"s_lshl_b32 s58, s57, 12\n"
"s_add_u32 s48, s50, s58\n"
"s_lshl_b32 s56, s3, 12\n"
"s_add_u32 s50, s50, s56\n"
"s_mul_i32 s51, s3, 0xa000\n"
"v_and_b32 v4, 15, v0\n"
"v_lshlrev_b32 v5, 2, v4\n"
"v_add_u32 v92, 0x14000, v5\n"
"s_lshl_b32 s56, s3, 6\n"
"v_add_u32 v93, s56, v92\n"
"v_lshlrev_b32 v88, 3, v0\n"
"s_lshl_b32 s56, s62, 3\n"
"s_lshl_b32 s57, s56, 10\n"
"s_add_u32 s44, 0xc1000, s57\n"
"s_lshl_b32 s56, s62, 6\n"
"s_lshl_b32 s57, s34, 3\n"
"s_add_u32 s58, s56, s57\n"
"s_add_u32 s59, s58, s63\n"
"s_lshl_b32 s60, s59, 10\n"
"s_add_u32 s45, 0x101000, s60\n"
"s_lshl_b32 s56, s62, 6\n"
"s_add_u32 s57, s56, s3\n"
"s_lshl_b32 s58, s57, 10\n"
"s_add_u32 s43, 0x101000, s58\n"
"s_waitcnt vmcnt(0)\n"
"s_barrier\n"
"s_cmp_lg_u32 s34, 7\n"
"s_cbranch_scc1 .Lk_np_68\n"
"s_mov_b64 exec, 0xff\n"
"buffer_store_dwordx4 v[2:5], v14, s[4:7], s49 offen sc1\n"
"s_mov_b64 exec, -1\n"
".Lk_np_68:\n"
"s_cmp_eq_u32 s3, 3\n"
"s_cbranch_scc1 .Lk_sb3_69\n"
"s_mul_i32 s56, s3, 0x1800\n"
"s_add_u32 s12, 0x14c00, s56\n"
"s_add_u32 m0, s12, 0\n"
"s_add_u32 s38, s25, 126976\n"
"buffer_load_dwordx4 v14, s[4:7], s38 offen sc1 lds\n"
"s_add_u32 m0, s12, 2048\n"
"s_add_u32 s38, s25, 147456\n"
"buffer_load_dwordx4 v14, s[4:7], s38 offen sc1 lds\n"
"s_add_u32 m0, s12, 4096\n"
"s_add_u32 s38, s25, 167936\n"
"buffer_load_dwordx4 v14, s[4:7], s38 offen sc1 lds\n"
"s_add_u32 m0, s12, 59392\n"
"s_add_u32 s38, s25, 188416\n"
"buffer_load_dwordx4 v14, s[4:7], s38 offen sc1 lds\n"
"s_add_u32 m0, s12, 61440\n"
"s_add_u32 s38, s25, 208896\n"
"buffer_load_dwordx4 v14, s[4:7], s38 offen sc1 lds\n"
"s_add_u32 m0, s12, 63488\n"
"s_add_u32 s38, s25, 229376\n"
"buffer_load_dwordx4 v14, s[4:7], s38 offen sc1 lds\n"
"s_branch .Lk_sbe_70\n"
".Lk_sb3_69:\n"
".Lk_sbe_70:\n"
"s_cmp_lg_u32 s34, 7\n"
"s_cbranch_scc1 .Lk_np_71\n"
"v_and_b32 v2, 7, v0\n"
"v_lshlrev_b32 v42, 10, v2\n"
"buffer_load_dwordx2 v[20:21], v42, s[4:7], s44 offen sc1\n"
"s_waitcnt vmcnt(0)\n"
"v_cmp_ne_u64 vcc, s[32:33], v[20:21]\n"
"s_nop 0\n"
"s_cmp_eq_u64 vcc, 0\n"
"s_cbranch_scc1 .Lk_polld_74\n"
".Lk_poll_73:\n"
"buffer_load_dwordx2 v[20:21], v42, s[4:7], s44 offen sc1\n"
"s_waitcnt vmcnt(0)\n"
"v_cmp_ne_u64 vcc, s[32:33], v[20:21]\n"
"s_nop 0\n"
"s_cmp_eq_u64 vcc, 0\n"
"s_cbranch_scc1 .Lk_polld_74\n"
"s_sub_u32 s37, s37, 1\n"
"s_cmp_gt_i32 s37, 0\n"
"s_cbranch_scc1 .Lk_poll_73\n"
".Lk_polld_74:\n"
"s_branch .Lk_np2_72\n"
".Lk_np_71:\n"
"v_mfma_f32_16x16x32_bf16 v[80:83], v[96:99], v[16:19], 0\n"
"v_mfma_f32_16x16x32_bf16 v[84:87], v[100:103], v[16:19], 0\n"
"v_mfma_f32_16x16x32_bf16 v[80:83], v[104:107], v[16:19], v[80:83]\n"
"v_mfma_f32_16x16x32_bf16 v[84:87], v[108:111], v[16:19], v[84:87]\n"
"v_mfma_f32_16x16x32_bf16 v[80:83], v[112:115], v[16:19], v[80:83]\n"
"v_mfma_f32_16x16x32_bf16 v[84:87], v[116:119], v[16:19], v[84:87]\n"
"v_mfma_f32_16x16x32_bf16 v[80:83], v[120:123], v[16:19], v[80:83]\n"
"v_mfma_f32_16x16x32_bf16 v[84:87], v[124:127], v[16:19], v[84:87]\n"
"v_mfma_f32_16x16x32_bf16 v[80:83], v[128:131], v[16:19], v[80:83]\n"
"v_mfma_f32_16x16x32_bf16 v[84:87], v[132:135], v[16:19], v[84:87]\n"
"v_mfma_f32_16x16x32_bf16 v[80:83], v[136:139], v[16:19], v[80:83]\n"
"v_mfma_f32_16x16x32_bf16 v[84:87], v[140:143], v[16:19], v[84:87]\n"
"v_mfma_f32_16x16x32_bf16 v[80:83], v[144:147], v[16:19], v[80:83]\n"
"v_mfma_f32_16x16x32_bf16 v[84:87], v[148:151], v[16:19], v[84:87]\n"
"v_mfma_f32_16x16x32_bf16 v[80:83], v[152:155], v[16:19], v[80:83]\n"
"v_mfma_f32_16x16x32_bf16 v[84:87], v[156:159], v[16:19], v[84:87]\n"
"v_mfma_f32_16x16x32_bf16 v[80:83], v[160:163], v[16:19], v[80:83]\n"
"v_mfma_f32_16x16x32_bf16 v[84:87], v[164:167], v[16:19], v[84:87]\n"
"v_mfma_f32_16x16x32_bf16 v[80:83], v[168:171], v[16:19], v[80:83]\n"
"v_mfma_f32_16x16x32_bf16 v[84:87], v[172:175], v[16:19], v[84:87]\n"
"v_mfma_f32_16x16x32_bf16 v[80:83], v[176:179], v[16:19], v[80:83]\n"
"v_mfma_f32_16x16x32_bf16 v[84:87], v[180:183], v[16:19], v[84:87]\n"
"v_mfma_f32_16x16x32_bf16 v[80:83], v[184:187], v[16:19], v[80:83]\n"
"v_mfma_f32_16x16x32_bf16 v[84:87], v[188:191], v[16:19], v[84:87]\n"
"v_mfma_f32_16x16x32_bf16 v[80:83], v[192:195], v[16:19], v[80:83]\n"
"v_mfma_f32_16x16x32_bf16 v[84:87], v[196:199], v[16:19], v[84:87]\n"
"v_mfma_f32_16x16x32_bf16 v[80:83], v[200:203], v[16:19], v[80:83]\n"
"v_mfma_f32_16x16x32_bf16 v[84:87], v[204:207], v[16:19], v[84:87]\n"
"v_mfma_f32_16x16x32_bf16 v[80:83], v[208:211], v[16:19], v[80:83]\n"
"v_mfma_f32_16x16x32_bf16 v[84:87], v[212:215], v[16:19], v[84:87]\n"
"v_mfma_f32_16x16x32_bf16 v[80:83], v[216:219], v[16:19], v[80:83]\n"
"v_mfma_f32_16x16x32_bf16 v[84:87], v[220:223], v[16:19], v[84:87]\n"
"v_mfma_f32_16x16x32_bf16 v[80:83], v[224:227], v[16:19], v[80:83]\n"
"v_mfma_f32_16x16x32_bf16 v[84:87], v[228:231], v[16:19], v[84:87]\n"
"v_mfma_f32_16x16x32_bf16 v[80:83], v[232:235], v[16:19], v[80:83]\n"
"v_mfma_f32_16x16x32_bf16 v[84:87], v[236:239], v[16:19], v[84:87]\n"
"v_mfma_f32_16x16x32_bf16 v[80:83], v[240:243], v[16:19], v[80:83]\n"
"v_mfma_f32_16x16x32_bf16 v[84:87], v[244:247], v[16:19], v[84:87]\n"
"v_mfma_f32_16x16x32_bf16 v[80:83], v[248:251], v[16:19], v[80:83]\n"
"v_mfma_f32_16x16x32_bf16 v[84:87], v[252:255], v[16:19], v[84:87]\n"
".Lk_np2_72:\n"
"s_barrier\n"
"s_mov_b32 m0, s47\n"
"s_nop 0\n"
"buffer_load_dwordx4 v14, s[4:7], s46 offen sc1 lds\n"
"s_add_u32 s46, s46, 0x2000\n"
"s_add_u32 s47, s47, 0x2000\n"
"s_mov_b32 m0, s47\n"
"s_nop 0\n"
"buffer_load_dwordx4 v14, s[4:7], s46 offen sc1 lds\n"
"s_add_u32 s46, s46, 0x2000\n"
"s_add_u32 s47, s47, 0x2000\n"
"s_mov_b32 m0, s47\n"
"s_nop 0\n"
"buffer_load_dwordx4 v14, s[4:7], s46 offen sc1 lds\n"
"s_add_u32 s46, s46, 0x2000\n"
"s_add_u32 s47, s47, 0x2000\n"
"s_mov_b32 m0, s47\n"
"s_nop 0\n"
"buffer_load_dwordx4 v14, s[4:7], s46 offen sc1 lds\n"
"s_add_u32 s46, s46, 0x2000\n"
"s_add_u32 s47, s47, 0x2000\n"
"s_mov_b32 m0, s47\n"
"s_nop 0\n"
"buffer_load_dwordx4 v14, s[4:7], s46 offen sc1 lds\n"
"s_add_u32 s46, s46, 0x2000\n"
"s_add_u32 s47, s47, 0x2000\n"
"v_lshrrev_b32 v2, 4, v0\n"
"v_lshlrev_b32 v3, 11, v2\n"
"v_and_b32 v4, 15, v0\n"
"v_lshlrev_b32 v5, 3, v4\n"
"v_add_u32 v88, v3, v5\n"
"s_lshl_b32 s56, s62, 13\n"
"s_add_u32 s57, 0xf91000, s56\n"
"s_lshl_b32 s58, s3, 7\n"
"s_add_u32 s44, s57, s58\n"
"buffer_load_dwordx2 v[48:49], v88, s[4:7], s44 offen offset:0 sc1\n"
"buffer_load_dwordx2 v[50:51], v88, s[4:7], s44 offen offset:512 sc1\n"
"buffer_load_dwordx2 v[52:53], v88, s[4:7], s44 offen offset:1024 sc1\n"
"buffer_load_dwordx2 v[54:55], v88, s[4:7], s44 offen offset:1536 sc1\n"
"s_waitcnt vmcnt(7)\n"
"s_barrier\n"
"s_mov_b32 m0, s47\n"
"s_nop 0\n"
"buffer_load_dwordx4 v14, s[4:7], s46 offen sc1 lds\n"
"s_add_u32 s46, s46, 0x2000\n"
"s_add_u32 s47, s47, 0x2000\n"
"s_mov_b32 m0, s47\n"
"s_nop 0\n"
"buffer_load_dwordx4 v14, s[4:7], s46 offen sc1 lds\n"
"s_add_u32 s46, s46, 0x2000\n"
"s_add_u32 s47, s47, 0x2000\n"
"s_mov_b32 m0, s47\n"
"s_nop 0\n"
"buffer_load_dwordx4 v14, s[4:7], s46 offen sc1 lds\n"
"s_add_u32 s46, s46, 0x2000\n"
"s_add_u32 s47, s47, 0x2000\n"
"s_mov_b32 m0, s47\n"
"s_nop 0\n"
"buffer_load_dwordx4 v14, s[4:7], s46 offen sc1 lds\n"
"s_add_u32 s46, s46, 0x2000\n"
"s_add_u32 s47, s47, 0x2000\n"
"s_mov_b32 m0, s47\n"
"s_nop 0\n"
"buffer_load_dwordx4 v14, s[4:7], s46 offen sc1 lds\n"
"s_waitcnt vmcnt(9)\n"
"s_barrier\n"
"s_waitcnt vmcnt(7)\n"
"v_add_f32 v48, v48, v50\n"
"v_add_f32 v49, v49, v51\n"
"s_waitcnt vmcnt(6)\n"
"v_add_f32 v48, v48, v52\n"
"v_add_f32 v49, v49, v53\n"
"s_waitcnt vmcnt(5)\n"
"v_add_f32 v48, v48, v54\n"
"v_add_f32 v49, v49, v55\n"
"v_mov_b32 v50, v48\n"
"v_mov_b32 v51, v49\n"
"s_nop 0\n"
"v_permlane16_swap_b32 v48, v50\n"
"v_permlane16_swap_b32 v49, v51\n"
"v_add_f32 v48, v48, v50\n"
"v_add_f32 v49, v49, v51\n"
"v_mov_b32 v50, v48\n"
"v_mov_b32 v51, v49\n"
"s_nop 0\n"
"v_permlane32_swap_b32 v48, v50\n"
"v_permlane32_swap_b32 v49, v51\n"
"v_add_f32 v48, v48, v50\n"
"v_add_f32 v49, v49, v51\n"
"v_mul_f32 v48, 0x3acccccd, v48\n"
"v_mul_f32 v49, 0x3acccccd, v49\n"
"v_fma_f32 v49, -v48, v48, v49\n"
"v_maximum3_f32 v49, v49, 0, 0\n"
"v_add_f32 v49, 0x3727c5ac, v49\n"
"v_rsq_f32 v49, v49\n"
"s_lshl_b32 s56, s3, 6\n"
"s_add_u32 s57, 0x14000, s56\n"
"v_and_b32 v2, 15, v0\n"
"v_lshlrev_b32 v3, 2, v2\n"
"v_add_u32 v88, s57, v3\n"
"ds_write_b32 v88, v48\n"
"ds_write_b32 v88, v49 offset:256\n"
"v_mov_b32 v50, 0\n"
"ds_write_b32 v88, v50 offset:512\n"
"s_waitcnt lgkmcnt(0)\n"
".Lk_w7b_75:\n"
"s_waitcnt vmcnt(0)\n"
"s_barrier\n"
"ds_read_b32 v88, v92 offset:0\n"
"ds_read_b32 v89, v92 offset:64\n"
"ds_read_b32 v90, v92 offset:128\n"
"ds_read_b32 v91, v92 offset:192\n"
"ds_read_b32 v15, v93 offset:256\n"
"s_mul_i32 s56, s3, 0x2800\n"
"s_add_u32 s57, 0x19000, s56\n"
"v_add_u32 v94, s57, v14\n"
"s_lshl_b32 s56, s3, 10\n"
"s_add_u32 s57, 0x20800, s56\n"
"v_add_u32 v95, s57, v14\n"
"s_mul_i32 s56, s3, 0x1800\n"
"s_add_u32 s57, 0x14800, s56\n"
"v_add_u32 v5, s57, v14\n"
"s_lshl_b32 s56, s34, 3\n"
"s_add_u32 s38, s56, s63\n"
"s_cmp_ge_u32 s38, 54\n"
"s_cbranch_scc1 .Lk_noft_76\n"
"ds_read_b128 v[16:19], v6 offset:0\n"
"ds_read_b128 v[20:23], v6 offset:256\n"
"ds_read_b128 v[24:27], v6 offset:512\n"
"ds_read_b128 v[28:31], v6 offset:768\n"
"s_waitcnt lgkmcnt(8)\n"
"v_mul_f32_e64 v48, -v88, v80\n"
"v_mul_f32_e64 v49, -v88, v81\n"
"v_mul_f32_e64 v50, -v88, v82\n"
"v_mul_f32_e64 v51, -v88, v83\n"
"v_mul_f32_e64 v52, -v88, v84\n"
"v_mul_f32_e64 v53, -v88, v85\n"
"v_mul_f32_e64 v54, -v88, v86\n"
"v_mul_f32_e64 v55, -v88, v87\n"
"s_waitcnt lgkmcnt(7)\n"
"v_mul_f32_e64 v56, -v89, v80\n"
"v_mul_f32_e64 v57, -v89, v81\n"
"v_mul_f32_e64 v58, -v89, v82\n"
"v_mul_f32_e64 v59, -v89, v83\n"
"v_mul_f32_e64 v60, -v89, v84\n"
"v_mul_f32_e64 v61, -v89, v85\n"
"v_mul_f32_e64 v62, -v89, v86\n"
"v_mul_f32_e64 v63, -v89, v87\n"
"s_waitcnt lgkmcnt(6)\n"
"v_mul_f32_e64 v64, -v90, v80\n"
"v_mul_f32_e64 v65, -v90, v81\n"
"v_mul_f32_e64 v66, -v90, v82\n"
"v_mul_f32_e64 v67, -v90, v83\n"
"v_mul_f32_e64 v68, -v90, v84\n"
"v_mul_f32_e64 v69, -v90, v85\n"
"v_mul_f32_e64 v70, -v90, v86\n"
"v_mul_f32_e64 v71, -v90, v87\n"
"s_waitcnt lgkmcnt(5)\n"
"v_mul_f32_e64 v72, -v91, v80\n"
"v_mul_f32_e64 v73, -v91, v81\n"
"v_mul_f32_e64 v74, -v91, v82\n"
"v_mul_f32_e64 v75, -v91, v83\n"
"v_mul_f32_e64 v76, -v91, v84\n"
"v_mul_f32_e64 v77, -v91, v85\n"
"v_mul_f32_e64 v78, -v91, v86\n"
"v_mul_f32_e64 v79, -v91, v87\n"
"s_waitcnt lgkmcnt(3)\n"
"v_mfma_f32_16x16x32_bf16 v[48:51], v[96:99], v[16:19], v[48:51]\n"
"ds_read_b128 v[32:35], v6 offset:4096\n"
"v_mfma_f32_16x16x32_bf16 v[52:55], v[100:103], v[16:19], v[52:55]\n"
"ds_read_b128 v[36:39], v6 offset:4352\n"
"s_waitcnt lgkmcnt(4)\n"
"v_mfma_f32_16x16x32_bf16 v[60:63], v[100:103], v[20:23], v[60:63]\n"
"ds_read_b128 v[40:43], v6 offset:4608\n"
"v_mfma_f32_16x16x32_bf16 v[56:59], v[96:99], v[20:23], v[56:59]\n"
"ds_read_b128 v[44:47], v6 offset:4864\n"
"s_waitcnt lgkmcnt(5)\n"
"v_mfma_f32_16x16x32_bf16 v[64:67], v[96:99], v[24:27], v[64:67]\n"
"v_mfma_f32_16x16x32_bf16 v[68:71], v[100:103], v[24:27], v[68:71]\n"
"s_waitcnt lgkmcnt(4)\n"
"v_mfma_f32_16x16x32_bf16 v[76:79], v[100:103], v[28:31], v[76:79]\n"
"v_mfma_f32_16x16x32_bf16 v[72:75], v[96:99], v[28:31], v[72:75]\n"
"s_waitcnt lgkmcnt(0)\n"
"v_mfma_f32_16x16x32_bf16 v[48:51], v[104:107], v[32:35], v[48:51]\n"
"ds_read_b128 v[16:19], v6 offset:8192\n"
"v_mfma_f32_16x16x32_bf16 v[52:55], v[108:111], v[32:35], v[52:55]\n"
"ds_read_b128 v[20:23], v6 offset:8448\n"
"v_mfma_f32_16x16x32_bf16 v[60:63], v[108:111], v[36:39], v[60:63]\n"
"ds_read_b128 v[24:27], v6 offset:8704\n"
"v_mfma_f32_16x16x32_bf16 v[56:59], v[104:107], v[36:39], v[56:59]\n"
"ds_read_b128 v[28:31], v6 offset:8960\n"
"v_mfma_f32_16x16x32_bf16 v[64:67], v[104:107], v[40:43], v[64:67]\n"
"ds_read_b128 v[96:99], v94 offset:0\n"
"v_mfma_f32_16x16x32_bf16 v[68:71], v[108:111], v[40:43], v[68:71]\n"
"v_mfma_f32_16x16x32_bf16 v[76:79], v[108:111], v[44:47], v[76:79]\n"
"v_mfma_f32_16x16x32_bf16 v[72:75], v[104:107], v[44:47], v[72:75]\n"
"ds_read_b128 v[100:103], v94 offset:1024\n"
"s_waitcnt lgkmcnt(2)\n"
"v_mfma_f32_16x16x32_bf16 v[48:51], v[112:115], v[16:19], v[48:51]\n"
"ds_read_b128 v[32:35], v6 offset:12288\n"
"v_mfma_f32_16x16x32_bf16 v[52:55], v[116:119], v[16:19], v[52:55]\n"
"ds_read_b128 v[36:39], v6 offset:12544\n"
"v_mfma_f32_16x16x32_bf16 v[60:63], v[116:119], v[20:23], v[60:63]\n"
"ds_read_b128 v[40:43], v6 offset:12800\n"
"v_mfma_f32_16x16x32_bf16 v[56:59], v[112:115], v[20:23], v[56:59]\n"
"ds_read_b128 v[44:47], v6 offset:13056\n"
"v_mfma_f32_16x16x32_bf16 v[64:67], v[112:115], v[24:27], v[64:67]\n"
"ds_read_b128 v[104:107], v94 offset:2048\n"
"v_mfma_f32_16x16x32_bf16 v[68:71], v[116:119], v[24:27], v[68:71]\n"
"v_mfma_f32_16x16x32_bf16 v[76:79], v[116:119], v[28:31], v[76:79]\n"
"v_mfma_f32_16x16x32_bf16 v[72:75], v[112:115], v[28:31], v[72:75]\n"
"ds_read_b128 v[108:111], v94 offset:3072\n"
"s_waitcnt lgkmcnt(2)\n"
"v_mfma_f32_16x16x32_bf16 v[48:51], v[120:123], v[32:35], v[48:51]\n"
"ds_read_b128 v[16:19], v6 offset:16384\n"
"v_mfma_f32_16x16x32_bf16 v[52:55], v[124:127], v[32:35], v[52:55]\n"
"ds_read_b128 v[20:23], v6 offset:16640\n"
"v_mfma_f32_16x16x32_bf16 v[60:63], v[124:127], v[36:39], v[60:63]\n"
"ds_read_b128 v[24:27], v6 offset:16896\n"
"v_mfma_f32_16x16x32_bf16 v[56:59], v[120:123], v[36:39], v[56:59]\n"
"ds_read_b128 v[28:31], v6 offset:17152\n"
"v_mfma_f32_16x16x32_bf16 v[64:67], v[120:123], v[40:43], v[64:67]\n"
"ds_read_b128 v[112:115], v94 offset:4096\n"
"v_mfma_f32_16x16x32_bf16 v[68:71], v[124:127], v[40:43], v[68:71]\n"
"v_mfma_f32_16x16x32_bf16 v[76:79], v[124:127], v[44:47], v[76:79]\n"
"v_mfma_f32_16x16x32_bf16 v[72:75], v[120:123], v[44:47], v[72:75]\n"
"ds_read_b128 v[116:119], v94 offset:5120\n"
"s_waitcnt lgkmcnt(2)\n"
"v_mfma_f32_16x16x32_bf16 v[48:51], v[128:131], v[16:19], v[48:51]\n"
"ds_read_b128 v[32:35], v6 offset:20480\n"
"v_mfma_f32_16x16x32_bf16 v[52:55], v[132:135], v[16:19], v[52:55]\n"
"ds_read_b128 v[36:39], v6 offset:20736\n"
"v_mfma_f32_16x16x32_bf16 v[60:63], v[132:135], v[20:23], v[60:63]\n"
"ds_read_b128 v[40:43], v6 offset:20992\n"
"v_mfma_f32_16x16x32_bf16 v[56:59], v[128:131], v[20:23], v[56:59]\n"
"ds_read_b128 v[44:47], v6 offset:21248\n"
"v_mfma_f32_16x16x32_bf16 v[64:67], v[128:131], v[24:27], v[64:67]\n"
"ds_read_b128 v[120:123], v94 offset:6144\n"
"v_mfma_f32_16x16x32_bf16 v[68:71], v[132:135], v[24:27], v[68:71]\n"
"v_mfma_f32_16x16x32_bf16 v[76:79], v[132:135], v[28:31], v[76:79]\n"
"v_mfma_f32_16x16x32_bf16 v[72:75], v[128:131], v[28:31], v[72:75]\n"
"ds_read_b128 v[124:127], v94 offset:7168\n"
"s_waitcnt lgkmcnt(2)\n"
"v_mfma_f32_16x16x32_bf16 v[48:51], v[136:139], v[32:35], v[48:51]\n"
"ds_read_b128 v[16:19], v6 offset:24576\n"
"v_mfma_f32_16x16x32_bf16 v[52:55], v[140:143], v[32:35], v[52:55]\n"
"ds_read_b128 v[20:23], v6 offset:24832\n"
"v_mfma_f32_16x16x32_bf16 v[60:63], v[140:143], v[36:39], v[60:63]\n"
"ds_read_b128 v[24:27], v6 offset:25088\n"
"v_mfma_f32_16x16x32_bf16 v[56:59], v[136:139], v[36:39], v[56:59]\n"
"ds_read_b128 v[28:31], v6 offset:25344\n"
"v_mfma_f32_16x16x32_bf16 v[64:67], v[136:139], v[40:43], v[64:67]\n"
"ds_read_b128 v[128:131], v94 offset:8192\n"
"v_mfma_f32_16x16x32_bf16 v[68:71], v[140:143], v[40:43], v[68:71]\n"
"v_mfma_f32_16x16x32_bf16 v[76:79], v[140:143], v[44:47], v[76:79]\n"
"v_mfma_f32_16x16x32_bf16 v[72:75], v[136:139], v[44:47], v[72:75]\n"
"ds_read_b128 v[132:135], v94 offset:9216\n"
"s_waitcnt lgkmcnt(2)\n"
"v_mfma_f32_16x16x32_bf16 v[48:51], v[144:147], v[16:19], v[48:51]\n"
"ds_read_b128 v[32:35], v6 offset:28672\n"
"v_mfma_f32_16x16x32_bf16 v[52:55], v[148:151], v[16:19], v[52:55]\n"
"ds_read_b128 v[36:39], v6 offset:28928\n"
"v_mfma_f32_16x16x32_bf16 v[60:63], v[148:151], v[20:23], v[60:63]\n"
"ds_read_b128 v[40:43], v6 offset:29184\n"
"v_mfma_f32_16x16x32_bf16 v[56:59], v[144:147], v[20:23], v[56:59]\n"
"ds_read_b128 v[44:47], v6 offset:29440\n"
"v_mfma_f32_16x16x32_bf16 v[64:67], v[144:147], v[24:27], v[64:67]\n"
"ds_read_b128 v[136:139], v95 offset:0\n"
"v_mfma_f32_16x16x32_bf16 v[68:71], v[148:151], v[24:27], v[68:71]\n"
"v_mfma_f32_16x16x32_bf16 v[76:79], v[148:151], v[28:31], v[76:79]\n"
"v_mfma_f32_16x16x32_bf16 v[72:75], v[144:147], v[28:31], v[72:75]\n"
"ds_read_b128 v[140:143], v95 offset:5120\n"
"s_waitcnt lgkmcnt(2)\n"
"v_mfma_f32_16x16x32_bf16 v[48:51], v[152:155], v[32:35], v[48:51]\n"
"ds_read_b128 v[16:19], v6 offset:32768\n"
"v_mfma_f32_16x16x32_bf16 v[52:55], v[156:159], v[32:35], v[52:55]\n"
"ds_read_b128 v[20:23], v6 offset:33024\n"
"v_mfma_f32_16x16x32_bf16 v[60:63], v[156:159], v[36:39], v[60:63]\n"
"ds_read_b128 v[24:27], v6 offset:33280\n"
"v_mfma_f32_16x16x32_bf16 v[56:59], v[152:155], v[36:39], v[56:59]\n"
"ds_read_b128 v[28:31], v6 offset:33536\n"
"v_mfma_f32_16x16x32_bf16 v[64:67], v[152:155], v[40:43], v[64:67]\n"
"ds_read_b128 v[144:147], v5 offset:0\n"
"v_mfma_f32_16x16x32_bf16 v[68:71], v[156:159], v[40:43], v[68:71]\n"
"v_mfma_f32_16x16x32_bf16 v[76:79], v[156:159], v[44:47], v[76:79]\n"
"v_mfma_f32_16x16x32_bf16 v[72:75], v[152:155], v[44:47], v[72:75]\n"
"ds_read_b128 v[148:151], v5 offset:1024\n"
"s_waitcnt lgkmcnt(2)\n"
"v_mfma_f32_16x16x32_bf16 v[48:51], v[160:163], v[16:19], v[48:51]\n"
"ds_read_b128 v[32:35], v6 offset:36864\n"
"v_mfma_f32_16x16x32_bf16 v[52:55], v[164:167], v[16:19], v[52:55]\n"
"ds_read_b128 v[36:39], v6 offset:37120\n"
"v_mfma_f32_16x16x32_bf16 v[60:63], v[164:167], v[20:23], v[60:63]\n"
"ds_read_b128 v[40:43], v6 offset:37376\n"
"v_mfma_f32_16x16x32_bf16 v[56:59], v[160:163], v[20:23], v[56:59]\n"
"ds_read_b128 v[44:47], v6 offset:37632\n"
"v_mfma_f32_16x16x32_bf16 v[64:67], v[160:163], v[24:27], v[64:67]\n"
"ds_read_b128 v[152:155], v5 offset:2048\n"
"v_mfma_f32_16x16x32_bf16 v[68:71], v[164:167], v[24:27], v[68:71]\n"
"v_mfma_f32_16x16x32_bf16 v[76:79], v[164:167], v[28:31], v[76:79]\n"
"v_mfma_f32_16x16x32_bf16 v[72:75], v[160:163], v[28:31], v[72:75]\n"
"ds_read_b128 v[156:159], v5 offset:3072\n"
"s_waitcnt lgkmcnt(2)\n"
"v_mfma_f32_16x16x32_bf16 v[48:51], v[168:171], v[32:35], v[48:51]\n"
"ds_read_b128 v[16:19], v6 offset:40960\n"
"v_mfma_f32_16x16x32_bf16 v[52:55], v[172:175], v[32:35], v[52:55]\n"
"ds_read_b128 v[20:23], v6 offset:41216\n"
"v_mfma_f32_16x16x32_bf16 v[60:63], v[172:175], v[36:39], v[60:63]\n"
"ds_read_b128 v[24:27], v6 offset:41472\n"
"v_mfma_f32_16x16x32_bf16 v[56:59], v[168:171], v[36:39], v[56:59]\n"
"ds_read_b128 v[28:31], v6 offset:41728\n"
"v_mfma_f32_16x16x32_bf16 v[64:67], v[168:171], v[40:43], v[64:67]\n"
"ds_read_b128 v[160:163], v5 offset:4096\n"
"v_mfma_f32_16x16x32_bf16 v[68:71], v[172:175], v[40:43], v[68:71]\n"
"v_mfma_f32_16x16x32_bf16 v[76:79], v[172:175], v[44:47], v[76:79]\n"
"v_mfma_f32_16x16x32_bf16 v[72:75], v[168:171], v[44:47], v[72:75]\n"
"ds_read_b128 v[164:167], v5 offset:5120\n"
"s_waitcnt lgkmcnt(2)\n"
"v_mfma_f32_16x16x32_bf16 v[48:51], v[176:179], v[16:19], v[48:51]\n"
"ds_read_b128 v[32:35], v6 offset:45056\n"
"v_mfma_f32_16x16x32_bf16 v[52:55], v[180:183], v[16:19], v[52:55]\n"
"ds_read_b128 v[36:39], v6 offset:45312\n"
"v_mfma_f32_16x16x32_bf16 v[60:63], v[180:183], v[20:23], v[60:63]\n"
"ds_read_b128 v[40:43], v6 offset:45568\n"
"v_mfma_f32_16x16x32_bf16 v[56:59], v[176:179], v[20:23], v[56:59]\n"
"ds_read_b128 v[44:47], v6 offset:45824\n"
"v_mfma_f32_16x16x32_bf16 v[64:67], v[176:179], v[24:27], v[64:67]\n"
"ds_read_b128 v[168:171], v5 offset:59392\n"
"v_mfma_f32_16x16x32_bf16 v[68:71], v[180:183], v[24:27], v[68:71]\n"
"v_mfma_f32_16x16x32_bf16 v[76:79], v[180:183], v[28:31], v[76:79]\n"
"v_mfma_f32_16x16x32_bf16 v[72:75], v[176:179], v[28:31], v[72:75]\n"
"ds_read_b128 v[172:175], v5 offset:60416\n"
"s_waitcnt lgkmcnt(2)\n"
"v_mfma_f32_16x16x32_bf16 v[48:51], v[184:187], v[32:35], v[48:51]\n"
"ds_read_b128 v[16:19], v6 offset:49152\n"
"v_mfma_f32_16x16x32_bf16 v[52:55], v[188:191], v[32:35], v[52:55]\n"
"ds_read_b128 v[20:23], v6 offset:49408\n"
"v_mfma_f32_16x16x32_bf16 v[60:63], v[188:191], v[36:39], v[60:63]\n"
"ds_read_b128 v[24:27], v6 offset:49664\n"
"v_mfma_f32_16x16x32_bf16 v[56:59], v[184:187], v[36:39], v[56:59]\n"
"ds_read_b128 v[28:31], v6 offset:49920\n"
"v_mfma_f32_16x16x32_bf16 v[64:67], v[184:187], v[40:43], v[64:67]\n"
"ds_read_b128 v[176:179], v5 offset:61440\n"
"v_mfma_f32_16x16x32_bf16 v[68:71], v[188:191], v[40:43], v[68:71]\n"
"v_mfma_f32_16x16x32_bf16 v[76:79], v[188:191], v[44:47], v[76:79]\n"
"v_mfma_f32_16x16x32_bf16 v[72:75], v[184:187], v[44:47], v[72:75]\n"
"ds_read_b128 v[180:183], v5 offset:62464\n"
"s_waitcnt lgkmcnt(2)\n"
"v_mfma_f32_16x16x32_bf16 v[48:51], v[192:195], v[16:19], v[48:51]\n"
"ds_read_b128 v[32:35], v6 offset:53248\n"
"v_mfma_f32_16x16x32_bf16 v[52:55], v[196:199], v[16:19], v[52:55]\n"
"ds_read_b128 v[36:39], v6 offset:53504\n"
"v_mfma_f32_16x16x32_bf16 v[60:63], v[196:199], v[20:23], v[60:63]\n"
"ds_read_b128 v[40:43], v6 offset:53760\n"
"v_mfma_f32_16x16x32_bf16 v[56:59], v[192:195], v[20:23], v[56:59]\n"
"ds_read_b128 v[44:47], v6 offset:54016\n"
"v_mfma_f32_16x16x32_bf16 v[64:67], v[192:195], v[24:27], v[64:67]\n"
"ds_read_b128 v[184:187], v5 offset:63488\n"
"v_mfma_f32_16x16x32_bf16 v[68:71], v[196:199], v[24:27], v[68:71]\n"
"v_mfma_f32_16x16x32_bf16 v[76:79], v[196:199], v[28:31], v[76:79]\n"
"v_mfma_f32_16x16x32_bf16 v[72:75], v[192:195], v[28:31], v[72:75]\n"
"ds_read_b128 v[188:191], v5 offset:64512\n"
"s_waitcnt lgkmcnt(2)\n"
"v_mfma_f32_16x16x32_bf16 v[48:51], v[200:203], v[32:35], v[48:51]\n"
"ds_read_b128 v[16:19], v6 offset:57344\n"
"v_mfma_f32_16x16x32_bf16 v[52:55], v[204:207], v[32:35], v[52:55]\n"
"ds_read_b128 v[20:23], v6 offset:57600\n"
"v_mfma_f32_16x16x32_bf16 v[60:63], v[204:207], v[36:39], v[60:63]\n"
"ds_read_b128 v[24:27], v6 offset:57856\n"
"v_mfma_f32_16x16x32_bf16 v[56:59], v[200:203], v[36:39], v[56:59]\n"
"ds_read_b128 v[28:31], v6 offset:58112\n"
"v_mfma_f32_16x16x32_bf16 v[64:67], v[200:203], v[40:43], v[64:67]\n"
"v_mfma_f32_16x16x32_bf16 v[68:71], v[204:207], v[40:43], v[68:71]\n"
"v_mfma_f32_16x16x32_bf16 v[76:79], v[204:207], v[44:47], v[76:79]\n"
"v_mfma_f32_16x16x32_bf16 v[72:75], v[200:203], v[44:47], v[72:75]\n"
"s_waitcnt lgkmcnt(0)\n"
"v_mfma_f32_16x16x32_bf16 v[48:51], v[208:211], v[16:19], v[48:51]\n"
"ds_read_b128 v[32:35], v6 offset:61440\n"
"v_mfma_f32_16x16x32_bf16 v[52:55], v[212:215], v[16:19], v[52:55]\n"
"ds_read_b128 v[36:39], v6 offset:61696\n"
"v_mfma_f32_16x16x32_bf16 v[60:63], v[212:215], v[20:23], v[60:63]\n"
"ds_read_b128 v[40:43], v6 offset:61952\n"
"v_mfma_f32_16x16x32_bf16 v[56:59], v[208:211], v[20:23], v[56:59]\n"
"ds_read_b128 v[44:47], v6 offset:62208\n"
"v_mfma_f32_16x16x32_bf16 v[64:67], v[208:211], v[24:27], v[64:67]\n"
"s_add_u32 s13, s25, 248832\n"
"buffer_load_dwordx4 v[192:195], v14, s[4:7], s13 offen sc1\n"
"v_mfma_f32_16x16x32_bf16 v[68:71], v[212:215], v[24:27], v[68:71]\n"
"v_mfma_f32_16x16x32_bf16 v[76:79], v[212:215], v[28:31], v[76:79]\n"
"v_mfma_f32_16x16x32_bf16 v[72:75], v[208:211], v[28:31], v[72:75]\n"
"buffer_load_dwordx4 v[196:199], v14, s[4:7], s13 offen offset:1024 sc1\n"
"s_waitcnt lgkmcnt(0)\n"
"v_mfma_f32_16x16x32_bf16 v[48:51], v[216:219], v[32:35], v[48:51]\n"
"ds_read_b128 v[16:19], v7 offset:0\n"
"v_mfma_f32_16x16x32_bf16 v[52:55], v[220:223], v[32:35], v[52:55]\n"
"ds_read_b128 v[20:23], v7 offset:256\n"
"v_mfma_f32_16x16x32_bf16 v[60:63], v[220:223], v[36:39], v[60:63]\n"
"ds_read_b128 v[24:27], v7 offset:512\n"
"v_mfma_f32_16x16x32_bf16 v[56:59], v[216:219], v[36:39], v[56:59]\n"
"ds_read_b128 v[28:31], v7 offset:768\n"
"v_mfma_f32_16x16x32_bf16 v[64:67], v[216:219], v[40:43], v[64:67]\n"
"s_cmp_lt_u32 s3, 2\n"
"s_cselect_b32 s25, s25, 0x7ff00000\n"
"s_add_u32 s13, s25, 269312\n"
"buffer_load_dwordx4 v[200:203], v14, s[4:7], s13 offen sc1\n"
"v_mfma_f32_16x16x32_bf16 v[68:71], v[220:223], v[40:43], v[68:71]\n"
"v_mfma_f32_16x16x32_bf16 v[76:79], v[220:223], v[44:47], v[76:79]\n"
"v_mfma_f32_16x16x32_bf16 v[72:75], v[216:219], v[44:47], v[72:75]\n"
"buffer_load_dwordx4 v[204:207], v14, s[4:7], s13 offen offset:1024 sc1\n"
"s_waitcnt lgkmcnt(0)\n"
"v_mfma_f32_16x16x32_bf16 v[48:51], v[224:227], v[16:19], v[48:51]\n"
"ds_read_b128 v[32:35], v7 offset:4096\n"
"v_mfma_f32_16x16x32_bf16 v[52:55], v[228:231], v[16:19], v[52:55]\n"
"ds_read_b128 v[36:39], v7 offset:4352\n"
"v_mfma_f32_16x16x32_bf16 v[60:63], v[228:231], v[20:23], v[60:63]\n"
"ds_read_b128 v[40:43], v7 offset:4608\n"
"v_mfma_f32_16x16x32_bf16 v[56:59], v[224:227], v[20:23], v[56:59]\n"
"ds_read_b128 v[44:47], v7 offset:4864\n"
"v_mfma_f32_16x16x32_bf16 v[64:67], v[224:227], v[24:27], v[64:67]\n"
"v_mfma_f32_16x16x32_bf16 v[68:71], v[228:231], v[24:27], v[68:71]\n"
"v_mfma_f32_16x16x32_bf16 v[76:79], v[228:231], v[28:31], v[76:79]\n"
"v_mfma_f32_16x16x32_bf16 v[72:75], v[224:227], v[28:31], v[72:75]\n"
"s_waitcnt lgkmcnt(0)\n"
"v_mfma_f32_16x16x32_bf16 v[48:51], v[232:235], v[32:35], v[48:51]\n"
"ds_read_b128 v[16:19], v7 offset:8192\n"
"v_mfma_f32_16x16x32_bf16 v[52:55], v[236:239], v[32:35], v[52:55]\n"
"ds_read_b128 v[20:23], v7 offset:8448\n"
"v_mfma_f32_16x16x32_bf16 v[60:63], v[236:239], v[36:39], v[60:63]\n"
"ds_read_b128 v[24:27], v7 offset:8704\n"
"v_mfma_f32_16x16x32_bf16 v[56:59], v[232:235], v[36:39], v[56:59]\n"
"ds_read_b128 v[28:31], v7 offset:8960\n"
"v_mfma_f32_16x16x32_bf16 v[64:67], v[232:235], v[40:43], v[64:67]\n"
"v_mfma_f32_16x16x32_bf16 v[68:71], v[236:239], v[40:43], v[68:71]\n"
"v_mfma_f32_16x16x32_bf16 v[76:79], v[236:239], v[44:47], v[76:79]\n"
"v_mfma_f32_16x16x32_bf16 v[72:75], v[232:235], v[44:47], v[72:75]\n"
"s_waitcnt lgkmcnt(0)\n"
"v_mfma_f32_16x16x32_bf16 v[48:51], v[240:243], v[16:19], v[48:51]\n"
"ds_read_b128 v[32:35], v7 offset:12288\n"
"s_nop 0\n"
"s_cbranch_execz .Lk_tramp\n"
"s_nop 0\n"
"v_mfma_f32_16x16x32_bf16 v[52:55], v[244:247], v[16:19], v[52:55]\n"
"ds_read_b128 v[36:39], v7 offset:12544\n"
"v_mfma_f32_16x16x32_bf16 v[60:63], v[244:247], v[20:23], v[60:63]\n"
"ds_read_b128 v[40:43], v7 offset:12800\n"
"v_mfma_f32_16x16x32_bf16 v[56:59], v[240:243], v[20:23], v[56:59]\n"
"ds_read_b128 v[44:47], v7 offset:13056\n"
"v_mfma_f32_16x16x32_bf16 v[64:67], v[240:243], v[24:27], v[64:67]\n"
"v_mfma_f32_16x16x32_bf16 v[68:71], v[244:247], v[24:27], v[68:71]\n"
"v_mfma_f32_16x16x32_bf16 v[76:79], v[244:247], v[28:31], v[76:79]\n"
"v_mfma_f32_16x16x32_bf16 v[72:75], v[240:243], v[28:31], v[72:75]\n"
"s_waitcnt lgkmcnt(0)\n"
"v_mfma_f32_16x16x32_bf16 v[48:51], v[248:251], v[32:35], v[48:51]\n"
"v_mfma_f32_16x16x32_bf16 v[52:55], v[252:255], v[32:35], v[52:55]\n"
"v_mfma_f32_16x16x32_bf16 v[60:63], v[252:255], v[36:39], v[60:63]\n"
"v_mfma_f32_16x16x32_bf16 v[56:59], v[248:251], v[36:39], v[56:59]\n"
"v_mfma_f32_16x16x32_bf16 v[64:67], v[248:251], v[40:43], v[64:67]\n"
"v_mfma_f32_16x16x32_bf16 v[68:71], v[252:255], v[40:43], v[68:71]\n"
"v_mfma_f32_16x16x32_bf16 v[76:79], v[252:255], v[44:47], v[76:79]\n"
"v_mfma_f32_16x16x32_bf16 v[72:75], v[248:251], v[44:47], v[72:75]\n"
"s_setprio 3\n"
"v_mov_b32 v2, s32\n"
"v_mov_b32 v3, s33\n"
"v_maximum3_f32 v48, v48, 0, 0\n"
"v_maximum3_f32 v49, v49, 0, 0\n"
"v_maximum3_f32 v50, v50, 0, 0\n"
"v_maximum3_f32 v51, v51, 0, 0\n"
"v_maximum3_f32 v52, v52, 0, 0\n"
"v_maximum3_f32 v53, v53, 0, 0\n"
"v_maximum3_f32 v54, v54, 0, 0\n"
"v_maximum3_f32 v55, v55, 0, 0\n"
"v_cvt_pk_bf16_f32 v208, v48, v49\n"
"v_cvt_pk_bf16_f32 v209, v50, v51\n"
"v_cvt_pk_bf16_f32 v210, v52, v53\n"
"v_cvt_pk_bf16_f32 v211, v54, v55\n"
"buffer_store_dwordx4 v[208:211], v6, s[4:7], s48 offen offset:0 sc1\n"
"v_maximum3_f32 v56, v56, 0, 0\n"
"v_maximum3_f32 v57, v57, 0, 0\n"
"v_maximum3_f32 v58, v58, 0, 0\n"
"v_maximum3_f32 v59, v59, 0, 0\n"
"v_maximum3_f32 v60, v60, 0, 0\n"
"v_maximum3_f32 v61, v61, 0, 0\n"
"v_maximum3_f32 v62, v62, 0, 0\n"
"v_maximum3_f32 v63, v63, 0, 0\n"
"v_cvt_pk_bf16_f32 v212, v56, v57\n"
"v_cvt_pk_bf16_f32 v213, v58, v59\n"
"v_cvt_pk_bf16_f32 v214, v60, v61\n"
"v_cvt_pk_bf16_f32 v215, v62, v63\n"
"buffer_store_dwordx4 v[212:215], v6, s[4:7], s48 offen offset:256 sc1\n"
"v_maximum3_f32 v64, v64, 0, 0\n"
"v_maximum3_f32 v65, v65, 0, 0\n"
"v_maximum3_f32 v66, v66, 0, 0\n"
"v_maximum3_f32 v67, v67, 0, 0\n"
"v_maximum3_f32 v68, v68, 0, 0\n"
"v_maximum3_f32 v69, v69, 0, 0\n"
"v_maximum3_f32 v70, v70, 0, 0\n"
"v_maximum3_f32 v71, v71, 0, 0\n"
"v_cvt_pk_bf16_f32 v216, v64, v65\n"
"v_cvt_pk_bf16_f32 v217, v66, v67\n"
"v_cvt_pk_bf16_f32 v218, v68, v69\n"
"v_cvt_pk_bf16_f32 v219, v70, v71\n"
"buffer_store_dwordx4 v[216:219], v6, s[4:7], s48 offen offset:512 sc1\n"
"v_maximum3_f32 v72, v72, 0, 0\n"
"v_maximum3_f32 v73, v73, 0, 0\n"
"v_maximum3_f32 v74, v74, 0, 0\n"
"v_maximum3_f32 v75, v75, 0, 0\n"
"v_maximum3_f32 v76, v76, 0, 0\n"
"v_maximum3_f32 v77, v77, 0, 0\n"
"v_maximum3_f32 v78, v78, 0, 0\n"
"v_maximum3_f32 v79, v79, 0, 0\n"
"v_cvt_pk_bf16_f32 v220, v72, v73\n"
"v_cvt_pk_bf16_f32 v221, v74, v75\n"
"v_cvt_pk_bf16_f32 v222, v76, v77\n"
"v_cvt_pk_bf16_f32 v223, v78, v79\n"
"buffer_store_dwordx4 v[220:223], v6, s[4:7], s48 offen offset:768 sc1\n"
"s_barrier\n"
"v_and_b32 v1, 7, v0\n"
"v_lshlrev_b32 v254, 12, v1\n"
"v_add_u32 v1, 0x8000, v254\n"
"s_sub_u32 s56, 3, s3\n"
"s_lshr_b32 s57, s56, 1\n"
"s_lshl_b32 s58, s57, 12\n"
"s_add_u32 s59, 0xc000, s58\n"
"v_min_u32 v255, s59, v1\n"
"buffer_load_dwordx2 v[252:253], v254, s[4:7], s43 offen sc1\n"
"v_lshlrev_b32 v5, 7, v0\n"
"s_waitcnt vmcnt(1)\n"
"s_mov_b64 exec, 0xff\n"
"buffer_store_dwordx4 v[2:5], v14, s[4:7], s45 offen sc1\n"
"s_mov_b64 exec, -1\n"
"s_setprio 0\n"
"s_branch .Lk_ftj_77\n"
".Lk_noft_76:\n"
"s_add_u32 s13, s25, 248832\n"
"buffer_load_dwordx4 v[192:195], v14, s[4:7], s13 offen sc1\n"
"buffer_load_dwordx4 v[196:199], v14, s[4:7], s13 offen offset:1024 sc1\n"
"s_cmp_lt_u32 s3, 2\n"
"s_cselect_b32 s25, s25, 0x7ff00000\n"
"s_add_u32 s13, s25, 269312\n"
"buffer_load_dwordx4 v[200:203], v14, s[4:7], s13 offen sc1\n"
"buffer_load_dwordx4 v[204:207], v14, s[4:7], s13 offen offset:1024 sc1\n"
"s_cmp_eq_u32 s34, 7\n"
"s_cbranch_scc1 .Lk_w7c_78\n"
"ds_read_b128 v[96:99], v94 offset:0\n"
"ds_read_b128 v[100:103], v94 offset:1024\n"
"ds_read_b128 v[104:107], v94 offset:2048\n"
"ds_read_b128 v[108:111], v94 offset:3072\n"
"ds_read_b128 v[112:115], v94 offset:4096\n"
"ds_read_b128 v[116:119], v94 offset:5120\n"
"ds_read_b128 v[120:123], v94 offset:6144\n"
"ds_read_b128 v[124:127], v94 offset:7168\n"
"ds_read_b128 v[128:131], v94 offset:8192\n"
"ds_read_b128 v[132:135], v94 offset:9216\n"
"ds_read_b128 v[136:139], v95 offset:0\n"
"ds_read_b128 v[140:143], v95 offset:5120\n"
"ds_read_b128 v[144:147], v5 offset:0\n"
"ds_read_b128 v[148:151], v5 offset:1024\n"
"ds_read_b128 v[152:155], v5 offset:2048\n"
"ds_read_b128 v[156:159], v5 offset:3072\n"
"ds_read_b128 v[160:163], v5 offset:4096\n"
"ds_read_b128 v[164:167], v5 offset:5120\n"
"ds_read_b128 v[168:171], v5 offset:59392\n"
"ds_read_b128 v[172:175], v5 offset:60416\n"
"ds_read_b128 v[176:179], v5 offset:61440\n"
"ds_read_b128 v[180:183], v5 offset:62464\n"
"ds_read_b128 v[184:187], v5 offset:63488\n"
"ds_read_b128 v[188:191], v5 offset:64512\n"
".Lk_w7c_78:\n"
"s_waitcnt lgkmcnt(0)\n"
"s_barrier\n"
"v_and_b32 v1, 7, v0\n"
"v_lshlrev_b32 v254, 12, v1\n"
"v_add_u32 v1, 0x8000, v254\n"
"s_sub_u32 s56, 3, s3\n"
"s_lshr_b32 s57, s56, 1\n"
"s_lshl_b32 s58, s57, 12\n"
"s_add_u32 s59, 0xc000, s58\n"
"v_min_u32 v255, s59, v1\n"
"buffer_load_dwordx2 v[252:253], v254, s[4:7], s43 offen sc1\n"
"s_waitcnt vmcnt(0)\n"
".Lk_ftj_77:\n"
"s_lshl_b32 s56, s3, 8\n"
"v_add_u32 v1, s56, v6\n"
"v_add_u32 v7, s51, v1\n"
"v_xor_b32 v2, 0x100, v7\n"
"v_xor_b32 v3, 0x200, v7\n"
"v_xor_b32 v4, 0x300, v7\n"
"s_waitcnt vmcnt(1)\n"
"v_cmp_ne_u64 vcc, s[32:33], v[252:253]\n"
"s_nop 0\n"
"s_cmp_eq_u64 vcc, 0\n"
"s_cbranch_scc1 .Lk_polld_80\n"
".Lk_poll_79:\n"
"s_sleep 4\n"
"buffer_load_dwordx2 v[252:253], v254, s[4:7], s43 offen sc1\n"
"s_waitcnt vmcnt(0)\n"
"v_cmp_ne_u64 vcc, s[32:33], v[252:253]\n"
"s_nop 0\n"
"s_cmp_eq_u64 vcc, 0\n"
"s_cbranch_scc1 .Lk_polld_80\n"
"s_sub_u32 s37, s37, 1\n"
"s_cmp_gt_i32 s37, 0\n"
"s_cbranch_scc1 .Lk_poll_79\n"
".Lk_polld_80:\n"
"s_add_u32 m0, s51, 8192\n"
"s_add_u32 s42, s50, 32768\n"
"buffer_load_dwordx4 v14, s[4:7], s42 offen sc1 lds\n"
"buffer_load_dwordx4 v14, s[4:7], s42 offen offset:1024 sc1 lds\n"
"buffer_load_dwordx4 v14, s[4:7], s42 offen offset:2048 sc1 lds\n"
"buffer_load_dwordx4 v14, s[4:7], s42 offen offset:3072 sc1 lds\n"
"s_add_u32 m0, s51, 12288\n"
"s_add_u32 s42, s50, 49152\n"
"buffer_load_dwordx4 v14, s[4:7], s42 offen sc1 lds\n"
"buffer_load_dwordx4 v14, s[4:7], s42 offen offset:1024 sc1 lds\n"
"buffer_load_dwordx4 v14, s[4:7], s42 offen offset:2048 sc1 lds\n"
"buffer_load_dwordx4 v14, s[4:7], s42 offen offset:3072 sc1 lds\n"
"s_setprio 0\n"
"s_barrier\n"
"ds_read_b128 v[16:19], v7 offset:0\n"
"ds_read_b128 v[20:23], v2 offset:0\n"
"ds_read_b128 v[24:27], v3 offset:0\n"
"ds_read_b128 v[28:31], v4 offset:0\n"
"s_barrier\n"
"s_waitcnt lgkmcnt(3)\n"
"v_mfma_f32_16x16x32_bf16 v[48:51], v[96:99], v[16:19], 0\n"
"ds_read_b128 v[32:35], v7 offset:4096\n"
"v_mfma_f32_16x16x32_bf16 v[52:55], v[100:103], v[16:19], 0\n"
"ds_read_b128 v[36:39], v2 offset:4096\n"
"s_add_u32 m0, s51, 16384\n"
"s_add_u32 s42, s50, 65536\n"
"buffer_load_dwordx4 v14, s[4:7], s42 offen offset:2048 sc1 lds\n"
"s_waitcnt lgkmcnt(4)\n"
"v_mfma_f32_16x16x32_bf16 v[60:63], v[100:103], v[20:23], 0\n"
"ds_read_b128 v[40:43], v3 offset:4096\n"
"v_mfma_f32_16x16x32_bf16 v[56:59], v[96:99], v[20:23], 0\n"
"ds_read_b128 v[44:47], v4 offset:4096\n"
"buffer_load_dwordx4 v14, s[4:7], s42 offen offset:3072 sc1 lds\n"
"s_waitcnt lgkmcnt(5)\n"
"v_mfma_f32_16x16x32_bf16 v[64:67], v[96:99], v[24:27], 0\n"
"v_mfma_f32_16x16x32_bf16 v[68:71], v[100:103], v[24:27], 0\n"
"s_waitcnt lgkmcnt(4)\n"
"v_mfma_f32_16x16x32_bf16 v[76:79], v[100:103], v[28:31], 0\n"
"v_mfma_f32_16x16x32_bf16 v[72:75], v[96:99], v[28:31], 0\n"
"s_waitcnt vmcnt(6)\n"
"s_barrier\n"
"s_waitcnt lgkmcnt(0)\n"
"v_mfma_f32_16x16x32_bf16 v[48:51], v[104:107], v[32:35], v[48:51]\n"
"ds_read_b128 v[16:19], v7 offset:8192\n"
"v_mfma_f32_16x16x32_bf16 v[52:55], v[108:111], v[32:35], v[52:55]\n"
"ds_read_b128 v[20:23], v2 offset:8192\n"
"buffer_load_dwordx2 v[252:253], v255, s[4:7], s43 offen sc1\n"
"v_mfma_f32_16x16x32_bf16 v[60:63], v[108:111], v[36:39], v[60:63]\n"
"ds_read_b128 v[24:27], v3 offset:8192\n"
"v_mfma_f32_16x16x32_bf16 v[56:59], v[104:107], v[36:39], v[56:59]\n"
"ds_read_b128 v[28:31], v4 offset:8192\n"
"s_add_u32 m0, s51, 20480\n"
"s_add_u32 s42, s50, 81920\n"
"buffer_load_dwordx4 v14, s[4:7], s42 offen offset:2048 sc1 lds\n"
"v_mfma_f32_16x16x32_bf16 v[64:67], v[104:107], v[40:43], v[64:67]\n"
"v_mfma_f32_16x16x32_bf16 v[68:71], v[108:111], v[40:43], v[68:71]\n"
"buffer_load_dwordx4 v14, s[4:7], s42 offen offset:3072 sc1 lds\n"
"v_mfma_f32_16x16x32_bf16 v[76:79], v[108:111], v[44:47], v[76:79]\n"
"v_mfma_f32_16x16x32_bf16 v[72:75], v[104:107], v[44:47], v[72:75]\n"
"s_waitcnt vmcnt(5)\n"
"s_barrier\n"
"s_waitcnt lgkmcnt(0)\n"
"v_mfma_f32_16x16x32_bf16 v[48:51], v[112:115], v[16:19], v[48:51]\n"
"ds_read_b128 v[32:35], v7 offset:12288\n"
"v_mfma_f32_16x16x32_bf16 v[52:55], v[116:119], v[16:19], v[52:55]\n"
"ds_read_b128 v[36:39], v2 offset:12288\n"
"s_add_u32 m0, s51, 24576\n"
"s_add_u32 s42, s50, 98304\n"
"buffer_load_dwordx4 v14, s[4:7], s42 offen offset:2048 sc1 lds\n"
"v_mfma_f32_16x16x32_bf16 v[60:63], v[116:119], v[20:23], v[60:63]\n"
"ds_read_b128 v[40:43], v3 offset:12288\n"
"v_mfma_f32_16x16x32_bf16 v[56:59], v[112:115], v[20:23], v[56:59]\n"
"ds_read_b128 v[44:47], v4 offset:12288\n"
"buffer_load_dwordx4 v14, s[4:7], s42 offen offset:3072 sc1 lds\n"
"v_mfma_f32_16x16x32_bf16 v[64:67], v[112:115], v[24:27], v[64:67]\n"
"v_mfma_f32_16x16x32_bf16 v[68:71], v[116:119], v[24:27], v[68:71]\n"
"v_mfma_f32_16x16x32_bf16 v[76:79], v[116:119], v[28:31], v[76:79]\n"
"v_mfma_f32_16x16x32_bf16 v[72:75], v[112:115], v[28:31], v[72:75]\n"
"s_waitcnt vmcnt(5)\n"
"s_barrier\n"
"s_waitcnt lgkmcnt(0)\n"
"v_mfma_f32_16x16x32_bf16 v[48:51], v[120:123], v[32:35], v[48:51]\n"
"ds_read_b128 v[16:19], v7 offset:16384\n"
"v_mfma_f32_16x16x32_bf16 v[52:55], v[124:127], v[32:35], v[52:55]\n"
"ds_read_b128 v[20:23], v2 offset:16384\n"
"s_add_u32 m0, s51, 28672\n"
"s_add_u32 s42, s50, 114688\n"
"buffer_load_dwordx4 v14, s[4:7], s42 offen offset:2048 sc1 lds\n"
"v_mfma_f32_16x16x32_bf16 v[60:63], v[124:127], v[36:39], v[60:63]\n"
"ds_read_b128 v[24:27], v3 offset:16384\n"
"v_mfma_f32_16x16x32_bf16 v[56:59], v[120:123], v[36:39], v[56:59]\n"
"ds_read_b128 v[28:31], v4 offset:16384\n"
"buffer_load_dwordx4 v14, s[4:7], s42 offen offset:3072 sc1 lds\n"
"v_mfma_f32_16x16x32_bf16 v[64:67], v[120:123], v[40:43], v[64:67]\n"
"v_mfma_f32_16x16x32_bf16 v[68:71], v[124:127], v[40:43], v[68:71]\n"
"v_mfma_f32_16x16x32_bf16 v[76:79], v[124:127], v[44:47], v[76:79]\n"
"v_mfma_f32_16x16x32_bf16 v[72:75], v[120:123], v[44:47], v[72:75]\n"
"s_waitcnt vmcnt(6)\n"
"v_cmp_ne_u64 vcc, s[32:33], v[252:253]\n"
"s_nop 0\n"
"s_cmp_eq_u64 vcc, 0\n"
"s_cbranch_scc1 .Lk_polld_83\n"
".Lk_poll_82:\n"
"s_sleep 4\n"
"buffer_load_dwordx2 v[252:253], v255, s[4:7], s43 offen sc1\n"
"s_waitcnt vmcnt(0)\n"
"v_cmp_ne_u64 vcc, s[32:33], v[252:253]\n"
"s_nop 0\n"
"s_cmp_eq_u64 vcc, 0\n"
"s_cbranch_scc1 .Lk_polld_83\n"
"s_sub_u32 s37, s37, 1\n"
"s_cmp_gt_i32 s37, 0\n"
"s_cbranch_scc1 .Lk_poll_82\n"
".Lk_polld_83:\n"
"s_waitcnt vmcnt(4)\n"
"s_barrier\n"
"s_waitcnt lgkmcnt(0)\n"
"v_mfma_f32_16x16x32_bf16 v[48:51], v[128:131], v[16:19], v[48:51]\n"
"ds_read_b128 v[32:35], v7 offset:20480\n"
"v_mfma_f32_16x16x32_bf16 v[52:55], v[132:135], v[16:19], v[52:55]\n"
"ds_read_b128 v[36:39], v2 offset:20480\n"
"s_add_u32 m0, s51, 32768\n"
"s_add_u32 s42, s50, 131072\n"
"buffer_load_dwordx4 v14, s[4:7], s42 offen offset:2048 sc1 lds\n"
"v_mfma_f32_16x16x32_bf16 v[60:63], v[132:135], v[20:23], v[60:63]\n"
"ds_read_b128 v[40:43], v3 offset:20480\n"
"v_mfma_f32_16x16x32_bf16 v[56:59], v[128:131], v[20:23], v[56:59]\n"
"ds_read_b128 v[44:47], v4 offset:20480\n"
"buffer_load_dwordx4 v14, s[4:7], s42 offen offset:3072 sc1 lds\n"
"v_mfma_f32_16x16x32_bf16 v[64:67], v[128:131], v[24:27], v[64:67]\n"
"v_mfma_f32_16x16x32_bf16 v[68:71], v[132:135], v[24:27], v[68:71]\n"
"v_mfma_f32_16x16x32_bf16 v[76:79], v[132:135], v[28:31], v[76:79]\n"
"v_mfma_f32_16x16x32_bf16 v[72:75], v[128:131], v[28:31], v[72:75]\n"
"s_waitcnt vmcnt(4)\n"
"s_barrier\n"
"s_waitcnt lgkmcnt(0)\n"
"v_mfma_f32_16x16x32_bf16 v[48:51], v[136:139], v[32:35], v[48:51]\n"
"ds_read_b128 v[16:19], v7 offset:24576\n"
"v_mfma_f32_16x16x32_bf16 v[52:55], v[140:143], v[32:35], v[52:55]\n"
"ds_read_b128 v[20:23], v2 offset:24576\n"
"s_add_u32 m0, s51, 36864\n"
"s_add_u32 s42, s50, 147456\n"
"buffer_load_dwordx4 v14, s[4:7], s42 offen offset:2048 sc1 lds\n"
"v_mfma_f32_16x16x32_bf16 v[60:63], v[140:143], v[36:39], v[60:63]\n"
"ds_read_b128 v[24:27], v3 offset:24576\n"
"v_mfma_f32_16x16x32_bf16 v[56:59], v[136:139], v[36:39], v[56:59]\n"
"ds_read_b128 v[28:31], v4 offset:24576\n"
"buffer_load_dwordx4 v14, s[4:7], s42 offen offset:3072 sc1 lds\n"
"v_mfma_f32_16x16x32_bf16 v[64:67], v[136:139], v[40:43], v[64:67]\n"
"v_mfma_f32_16x16x32_bf16 v[68:71], v[140:143], v[40:43], v[68:71]\n"
"v_mfma_f32_16x16x32_bf16 v[76:79], v[140:143], v[44:47], v[76:79]\n"
"v_mfma_f32_16x16x32_bf16 v[72:75], v[136:139], v[44:47], v[72:75]\n"
"s_waitcnt vmcnt(4)\n"
"s_barrier\n"
"s_waitcnt lgkmcnt(0)\n"
"v_mfma_f32_16x16x32_bf16 v[48:51], v[144:147], v[16:19], v[48:51]\n"
"ds_read_b128 v[32:35], v7 offset:28672\n"
"v_mfma_f32_16x16x32_bf16 v[52:55], v[148:151], v[16:19], v[52:55]\n"
"ds_read_b128 v[36:39], v2 offset:28672\n"
"s_add_u32 m0, s51, 0\n"
"s_add_u32 s42, s50, 163840\n"
"buffer_load_dwordx4 v14, s[4:7], s42 offen offset:2048 sc1 lds\n"
"v_mfma_f32_16x16x32_bf16 v[60:63], v[148:151], v[20:23], v[60:63]\n"
"ds_read_b128 v[40:43], v3 offset:28672\n"
"v_mfma_f32_16x16x32_bf16 v[56:59], v[144:147], v[20:23], v[56:59]\n"
"ds_read_b128 v[44:47], v4 offset:28672\n"
"buffer_load_dwordx4 v14, s[4:7], s42 offen offset:3072 sc1 lds\n"
"v_mfma_f32_16x16x32_bf16 v[64:67], v[144:147], v[24:27], v[64:67]\n"
"s_xor_b32 s56, s3, 1\n"
"s_mul_i32 s57, s56, 0xa000\n"
"v_mfma_f32_16x16x32_bf16 v[68:71], v[148:151], v[24:27], v[68:71]\n"
"s_add_u32 s58, s57, 0x6400\n"
"v_add_u32 v121, s58, v14\n"
"v_mfma_f32_16x16x32_bf16 v[76:79], v[148:151], v[28:31], v[76:79]\n"
"s_xor_b32 s56, s3, 2\n"
"s_mul_i32 s57, s56, 0xa000\n"
"v_mfma_f32_16x16x32_bf16 v[72:75], v[144:147], v[28:31], v[72:75]\n"
"s_add_u32 s58, s57, 0x6c00\n"
"v_add_u32 v122, s58, v14\n"
"s_waitcnt vmcnt(4)\n"
"s_barrier\n"
"s_waitcnt lgkmcnt(0)\n"
"v_mfma_f32_16x16x32_bf16 v[48:51], v[152:155], v[32:35], v[48:51]\n"
"ds_read_b128 v[16:19], v7 offset:32768\n"
"v_mfma_f32_16x16x32_bf16 v[52:55], v[156:159], v[32:35], v[52:55]\n"
"ds_read_b128 v[20:23], v2 offset:32768\n"
"s_add_u32 m0, s51, 4096\n"
"s_add_u32 s42, s50, 180224\n"
"buffer_load_dwordx4 v14, s[4:7], s42 offen offset:2048 sc1 lds\n"
"v_mfma_f32_16x16x32_bf16 v[60:63], v[156:159], v[36:39], v[60:63]\n"
"ds_read_b128 v[24:27], v3 offset:32768\n"
"v_mfma_f32_16x16x32_bf16 v[56:59], v[152:155], v[36:39], v[56:59]\n"
"ds_read_b128 v[28:31], v4 offset:32768\n"
"buffer_load_dwordx4 v14, s[4:7], s42 offen offset:3072 sc1 lds\n"
"v_mfma_f32_16x16x32_bf16 v[64:67], v[152:155], v[40:43], v[64:67]\n"
"s_xor_b32 s56, s3, 3\n"
"s_mul_i32 s57, s56, 0xa000\n"
"v_mfma_f32_16x16x32_bf16 v[68:71], v[156:159], v[40:43], v[68:71]\n"
"s_add_u32 s58, s57, 0x7400\n"
"v_add_u32 v123, s58, v14\n"
"v_mfma_f32_16x16x32_bf16 v[76:79], v[156:159], v[44:47], v[76:79]\n"
"s_add_u32 s56, s51, 0x6400\n"
"v_add_u32 v120, s56, v14\n"
"v_mfma_f32_16x16x32_bf16 v[72:75], v[152:155], v[44:47], v[72:75]\n"
"s_lshl_b32 s56, s62, 6\n"
"s_lshl_b32 s57, s3, 4\n"
"s_waitcnt vmcnt(4)\n"
"s_barrier\n"
"s_waitcnt lgkmcnt(0)\n"
"v_mfma_f32_16x16x32_bf16 v[48:51], v[160:163], v[16:19], v[48:51]\n"
"ds_read_b128 v[32:35], v7 offset:36864\n"
"v_mfma_f32_16x16x32_bf16 v[52:55], v[164:167], v[16:19], v[52:55]\n"
"ds_read_b128 v[36:39], v2 offset:36864\n"
"s_add_u32 m0, s51, 8192\n"
"s_add_u32 s42, s50, 196608\n"
"buffer_load_dwordx4 v14, s[4:7], s42 offen offset:2048 sc1 lds\n"
"v_mfma_f32_16x16x32_bf16 v[60:63], v[164:167], v[20:23], v[60:63]\n"
"ds_read_b128 v[40:43], v3 offset:36864\n"
"v_mfma_f32_16x16x32_bf16 v[56:59], v[160:163], v[20:23], v[56:59]\n"
"ds_read_b128 v[44:47], v4 offset:36864\n"
"buffer_load_dwordx4 v14, s[4:7], s42 offen offset:3072 sc1 lds\n"
"v_mfma_f32_16x16x32_bf16 v[64:67], v[160:163], v[24:27], v[64:67]\n"
"s_add_u32 s58, s56, s57\n"
"s_mul_i32 s59, s58, 0x500\n"
"v_mfma_f32_16x16x32_bf16 v[68:71], v[164:167], v[24:27], v[68:71]\n"
"s_mul_i32 s60, s63, 0xa0\n"
"s_add_u32 s46, s59, s60\n"
"v_mfma_f32_16x16x32_bf16 v[76:79], v[164:167], v[28:31], v[76:79]\n"
"v_lshrrev_b32 v1, 2, v0\n"
"v_mul_u32_u24 v126, 0x500, v1\n"
"v_mfma_f32_16x16x32_bf16 v[72:75], v[160:163], v[28:31], v[72:75]\n"
"v_and_b32 v1, 3, v0\n"
"v_lshlrev_b32 v140, 4, v1\n"
"s_waitcnt vmcnt(4)\n"
"s_barrier\n"
"s_waitcnt lgkmcnt(0)\n"
"v_mfma_f32_16x16x32_bf16 v[48:51], v[168:171], v[32:35], v[48:51]\n"
"ds_read_b128 v[16:19], v7 offset:0\n"
"v_mfma_f32_16x16x32_bf16 v[52:55], v[172:175], v[32:35], v[52:55]\n"
"ds_read_b128 v[20:23], v2 offset:0\n"
"s_cmp_ge_u32 s3, 2\n"
"s_cbranch_scc1 .Lk_d13_84\n"
"s_add_u32 m0, s51, 12288\n"
"s_add_u32 s42, s50, 212992\n"
"buffer_load_dwordx4 v14, s[4:7], s42 offen offset:2048 sc1 lds\n"
"buffer_load_dwordx4 v14, s[4:7], s42 offen offset:3072 sc1 lds\n"
".Lk_d13_84:\n"
"v_mfma_f32_16x16x32_bf16 v[60:63], v[172:175], v[36:39], v[60:63]\n"
"ds_read_b128 v[24:27], v3 offset:0\n"
"v_mfma_f32_16x16x32_bf16 v[56:59], v[168:171], v[36:39], v[56:59]\n"
"ds_read_b128 v[28:31], v4 offset:0\n"
"v_mfma_f32_16x16x32_bf16 v[64:67], v[168:171], v[40:43], v[64:67]\n"
"v_add_u32 v124, v126, v140\n"
"v_and_b32 v1, 3, v0\n"
"v_mfma_f32_16x16x32_bf16 v[68:71], v[172:175], v[40:43], v[68:71]\n"
"v_lshlrev_b32 v140, 6, v1\n"
"v_and_b32 v141, 60, v0\n"
"v_mfma_f32_16x16x32_bf16 v[76:79], v[172:175], v[44:47], v[76:79]\n"
"v_add_u32 v126, v140, v141\n"
"v_lshlrev_b32 v128, 16, v8\n"
"v_mfma_f32_16x16x32_bf16 v[72:75], v[168:171], v[44:47], v[72:75]\n"
"v_and_b32 v129, 0xffff0000, v8\n"
"v_lshlrev_b32 v130, 16, v9\n"
"s_waitcnt vmcnt(2)\n"
"s_barrier\n"
"s_waitcnt lgkmcnt(0)\n"
"v_mfma_f32_16x16x32_bf16 v[48:51], v[176:179], v[16:19], v[48:51]\n"
"ds_read_b128 v[32:35], v7 offset:4096\n"
"v_mfma_f32_16x16x32_bf16 v[52:55], v[180:183], v[16:19], v[52:55]\n"
"ds_read_b128 v[36:39], v2 offset:4096\n"
"v_mfma_f32_16x16x32_bf16 v[60:63], v[180:183], v[20:23], v[60:63]\n"
"ds_read_b128 v[40:43], v3 offset:4096\n"
"v_mfma_f32_16x16x32_bf16 v[56:59], v[176:179], v[20:23], v[56:59]\n"
"ds_read_b128 v[44:47], v4 offset:4096\n"
"v_mfma_f32_16x16x32_bf16 v[64:67], v[176:179], v[24:27], v[64:67]\n"
"v_and_b32 v131, 0xffff0000, v9\n"
"v_lshlrev_b32 v132, 16, v10\n"
"v_mfma_f32_16x16x32_bf16 v[68:71], v[180:183], v[24:27], v[68:71]\n"
"v_and_b32 v133, 0xffff0000, v10\n"
"v_lshlrev_b32 v134, 16, v11\n"
"v_mfma_f32_16x16x32_bf16 v[76:79], v[180:183], v[28:31], v[76:79]\n"
"v_and_b32 v135, 0xffff0000, v11\n"
"v_mfma_f32_16x16x32_bf16 v[72:75], v[176:179], v[28:31], v[72:75]\n"
"s_waitcnt vmcnt(0)\n"
"s_barrier\n"
"s_cmp_ge_u32 s3, 2\n"
"s_cbranch_scc1 .Lk_no13_81\n"
"s_waitcnt lgkmcnt(2)\n"
"v_mfma_f32_16x16x32_bf16 v[56:59], v[184:187], v[36:39], v[56:59]\n"
"ds_read_b128 v[16:19], v2 offset:8192\n"
"v_mfma_f32_16x16x32_bf16 v[60:63], v[188:191], v[36:39], v[60:63]\n"
"ds_read_b128 v[20:23], v2 offset:12288\n"
"s_waitcnt lgkmcnt(3)\n"
"v_mfma_f32_16x16x32_bf16 v[64:67], v[184:187], v[40:43], v[64:67]\n"
"ds_read_b128 v[24:27], v3 offset:8192\n"
"v_mfma_f32_16x16x32_bf16 v[68:71], v[188:191], v[40:43], v[68:71]\n"
"ds_read_b128 v[28:31], v3 offset:12288\n"
"s_waitcnt lgkmcnt(3)\n"
"v_mfma_f32_16x16x32_bf16 v[56:59], v[192:195], v[16:19], v[56:59]\n"
"ds_read_b128 v[36:39], v4 offset:8192\n"
"v_mfma_f32_16x16x32_bf16 v[60:63], v[196:199], v[16:19], v[60:63]\n"
"ds_read_b128 v[40:43], v4 offset:12288\n"
"s_waitcnt lgkmcnt(4)\n"
"v_mfma_f32_16x16x32_bf16 v[56:59], v[200:203], v[20:23], v[56:59]\n"
"ds_read_b128 v[16:19], v7 offset:8192\n"
"v_mfma_f32_16x16x32_bf16 v[60:63], v[204:207], v[20:23], v[60:63]\n"
"s_waitcnt lgkmcnt(4)\n"
"v_mfma_f32_16x16x32_bf16 v[64:67], v[192:195], v[24:27], v[64:67]\n"
"ds_read_b128 v[20:23], v7 offset:12288\n"
"v_mfma_f32_16x16x32_bf16 v[68:71], v[196:199], v[24:27], v[68:71]\n"
"s_waitcnt lgkmcnt(4)\n"
"v_mfma_f32_16x16x32_bf16 v[64:67], v[200:203], v[28:31], v[64:67]\n"
"ds_write_b128 v121, v[56:59] offset:0\n"
"v_mfma_f32_16x16x32_bf16 v[68:71], v[204:207], v[28:31], v[68:71]\n"
"ds_write_b128 v121, v[60:63] offset:1024\n"
"v_mfma_f32_16x16x32_bf16 v[72:75], v[184:187], v[44:47], v[72:75]\n"
"v_mfma_f32_16x16x32_bf16 v[76:79], v[188:191], v[44:47], v[76:79]\n"
"s_waitcnt lgkmcnt(5)\n"
"v_mfma_f32_16x16x32_bf16 v[72:75], v[192:195], v[36:39], v[72:75]\n"
"v_mfma_f32_16x16x32_bf16 v[76:79], v[196:199], v[36:39], v[76:79]\n"
"ds_write_b128 v122, v[64:67] offset:0\n"
"s_waitcnt lgkmcnt(5)\n"
"v_mfma_f32_16x16x32_bf16 v[72:75], v[200:203], v[40:43], v[72:75]\n"
"ds_write_b128 v122, v[68:71] offset:1024\n"
"v_mfma_f32_16x16x32_bf16 v[76:79], v[204:207], v[40:43], v[76:79]\n"
"v_mfma_f32_16x16x32_bf16 v[48:51], v[184:187], v[32:35], v[48:51]\n"
"v_mfma_f32_16x16x32_bf16 v[52:55], v[188:191], v[32:35], v[52:55]\n"
"s_waitcnt lgkmcnt(5)\n"
"v_mfma_f32_16x16x32_bf16 v[48:51], v[192:195], v[16:19], v[48:51]\n"
"v_mfma_f32_16x16x32_bf16 v[52:55], v[196:199], v[16:19], v[52:55]\n"
"s_waitcnt lgkmcnt(4)\n"
"v_mfma_f32_16x16x32_bf16 v[48:51], v[200:203], v[20:23], v[48:51]\n"
"ds_write_b128 v123, v[72:75] offset:0\n"
"v_mfma_f32_16x16x32_bf16 v[52:55], v[204:207], v[20:23], v[52:55]\n"
"ds_write_b128 v123, v[76:79] offset:1024\n"
"s_waitcnt lgkmcnt(0)\n"
"s_branch .Lk_e13_85\n"
".Lk_no13_81:\n"
"s_waitcnt lgkmcnt(2)\n"
"v_mfma_f32_16x16x32_bf16 v[56:59], v[184:187], v[36:39], v[56:59]\n"
"ds_read_b128 v[16:19], v2 offset:8192\n"
"v_mfma_f32_16x16x32_bf16 v[60:63], v[188:191], v[36:39], v[60:63]\n"
"ds_read_b128 v[20:23], v3 offset:8192\n"
"s_waitcnt lgkmcnt(3)\n"
"v_mfma_f32_16x16x32_bf16 v[64:67], v[184:187], v[40:43], v[64:67]\n"
"ds_read_b128 v[24:27], v4 offset:8192\n"
"v_mfma_f32_16x16x32_bf16 v[68:71], v[188:191], v[40:43], v[68:71]\n"
"ds_read_b128 v[28:31], v7 offset:8192\n"
"s_waitcnt lgkmcnt(3)\n"
"v_mfma_f32_16x16x32_bf16 v[56:59], v[192:195], v[16:19], v[56:59]\n"
"v_mfma_f32_16x16x32_bf16 v[60:63], v[196:199], v[16:19], v[60:63]\n"
"s_waitcnt lgkmcnt(2)\n"
"v_mfma_f32_16x16x32_bf16 v[64:67], v[192:195], v[20:23], v[64:67]\n"
"v_mfma_f32_16x16x32_bf16 v[68:71], v[196:199], v[20:23], v[68:71]\n"
"v_mfma_f32_16x16x32_bf16 v[72:75], v[184:187], v[44:47], v[72:75]\n"
"v_mfma_f32_16x16x32_bf16 v[76:79], v[188:191], v[44:47], v[76:79]\n"
"s_waitcnt lgkmcnt(1)\n"
"v_mfma_f32_16x16x32_bf16 v[72:75], v[192:195], v[24:27], v[72:75]\n"
"ds_write_b128 v121, v[56:59] offset:0\n"
"v_mfma_f32_16x16x32_bf16 v[76:79], v[196:199], v[24:27], v[76:79]\n"
"ds_write_b128 v121, v[60:63] offset:1024\n"
"v_mfma_f32_16x16x32_bf16 v[48:51], v[184:187], v[32:35], v[48:51]\n"
"ds_write_b128 v122, v[64:67] offset:0\n"
"v_mfma_f32_16x16x32_bf16 v[52:55], v[188:191], v[32:35], v[52:55]\n"
"ds_write_b128 v122, v[68:71] offset:1024\n"
"s_waitcnt lgkmcnt(4)\n"
"v_mfma_f32_16x16x32_bf16 v[48:51], v[192:195], v[28:31], v[48:51]\n"
"ds_write_b128 v123, v[72:75] offset:0\n"
"v_mfma_f32_16x16x32_bf16 v[52:55], v[196:199], v[28:31], v[52:55]\n"
"ds_write_b128 v123, v[76:79] offset:1024\n"
".Lk_e13_85:\n"
"s_waitcnt lgkmcnt(0)\n"
"s_barrier\n"
"ds_read_b128 v[56:59], v120 offset:0\n"
"ds_read_b128 v[60:63], v120 offset:1024\n"
"ds_read_b128 v[64:67], v120 offset:2048\n"
"ds_read_b128 v[68:71], v120 offset:3072\n"
"ds_read_b128 v[72:75], v120 offset:4096\n"
"ds_read_b128 v[76:79], v120 offset:5120\n"
"s_waitcnt lgkmcnt(5)\n"
"v_pk_add_f32 v[48:49], v[48:49], v[56:57]\n"
"v_pk_add_f32 v[50:51], v[50:51], v[58:59]\n"
"s_waitcnt lgkmcnt(4)\n"
"v_pk_add_f32 v[52:53], v[52:53], v[60:61]\n"
"v_pk_add_f32 v[54:55], v[54:55], v[62:63]\n"
"s_waitcnt lgkmcnt(3)\n"
"v_pk_add_f32 v[48:49], v[48:49], v[64:65]\n"
"v_pk_add_f32 v[50:51], v[50:51], v[66:67]\n"
"s_waitcnt lgkmcnt(2)\n"
"v_pk_add_f32 v[52:53], v[52:53], v[68:69]\n"
"v_pk_add_f32 v[54:55], v[54:55], v[70:71]\n"
"s_waitcnt lgkmcnt(1)\n"
"v_pk_add_f32 v[48:49], v[48:49], v[72:73]\n"
"v_pk_add_f32 v[50:51], v[50:51], v[74:75]\n"
"s_waitcnt lgkmcnt(0)\n"
"v_pk_add_f32 v[52:53], v[52:53], v[76:77]\n"
"v_pk_add_f32 v[54:55], v[54:55], v[78:79]\n"
"v_pk_fma_f32 v[128:129], v[48:49], v[14:15], v[128:129] op_sel:[0,1,0] op_sel_hi:[1,1,1]\n"
"v_pk_fma_f32 v[130:131], v[50:51], v[14:15], v[130:131] op_sel:[0,1,0] op_sel_hi:[1,1,1]\n"
"v_pk_fma_f32 v[132:133], v[52:53], v[14:15], v[132:133] op_sel:[0,1,0] op_sel_hi:[1,1,1]\n"
"v_pk_fma_f32 v[134:135], v[54:55], v[14:15], v[134:135] op_sel:[0,1,0] op_sel_hi:[1,1,1]\n"
"v_cvt_pk_bf16_f32 v32, v128, v129\n"
"v_cvt_pk_bf16_f32 v33, v130, v131\n"
"v_cvt_pk_bf16_f32 v34, v132, v133\n"
"v_cvt_pk_bf16_f32 v35, v134, v135\n"
"ds_bpermute_b32 v32, v126, v32\n"
"ds_bpermute_b32 v33, v126, v33\n"
"ds_bpermute_b32 v34, v126, v34\n"
"ds_bpermute_b32 v35, v126, v35\n"
"s_waitcnt lgkmcnt(0)\n"
"buffer_store_dwordx4 v[32:35], v124, s[28:31], s46 offen offset:64\n"
".Lk_bhe_35:\n"
".Lk_last:\n"
"s_endpgm\n"
".Lk_slow_42:\n"
"v_lshrrev_b32 v4, 5, v0\n"
"v_lshlrev_b32 v5, 11, v4\n"
"v_and_b32 v8, 31, v0\n"
"v_lshlrev_b32 v9, 4, v8\n"
"v_add_u32 v10, v5, v9\n"
"v_and_b32 v4, 31, v0\n"
"v_lshlrev_b32 v5, 3, v4\n"
"v_add_u32 v11, 0x1000, v5\n"
"v_lshrrev_b32 v4, 5, v0\n"
"v_mul_u32_u24 v5, 0x140, v4\n"
"v_add_u32 v8, 0x1400, v5\n"
"v_and_b32 v9, 19, v0\n"
"v_and_b32 v4, 4, v0\n"
"v_lshlrev_b32 v5, 1, v4\n"
"v_or_b32 v4, v9, v5\n"
"v_and_b32 v9, 8, v0\n"
"v_lshrrev_b32 v5, 1, v9\n"
"v_or_b32 v9, v4, v5\n"
"v_lshlrev_b32 v4, 4, v9\n"
"v_add_u32 v12, v8, v4\n"
"v_mov_b32 v48, 0\n"
"v_mov_b32 v49, 0\n"
"v_mov_b32 v50, 0\n"
"v_mov_b32 v51, 0\n"
"v_mov_b32 v52, 0\n"
"v_mov_b32 v53, 0\n"
"v_mov_b32 v54, 0\n"
"v_mov_b32 v55, 0\n"
"v_mov_b32 v56, 0\n"
"v_mov_b32 v57, 0\n"
"v_mov_b32 v58, 0\n"
"v_mov_b32 v59, 0\n"
"v_mov_b32 v60, 0\n"
"v_mov_b32 v61, 0\n"
"v_mov_b32 v62, 0\n"
"v_mov_b32 v63, 0\n"
"v_mov_b32 v4, 0xff61b1e6\n"
"s_mul_i32 s56, s40, 0x28000\n"
"s_add_u32 s16, 0x591000, s56\n"
"s_mov_b32 s17, 0\n"
"s_cbranch_execz .Lk_tramp\n"
"s_nop 0\n"
"s_waitcnt vmcnt(0) lgkmcnt(0)\n"
".Lk_slp_86:\n"
"s_lshr_b32 s56, s17, 2\n"
"s_mul_i32 s57, s56, 0x2800\n"
"s_add_u32 s42, s16, s57\n"
"s_and_b32 s43, s17, 3\n"
"s_lshl_b32 s56, s43, 9\n"
"s_add_u32 s44, s42, s56\n"
"s_lshl_b32 s56, s43, 8\n"
"s_add_u32 s45, s42, s56\n"
"s_mul_i32 s56, s43, 0x500\n"
"s_add_u32 s46, s42, s56\n"
"buffer_load_dwordx4 v[72:75], v10, s[4:7], s44 offen sc1\n"
"buffer_load_dwordx2 v[70:71], v11, s[4:7], s45 offen sc1\n"
"s_mov_b64 exec, s[18:19]\n"
"buffer_load_dwordx4 v[80:83], v12, s[4:7], s46 offen offset:0 sc1\n"
"buffer_load_dwordx4 v[84:87], v12, s[4:7], s46 offen offset:640 sc1\n"
"s_mov_b64 exec, -1\n"
"s_waitcnt vmcnt(2)\n"
"v_mfma_f32_32x32x8_bf16 v[16:31], v[70:71], v[68:69], 0\n"
"v_mfma_f32_32x32x16_bf16 v[16:31], v[72:75], v[64:67], v[16:31]\n"
"s_nop 11\n"
"v_max3_f32 v8, v4, v16, v17\n"
"v_max3_f32 v8, v8, v18, v19\n"
"v_max3_f32 v8, v8, v20, v21\n"
"v_max3_f32 v8, v8, v22, v23\n"
"v_max3_f32 v8, v8, v24, v25\n"
"v_max3_f32 v8, v8, v26, v27\n"
"v_max3_f32 v8, v8, v28, v29\n"
"v_max3_f32 v8, v8, v30, v31\n"
"v_mov_b32 v9, v8\n"
"s_nop 1\n"
"v_permlane32_swap_b32 v8, v9\n"
"v_max_f32 v8, v8, v9\n"
"v_sub_f32 v5, v4, v8\n"
"v_exp_f32 v5, v5\n"
"v_mov_b32 v4, v8\n"
"v_sub_f32 v16, v16, v4\n"
"v_sub_f32 v17, v17, v4\n"
"v_sub_f32 v18, v18, v4\n"
"v_sub_f32 v19, v19, v4\n"
"v_sub_f32 v20, v20, v4\n"
"v_sub_f32 v21, v21, v4\n"
"v_sub_f32 v22, v22, v4\n"
"v_sub_f32 v23, v23, v4\n"
"v_sub_f32 v24, v24, v4\n"
"v_sub_f32 v25, v25, v4\n"
"v_sub_f32 v26, v26, v4\n"
"v_sub_f32 v27, v27, v4\n"
"v_sub_f32 v28, v28, v4\n"
"v_sub_f32 v29, v29, v4\n"
"v_sub_f32 v30, v30, v4\n"
"v_sub_f32 v31, v31, v4\n"
"v_exp_f32 v16, v16\n"
"v_exp_f32 v17, v17\n"
"v_exp_f32 v18, v18\n"
"v_exp_f32 v19, v19\n"
"v_exp_f32 v20, v20\n"
"v_exp_f32 v21, v21\n"
"v_exp_f32 v22, v22\n"
"v_exp_f32 v23, v23\n"
"v_exp_f32 v24, v24\n"
"v_exp_f32 v25, v25\n"
"v_exp_f32 v26, v26\n"
"v_exp_f32 v27, v27\n"
"v_exp_f32 v28, v28\n"
"v_exp_f32 v29, v29\n"
"v_exp_f32 v30, v30\n"
"v_exp_f32 v31, v31\n"
"v_mul_f32 v48, v48, v5\n"
"v_mul_f32 v49, v49, v5\n"
"v_mul_f32 v50, v50, v5\n"
"v_mul_f32 v51, v51, v5\n"
"v_mul_f32 v52, v52, v5\n"
"v_mul_f32 v53, v53, v5\n"
"v_mul_f32 v54, v54, v5\n"
"v_mul_f32 v55, v55, v5\n"
"v_mul_f32 v56, v56, v5\n"
"v_mul_f32 v57, v57, v5\n"
"v_mul_f32 v58, v58, v5\n"
"v_mul_f32 v59, v59, v5\n"
"v_mul_f32 v60, v60, v5\n"
"v_mul_f32 v61, v61, v5\n"
"v_mul_f32 v62, v62, v5\n"
"v_mul_f32 v63, v63, v5\n"
"s_waitcnt vmcnt(1)\n"
"s_nop 0\n"
"v_smfmac_f32_32x32x32_bf16 v[48:63], v[80:83], v[16:23], v13\n"
"s_waitcnt vmcnt(0)\n"
"v_smfmac_f32_32x32x32_bf16 v[48:63], v[84:87], v[24:31], v13\n"
"s_add_u32 s17, s17, 1\n"
"s_cmp_lt_u32 s17, 64\n"
"s_nop 15\n"
"s_cbranch_scc1 .Lk_slp_86\n"
"s_nop 1\n"
"s_branch .Lk_resume_43\n"
".Lk_slow_66:\n"
"v_lshrrev_b32 v4, 5, v0\n"
"v_lshlrev_b32 v5, 11, v4\n"
"v_and_b32 v8, 31, v0\n"
"v_lshlrev_b32 v9, 4, v8\n"
"v_add_u32 v10, v5, v9\n"
"v_and_b32 v4, 31, v0\n"
"v_lshlrev_b32 v5, 3, v4\n"
"v_add_u32 v11, 0x1000, v5\n"
"v_lshrrev_b32 v4, 5, v0\n"
"v_mul_u32_u24 v5, 0x140, v4\n"
"v_add_u32 v8, 0x1400, v5\n"
"v_and_b32 v9, 19, v0\n"
"v_and_b32 v4, 4, v0\n"
"v_lshlrev_b32 v5, 1, v4\n"
"v_or_b32 v4, v9, v5\n"
"v_and_b32 v9, 8, v0\n"
"v_lshrrev_b32 v5, 1, v9\n"
"v_or_b32 v9, v4, v5\n"
"v_lshlrev_b32 v4, 4, v9\n"
"v_add_u32 v12, v8, v4\n"
"v_mov_b32 v48, 0\n"
"v_mov_b32 v49, 0\n"
"v_mov_b32 v50, 0\n"
"v_mov_b32 v51, 0\n"
"v_mov_b32 v52, 0\n"
"v_mov_b32 v53, 0\n"
"v_mov_b32 v54, 0\n"
"v_mov_b32 v55, 0\n"
"v_mov_b32 v56, 0\n"
"v_mov_b32 v57, 0\n"
"v_mov_b32 v58, 0\n"
"v_mov_b32 v59, 0\n"
"v_mov_b32 v60, 0\n"
"v_mov_b32 v61, 0\n"
"v_mov_b32 v62, 0\n"
"v_mov_b32 v63, 0\n"
"v_mov_b32 v4, 0xff61b1e6\n"
"s_mul_i32 s56, s40, 0x28000\n"
"s_add_u32 s16, 0x591000, s56\n"
"s_mov_b32 s17, 0\n"
"s_waitcnt vmcnt(0) lgkmcnt(0)\n"
".Lk_slp_87:\n"
"s_lshr_b32 s56, s17, 2\n"
"s_mul_i32 s57, s56, 0x2800\n"
"s_add_u32 s42, s16, s57\n"
"s_and_b32 s43, s17, 3\n"
"s_lshl_b32 s56, s43, 9\n"
"s_add_u32 s44, s42, s56\n"
"s_lshl_b32 s56, s43, 8\n"
"s_add_u32 s45, s42, s56\n"
"s_mul_i32 s56, s43, 0x500\n"
"s_add_u32 s46, s42, s56\n"
"buffer_load_dwordx4 v[72:75], v10, s[4:7], s44 offen sc1\n"
"buffer_load_dwordx2 v[70:71], v11, s[4:7], s45 offen sc1\n"
"s_mov_b64 exec, s[18:19]\n"
"buffer_load_dwordx4 v[80:83], v12, s[4:7], s46 offen offset:0 sc1\n"
"buffer_load_dwordx4 v[84:87], v12, s[4:7], s46 offen offset:640 sc1\n"
"s_mov_b64 exec, -1\n"
"s_waitcnt vmcnt(2)\n"
"v_mfma_f32_32x32x8_bf16 v[16:31], v[70:71], v[68:69], 0\n"
"v_mfma_f32_32x32x16_bf16 v[16:31], v[72:75], v[64:67], v[16:31]\n"
"s_nop 11\n"
"v_max3_f32 v8, v4, v16, v17\n"
"v_max3_f32 v8, v8, v18, v19\n"
"v_max3_f32 v8, v8, v20, v21\n"
"v_max3_f32 v8, v8, v22, v23\n"
"v_max3_f32 v8, v8, v24, v25\n"
"v_max3_f32 v8, v8, v26, v27\n"
"v_max3_f32 v8, v8, v28, v29\n"
"v_max3_f32 v8, v8, v30, v31\n"
"v_mov_b32 v9, v8\n"
"s_nop 1\n"
"v_permlane32_swap_b32 v8, v9\n"
"v_max_f32 v8, v8, v9\n"
"v_sub_f32 v5, v4, v8\n"
"v_exp_f32 v5, v5\n"
"v_mov_b32 v4, v8\n"
"v_sub_f32 v16, v16, v4\n"
"v_sub_f32 v17, v17, v4\n"
"v_sub_f32 v18, v18, v4\n"
"v_sub_f32 v19, v19, v4\n"
"v_sub_f32 v20, v20, v4\n"
"v_sub_f32 v21, v21, v4\n"
"v_sub_f32 v22, v22, v4\n"
"v_sub_f32 v23, v23, v4\n"
"v_sub_f32 v24, v24, v4\n"
"v_sub_f32 v25, v25, v4\n"
"v_sub_f32 v26, v26, v4\n"
"v_sub_f32 v27, v27, v4\n"
"v_sub_f32 v28, v28, v4\n"
"v_sub_f32 v29, v29, v4\n"
"v_sub_f32 v30, v30, v4\n"
"v_sub_f32 v31, v31, v4\n"
"v_exp_f32 v16, v16\n"
"v_exp_f32 v17, v17\n"
"v_exp_f32 v18, v18\n"
"v_exp_f32 v19, v19\n"
"v_exp_f32 v20, v20\n"
"v_exp_f32 v21, v21\n"
"v_exp_f32 v22, v22\n"
"v_exp_f32 v23, v23\n"
"v_exp_f32 v24, v24\n"
"v_exp_f32 v25, v25\n"
"v_exp_f32 v26, v26\n"
"v_exp_f32 v27, v27\n"
"v_exp_f32 v28, v28\n"
"v_exp_f32 v29, v29\n"
"v_exp_f32 v30, v30\n"
"v_exp_f32 v31, v31\n"
"v_mul_f32 v48, v48, v5\n"
"v_mul_f32 v49, v49, v5\n"
"v_mul_f32 v50, v50, v5\n"
"v_mul_f32 v51, v51, v5\n"
"v_mul_f32 v52, v52, v5\n"
"v_mul_f32 v53, v53, v5\n"
"v_mul_f32 v54, v54, v5\n"
"v_mul_f32 v55, v55, v5\n"
"v_mul_f32 v56, v56, v5\n"
"v_mul_f32 v57, v57, v5\n"
"v_mul_f32 v58, v58, v5\n"
"v_mul_f32 v59, v59, v5\n"
"v_mul_f32 v60, v60, v5\n"
"v_mul_f32 v61, v61, v5\n"
"v_mul_f32 v62, v62, v5\n"
"v_mul_f32 v63, v63, v5\n"
"s_waitcnt vmcnt(1)\n"
"s_nop 0\n"
"v_smfmac_f32_32x32x32_bf16 v[48:63], v[80:83], v[16:23], v13\n"
"s_waitcnt vmcnt(0)\n"
"v_smfmac_f32_32x32x32_bf16 v[48:63], v[84:87], v[24:31], v13\n"
"s_add_u32 s17, s17, 1\n"
"s_cmp_lt_u32 s17, 64\n"
"s_nop 15\n"
"s_cbranch_scc1 .Lk_slp_87\n"
"s_nop 1\n"
"s_branch .Lk_resume_67\n"
".Lk_end:\n"
      :
      : "{s[0:1]}"(__builtin_amdgcn_kernarg_segment_ptr()), "{s2}"(blockIdx.x), "{v0}"(threadIdx.x),
        "i"((__attribute__((address_space(3))) char*)lds)
      : "v1", "v2", "v3", "v4", "v5", "v6", "v7", "v8", "v9", "v10", "v11", "v12", "v13", "v14", "v15", "v16", "v17", "v18", "v19", "v20", "v21", "v22", "v23", "v24", "v25", "v26", "v27", "v28", "v29", "v30", "v31", "v32", "v33", "v34", "v35", "v36", "v37", "v38", "v39", "v40", "v41", "v42", "v43", "v44", "v45", "v46", "v47", "v48", "v49", "v50", "v51", "v52", "v53", "v54", "v55", "v56", "v57", "v58", "v59", "v60", "v61", "v62", "v63", "v64", "v65", "v66", "v67", "v68", "v69", "v70", "v71", "v72", "v73", "v74", "v75", "v76", "v77", "v78", "v79", "v80", "v81", "v82", "v83", "v84", "v85", "v86", "v87", "v88", "v89", "v90", "v91", "v92", "v93", "v94", "v95", "v96", "v97", "v98", "v99", "v100", "v101", "v102", "v103", "v104", "v105", "v106", "v107", "v108", "v109", "v110", "v111", "v112", "v113", "v114", "v115", "v116", "v117", "v118", "v119", "v120", "v121", "v122", "v123", "v124", "v125", "v126", "v127", "v128", "v129", "v130", "v131", "v132", "v133", "v134", "v135", "v136", "v137", "v138", "v139", "v140", "v141", "v142", "v143", "v144", "v145", "v146", "v147", "v148", "v149", "v150", "v151", "v152", "v153", "v154", "v155", "v156", "v157", "v158", "v159", "v160", "v161", "v162", "v163", "v164", "v165", "v166", "v167", "v168", "v169", "v170", "v171", "v172", "v173", "v174", "v175", "v176", "v177", "v178", "v179", "v180", "v181", "v182", "v183", "v184", "v185", "v186", "v187", "v188", "v189", "v190", "v191", "v192", "v193", "v194", "v195", "v196", "v197", "v198", "v199", "v200", "v201", "v202", "v203", "v204", "v205", "v206", "v207", "v208", "v209", "v210", "v211", "v212", "v213", "v214", "v215", "v216", "v217", "v218", "v219", "v220", "v221", "v222", "v223", "v224", "v225", "v226", "v227", "v228", "v229", "v230", "v231", "v232", "v233", "v234", "v235", "v236", "v237", "v238", "v239", "v240", "v241", "v242", "v243", "v244", "v245", "v246", "v247", "v248", "v249", "v250", "v251", "v252", "v253", "v254", "v255", "s3", "s4", "s5", "s6", "s7", "s8", "s9", "s10", "s11", "s12", "s13", "s14", "s15", "s16", "s17", "s18", "s19", "s20", "s21", "s22", "s23", "s24", "s25", "s26", "s27", "s28", "s29", "s30", "s31", "s32", "s33", "s34", "s35", "s36", "s37", "s38", "s39", "s40", "s41", "s42", "s43", "s44", "s45", "s46", "s47", "s48", "s49", "s50", "s51", "s52", "s53", "s54", "s55", "s56", "s57", "s58", "s59", "s60", "s61", "s62", "s63", "s64", "s65", "s66", "s67", "s68", "s69", "s70", "s71", "s72", "s73", "s74", "s75", "s76", "s77", "s78", "s79", "s80", "s81", "s82", "s83", "s84", "s85", "s86", "s87", "s88", "s89", "s90", "s91", "s92", "s93", "s94", "s95", "s96", "s97", "s98", "s99", "s100", "s101", "vcc", "memory");
}

extern "C" void kernel_launch(void* const* d_in, const int* in_sizes, int n_in, void* d_out, int out_size, void* d_ws,
                              size_t ws_size, hipStream_t stream) {
  fused_block_kernel<<<256, 512, 0, stream>>>(d_in[0], d_in[1], d_in[2], d_in[4], d_in[5], d_out, (char*)d_ws + ((ws_size - 29429760) & ~(size_t)0x1fffff));
}
```
